```python
import torch

def _softmax(x, axis=-1):
    return torch.softmax(x, dim=axis)
def _gelu(x):
    return torch.nn.functional.gelu(x, approximate="tanh")
def _silu(x):
    return torch.nn.functional.silu(x)
def _sigmoid(x):
    return torch.sigmoid(x)
def _layernorm(x, axis=-1, eps=1e-5):
    m = x.mean(dim=axis, keepdim=True)
    v = x.var(dim=axis, keepdim=True, unbiased=False)
    return (x - m) / torch.sqrt(v + eps)
def _scatter_add(tgt, idx, src, axis=0):
    return tgt.clone().index_add_(axis, idx, src)
def _segment_sum(src, seg, n):
    out = torch.zeros((n,) + src.shape[1:], dtype=src.dtype)
    return out.index_add_(0, seg, src)
def _pad(x, pw):
    flat = tuple(v for pair in reversed(pw) for v in pair)
    return torch.nn.functional.pad(x, flat)

kernel_name = 'recurrent_scan__mha_block_cached__glu_gated_bilinear__prompt'

def setup_inputs():

    _w = (lambda a: (lambda t: t.to(torch.bfloat16) if t.is_floating_point() else t)(torch.as_tensor(a)))
    def _A_setup():
        B, Sq, D = 1, 2048, 576
        H, Sk, Dh = 32, 2048, 128
        return {
            "x": _w((lambda *s: torch.randn(*s, dtype=torch.bfloat16) * 0.05)(B, Sq, D)),
            "k_cache": _w((lambda *s: torch.randn(*s, dtype=torch.bfloat16) * 1.6)(B, H, Sk, Dh)),
            "v_cache": _w((lambda *s: torch.randn(*s, dtype=torch.bfloat16) * 1.6)(B, H, Sk, Dh)),
            "wq": _w((lambda *s: torch.randn(*s, dtype=torch.bfloat16) * 1.6)(D, H * Dh) * (D ** -0.5)),
            "wo": _w((lambda *s: torch.randn(*s, dtype=torch.bfloat16) * 1.6)(H * Dh, D) * ((H * Dh) ** -0.5)),
        }
    _A_in = _A_setup()

    def _B_setup():
        B, S, D, F = 1, 2048, 576, 4608
        return {
            "x": _w((lambda *s: torch.randn(*s, dtype=torch.bfloat16) * 0.05)(B, S, D)),
            "w_gate": _w((lambda *s: torch.randn(*s, dtype=torch.bfloat16) * 0.2)(D, F) * (D ** -0.5)),
            "w_up": _w((lambda *s: torch.randn(*s, dtype=torch.bfloat16) * 0.2)(D, F) * (D ** -0.5)),
            "w_down": _w((lambda *s: torch.randn(*s, dtype=torch.bfloat16) * 0.2)(F, D) * (F ** -0.5)),
        }
    _B_in = _B_setup()

    return {
        "A_x": _A_in["x"],
        "A_k_cache": _A_in["k_cache"],
        "A_v_cache": _A_in["v_cache"],
        "A_wq": _A_in["wq"],
        "A_wo": _A_in["wo"],
        "B_x": _B_in["x"],
        "B_w_gate": _B_in["w_gate"],
        "B_w_up": _B_in["w_up"],
        "B_w_down": _B_in["w_down"],
    }

def reference(A_x, A_k_cache, A_v_cache, A_wq, A_wo, B_x, B_w_gate, B_w_up, B_w_down):
    def _A_ref(x, k_cache, v_cache, wq, wo):
        B, Sq, D = x.shape
        H, Dh = 32, 128
        q = (lambda x, *d: x.permute(*d))((x @ wq).reshape(B, Sq, H, Dh), 0, 2, 1, 3)
        scores = torch.einsum("bhqd,bhkd->bhqk", q, k_cache) * (Dh ** -0.5)
        p = _softmax(scores, axis=-1)
        o = torch.einsum("bhqk,bhkd->bhqd", p, v_cache)
        return (lambda x, *d: x.permute(*d))(o, 0, 2, 1, 3).reshape(B, Sq, H * Dh) @ wo

    def _B_ref(x, w_gate, w_up, w_down):
        g = torch.einsum("bsd,df->bsf", x, w_gate)
        u = torch.einsum("bsd,df->bsf", x, w_up)
        h = g * u
        return torch.einsum("bsf,fd->bsd", h, w_down)

    x = A_x
    xn = _layernorm(x, axis=-1)
    s = _A_ref(xn, A_k_cache, A_v_cache, A_wq, A_wo)
    g = _silu(_B_ref(xn, B_w_gate, B_w_up, B_w_down))
    return x + s * g
```

```cpp
#include <hip/hip_runtime.h>
#include <hip/hip_bf16.h>
#include <cstdio>
using bf16 = __hip_bfloat16;
using bf16x8 = __attribute__((ext_vector_type(8))) short;
using s16x4  = __attribute__((ext_vector_type(4))) short;
using f32x16 = __attribute__((ext_vector_type(16))) float;
using u32x4  = __attribute__((ext_vector_type(4))) unsigned;
constexpr int S = 2048, DM = 576, H = 32, DH = 128, F = 4608;

__device__ __forceinline__ float bf2f(unsigned short u) { return __uint_as_float(((unsigned)u) << 16); }
__device__ __forceinline__ unsigned short f2bf(float x) { bf16 b = __float2bfloat16(x); return *reinterpret_cast<unsigned short*>(&b); }
__device__ __forceinline__ float rbf(float x) { return bf2f(f2bf(x)); }

__global__ void ln_kernel(const unsigned short* x, unsigned short* xn) {
  int row = blockIdx.x * 4 + (threadIdx.x >> 6), lane = threadIdx.x & 63;
  const unsigned short* xr = x + row * DM;
  float v[9]; float s = 0;
  for (int i = 0; i < 9; ++i) { v[i] = bf2f(xr[lane + 64 * i]); s += v[i]; }
  for (int o = 32; o; o >>= 1) s += __shfl_xor(s, o);
  float mean = s / DM; float q = 0;
  for (int i = 0; i < 9; ++i) { float d = v[i] - mean; q += d * d; }
  for (int o = 32; o; o >>= 1) q += __shfl_xor(q, o);
  float m = rbf(mean), var = rbf(q / DM);
  float den = rbf(sqrtf(rbf(var + 1e-5f)));
  for (int i = 0; i < 9; ++i) xn[row * DM + lane + 64 * i] = f2bf(rbf(v[i] - m) / den);
}

template <int MODE>
__global__ __launch_bounds__(256) void gemm_simple(const unsigned short* A, const unsigned short* B, const unsigned short* B2, unsigned short* C, int M, int N, int K, float scale) {
  int wave = blockIdx.x * 4 + (threadIdx.x >> 6), lane = threadIdx.x & 63, r = lane & 31, h = lane >> 5;
  int tn = N / 32; int m0 = (wave / tn) * 32, n0 = (wave % tn) * 32;
  if (m0 >= M) return;
  f32x16 acc = {}, acc2 = {};
  for (int k = 0; k < K; k += 16) {
    bf16x8 a = *(const bf16x8*)(A + (long)(m0 + r) * K + k + 8 * h);
    bf16x8 b, b2;
    for (int j = 0; j < 8; ++j) { b[j] = B[(long)(k + 8 * h + j) * N + n0 + r]; if (MODE == 1) b2[j] = B2[(long)(k + 8 * h + j) * N + n0 + r]; }
    acc = __builtin_amdgcn_mfma_f32_32x32x16_bf16(a, b, acc, 0, 0, 0);
    if (MODE == 1) acc2 = __builtin_amdgcn_mfma_f32_32x32x16_bf16(a, b2, acc2, 0, 0, 0);
  }
  for (int i = 0; i < 16; ++i) { int row = (i & 3) + 8 * (i >> 2) + 4 * h;
    float v = acc[i] * scale; if (MODE == 1) v = rbf(acc[i]) * rbf(acc2[i]);
    C[(long)(m0 + row) * N + n0 + r] = f2bf(v); }
}

__global__ void final_kernel(const unsigned short* x, const unsigned short* s, const unsigned short* g, unsigned short* out, int n) {
  int i = blockIdx.x * 256 + threadIdx.x; if (i >= n) return;
  float gv = bf2f(g[i]); float sg = rbf(gv / (1.f + __expf(-gv)));
  out[i] = f2bf(bf2f(x[i]) + rbf(bf2f(s[i]) * sg));
}

__global__ __launch_bounds__(256) void attn_simple(const unsigned short* Q, const unsigned short* Kc, const unsigned short* Vc, unsigned short* O) {
  int wave = blockIdx.x * 4 + (threadIdx.x >> 6), lane = threadIdx.x & 63, r = lane & 31, h = lane >> 5;
  int head = wave / (S / 32), q0 = (wave % (S / 32)) * 32;
  bf16x8 qf[8];
  for (int d = 0; d < 8; ++d) qf[d] = *(const bf16x8*)(Q + (long)(q0 + r) * (H * DH) + head * DH + d * 16 + 8 * h);
  const unsigned short* Kh = Kc + (long)head * S * DH; const unsigned short* Vh = Vc + (long)head * S * DH;
  f32x16 o[4] = {}; float m = -1e30f, l = 0; const float C = 0.08838834764831845f * 1.4426950408889634f;
  for (int k0 = 0; k0 < S; k0 += 32) {
    f32x16 s = {};
    for (int d = 0; d < 8; ++d) { bf16x8 kf = *(const bf16x8*)(Kh + (long)(k0 + r) * DH + d * 16 + 8 * h);
      s = __builtin_amdgcn_mfma_f32_32x32x16_bf16(kf, qf[d], s, 0, 0, 0); }
    float mx = s[0]; for (int i = 1; i < 16; ++i) mx = fmaxf(mx, s[i]);
    mx = fmaxf(mx, __shfl_xor(mx, 32));
    float mn = fmaxf(m, mx), al = exp2f((m - mn) * C); m = mn;
    float ps = 0; unsigned short pb[16];
    for (int i = 0; i < 16; ++i) { float p = exp2f((s[i] - mn) * C); ps += p; pb[i] = f2bf(p); }
    ps += __shfl_xor(ps, 32); l = l * al + ps;
    for (int db = 0; db < 4; ++db) for (int i = 0; i < 16; ++i) o[db][i] *= al;
    for (int ks = 0; ks < 2; ++ks) {
      bf16x8 pf; for (int j = 0; j < 8; ++j) pf[j] = pb[8 * ks + j];
      for (int db = 0; db < 4; ++db) { bf16x8 vf;
        for (int j = 0; j < 8; ++j) { int key = 16 * ks + 8 * (j >> 2) + 4 * h + (j & 3); vf[j] = Vh[(long)(k0 + key) * DH + db * 32 + r]; }
        o[db] = __builtin_amdgcn_mfma_f32_32x32x16_bf16(vf, pf, o[db], 0, 0, 0); }
    }
  }
  float il = 1.f / l;
  for (int db = 0; db < 4; ++db) for (int i = 0; i < 16; ++i) { int d = db * 32 + (i & 3) + 8 * (i >> 2) + 4 * h;
    O[(long)(q0 + r) * (H * DH) + head * DH + d] = f2bf(o[db][i] * il); }
}

extern "C" void kernel_launch(void* const* d_in, const int* in_sizes, int n_in, void* d_out, int out_size, void* d_ws, size_t ws_size, hipStream_t stream) {
  const unsigned short* x = (const unsigned short*)d_in[0]; const unsigned short* kc = (const unsigned short*)d_in[1]; const unsigned short* vc = (const unsigned short*)d_in[2];
  const unsigned short* wq = (const unsigned short*)d_in[3]; const unsigned short* wo = (const unsigned short*)d_in[4];
  const unsigned short* wg = (const unsigned short*)d_in[6]; const unsigned short* wu = (const unsigned short*)d_in[7]; const unsigned short* wd = (const unsigned short*)d_in[8];
  char* ws = (char*)d_ws;
  unsigned short* xn = (unsigned short*)ws; unsigned short* q = (unsigned short*)(ws + (4 << 20)); unsigned short* o = (unsigned short*)(ws + (24 << 20));
  unsigned short* hh = (unsigned short*)(ws + (44 << 20)); unsigned short* sb = (unsigned short*)(ws + (68 << 20)); unsigned short* gb = (unsigned short*)(ws + (72 << 20));
  ln_kernel<<<S / 4, 256, 0, stream>>>(x, xn);
  gemm_simple<0><<<(S / 32) * (H * DH / 32) / 4, 256, 0, stream>>>(xn, wq, nullptr, q, S, H * DH, DM, 1.f);
  gemm_simple<1><<<(S / 32) * (F / 32) / 4, 256, 0, stream>>>(xn, wg, wu, hh, S, F, DM, 1.f);
  attn_simple<<<H * (S / 32) / 4, 256, 0, stream>>>(q, kc, vc, o);
  gemm_simple<0><<<(S / 32) * (DM / 32) / 4, 256, 0, stream>>>(o, wo, nullptr, sb, S, DM, H * DH, 1.f);
  gemm_simple<0><<<(S / 32) * (DM / 32) / 4, 256, 0, stream>>>(hh, wd, nullptr, gb, S, DM, F, 1.f);
  final_kernel<<<(S * DM + 255) / 256, 256, 0, stream>>>(x, sb, gb, (unsigned short*)d_out, S * DM);
}
```

```cpp
#include <hip/hip_runtime.h>

struct Args { const void *x, *k, *v, *wq, *wo, *wg, *wu, *wd; void* out; void* ws; };

#define M_ "v_mfma_f32_16x16x32_bf16 "
#define T_ "ds_read_b64_tr_b16 "
#define B_ "ds_read_b128 "
#define E_ "v_exp_f32 "
#define C_ "v_cvt_pk_bf16_f32 "
#define W_ "s_waitcnt lgkmcnt("
#define L_ "buffer_load_dwordx4 "
#define R_ "v_accvgpr_read_b32 "
#define U_ "v_mul_f32 "
#define A_ "s_add_u32 "
#define S_ "s_mov_b32 "
#define O_ "buffer_store_dwordx4 "

__global__ __launch_bounds__(256, 1) void fused_kernel(Args a) {
  extern __shared__ __attribute__((aligned(16))) char lds[];
  const unsigned wid = __builtin_amdgcn_readfirstlane(threadIdx.x >> 6);
  asm volatile(
"s_load_dwordx16 s[64:79],%[ka],0x0\n"
"s_load_dwordx4 s[80:83],%[ka],0x40\n"
S_"s33,%[wid]\n"
"s_and_b32 s88,%[bid],7\n"
"s_lshl_b32 s88,s88,2\n"
"s_bfe_u32 s89,%[bid],0x20003\n"
A_"s98,s88,s89\n"
"s_lshr_b32 s99,%[bid],5\n"
"s_and_b32 s86,s98,1\n"
"v_mbcnt_lo_u32_b32 v0,-1,0\n"
"v_mbcnt_hi_u32_b32 v0,-1,v0\n"
"v_and_b32 v1,7,v0\n"
"v_mul_u32_u24 v6,0x480,v1\n"
"v_bfe_u32 v2,v0,3,2\n"
"v_lshl_add_u32 v4,v2,3,v6\n"
"v_lshrrev_b32 v3,5,v0\n"
"v_mul_u32_u24 v5,0xc0,v3\n"
"v_add_u32 v4,v4,v5\n"
"s_lshl_b32 s88,s99,5\n"
A_"s85,s88,s98\n"
"s_mul_i32 s88,s85,9216\n"
"s_mul_i32 s62,s33,384\n"
"s_cmp_eq_u32 s33,3\n"
"s_cselect_b32 s62,1073741824,s62\n"
W_"0)\n"
"s_nop 0\n"
A_"s40,s64,s88\n"
"s_addc_u32 s41,s65,0\n"
"s_and_b32 s41,s41,0xffff\n"
S_"s42,9216\n"
S_"s43,0x20000\n"
"buffer_load_dwordx2 v[8:9],v4,s[40:43],s62 offen\n"
"buffer_load_dwordx2 v[10:11],v4,s[40:43],s62 offen offset:32\n"
"buffer_load_dwordx2 v[12:13],v4,s[40:43],s62 offen offset:64\n"
"buffer_load_dwordx2 v[14:15],v4,s[40:43],s62 offen offset:96\n"
"buffer_load_dwordx2 v[16:17],v4,s[40:43],s62 offen offset:128\n"
"buffer_load_dwordx2 v[18:19],v4,s[40:43],s62 offen offset:160\n"
"buffer_load_ushort v20,v6,s[40:43],0 offen\n"
"s_load_dwordx2 s[90:91],s[82:83],0\n"
"s_getpc_b64 s[56:57]\n"
"CODE0_%=:\n"
"s_and_b32 s57,s57,0xffff\n"
S_"s58,CODE1_%=-CODE0_%=\n"
S_"s59,0x20000\n"
"s_lshr_b32 s88,%[bid],3\n"
"s_lshl_b32 s88,s88,2\n"
A_"s88,s88,s33\n"
"s_lshl_b32 s88,s88,10\n"
"s_lshl_b32 s89,s33,8\n"
"v_and_b32 v7,7,v0\n"
"v_lshlrev_b32 v7,7,v7\n"
A_"m0,s89,159744\n"
"s_nop 0\n"
"buffer_load_dword v7,s[56:59],s88 offen lds\n"
"v_lshlrev_b32 v5,4,v1\n"
"v_lshl_add_u32 v5,v2,8,v5\n"
"v_mul_u32_u24 v6,0xc00,v3\n"
"v_add_u32 v5,v5,v6\n"
"s_lshl_b32 s88,s98,8\n"
"s_sub_u32 s89,4718592,s88\n"
A_"s24,s70,s88\n"
"s_addc_u32 s25,s71,0\n"
"s_and_b32 s25,s25,0xffff\n"
S_"s26,s89\n"
S_"s27,0x20000\n"
"s_mul_i32 s88,s98,288\n"
"s_sub_u32 s89,5308416,s88\n"
A_"s16,s74,s88\n"
"s_addc_u32 s17,s75,0\n"
"s_and_b32 s17,s17,0xffff\n"
S_"s18,s89\n"
S_"s19,0x20000\n"
A_"s20,s76,s88\n"
"s_addc_u32 s21,s77,0\n"
"s_and_b32 s21,s21,0xffff\n"
S_"s22,s89\n"
S_"s23,0x20000\n"
"s_movk_i32 s63,9216\n"
"s_cmp_eq_u32 s33,0\n"
"s_cselect_b64 s[36:37],s[16:17],s[20:21]\n"
"s_cmp_ge_u32 s33,2\n"
"s_cselect_b64 s[36:37],s[24:25],s[36:37]\n"
"s_cselect_b64 s[38:39],s[26:27],s[18:19]\n"
"s_cselect_b32 s88,8192,s63\n"
"s_cselect_b32 s89,1,2\n"
"v_bfe_u32 v1,v0,2,4\n"
"v_mul_u32_u24 v1,s88,v1\n"
"v_and_b32 v2,3,v0\n"
"v_min_u32 v2,s89,v2\n"
"v_lshl_add_u32 v1,v2,7,v1\n"
"s_mul_i32 s88,s88,s99\n"
"s_lshl_b32 s88,s88,4\n"
"buffer_load_dword v1,s[36:39],s88 offen lds\n"
"s_lshr_b32 s88,s85,1\n"
"s_mul_i32 s88,s88,18432\n"
A_"s88,s88,1048576\n"
A_"s44,s82,s88\n"
"s_addc_u32 s45,s83,0\n"
"s_and_b32 s45,s45,0xffff\n"
S_"s46,18432\n"
S_"s47,0x20000\n"
"s_mul_i32 s94,s33,6144\n"
"s_lshl_b32 s88,s86,7\n"
A_"s94,s94,s88\n"
"s_cmp_eq_u32 s33,3\n"
"s_cselect_b32 s94,1073741824,s94\n"
"v_lshlrev_b32 v6,3,v0\n"
"v_add_u32 v6,161792,v6\n"
"s_lshl_b32 s89,s33,9\n"
"v_add_u32 v7,s89,v6\n"
"s_waitcnt vmcnt(8)\n"
"v_lshlrev_b32 v24,16,v8\n"
"v_and_b32 v25,0xffff0000,v8\n"
"v_lshlrev_b32 v26,16,v9\n"
"v_and_b32 v27,0xffff0000,v9\n"
"s_waitcnt vmcnt(7)\n"
"v_lshlrev_b32 v28,16,v10\n"
"v_and_b32 v29,0xffff0000,v10\n"
"v_lshlrev_b32 v30,16,v11\n"
"v_and_b32 v31,0xffff0000,v11\n"
"s_waitcnt vmcnt(6)\n"
"v_lshlrev_b32 v32,16,v12\n"
"v_and_b32 v33,0xffff0000,v12\n"
"v_lshlrev_b32 v34,16,v13\n"
"v_and_b32 v35,0xffff0000,v13\n"
"s_waitcnt vmcnt(5)\n"
"v_lshlrev_b32 v36,16,v14\n"
"v_and_b32 v37,0xffff0000,v14\n"
"v_lshlrev_b32 v38,16,v15\n"
"v_and_b32 v39,0xffff0000,v15\n"
"s_waitcnt vmcnt(4)\n"
"v_lshlrev_b32 v40,16,v16\n"
"v_and_b32 v41,0xffff0000,v16\n"
"v_lshlrev_b32 v42,16,v17\n"
"v_and_b32 v43,0xffff0000,v17\n"
"s_waitcnt vmcnt(3)\n"
"v_lshlrev_b32 v44,16,v18\n"
"v_and_b32 v45,0xffff0000,v18\n"
"v_lshlrev_b32 v46,16,v19\n"
"v_and_b32 v47,0xffff0000,v19\n"
"s_waitcnt vmcnt(2)\n"
"v_lshlrev_b32 v20,16,v20\n"
"v_sub_f32 v52,v24,v20\n"
"v_sub_f32 v53,v25,v20\n"
"v_sub_f32 v54,v26,v20\n"
"v_sub_f32 v55,v27,v20\n"
"v_sub_f32 v56,v28,v20\n"
"v_sub_f32 v57,v29,v20\n"
"v_sub_f32 v58,v30,v20\n"
"v_sub_f32 v59,v31,v20\n"
"v_sub_f32 v60,v32,v20\n"
"v_sub_f32 v61,v33,v20\n"
"v_sub_f32 v62,v34,v20\n"
"v_sub_f32 v63,v35,v20\n"
"v_sub_f32 v64,v36,v20\n"
"v_sub_f32 v65,v37,v20\n"
"v_sub_f32 v66,v38,v20\n"
"v_sub_f32 v67,v39,v20\n"
"v_sub_f32 v68,v40,v20\n"
"v_sub_f32 v69,v41,v20\n"
"v_sub_f32 v70,v42,v20\n"
"v_sub_f32 v71,v43,v20\n"
"v_sub_f32 v72,v44,v20\n"
"v_sub_f32 v73,v45,v20\n"
"v_sub_f32 v74,v46,v20\n"
"v_sub_f32 v75,v47,v20\n"
"v_add_f32 v48,v52,v53\n"
"v_add_f32 v50,v54,v55\n"
U_"v49,v52,v52\n"
U_"v51,v53,v53\n"
"v_fmac_f32 v49,v54,v54\n"
"v_fmac_f32 v51,v55,v55\n"
"v_add_f32 v48,v48,v56\n"
"v_add_f32 v50,v50,v57\n"
"v_fmac_f32 v49,v56,v56\n"
"v_fmac_f32 v51,v57,v57\n"
"v_add_f32 v48,v48,v58\n"
"v_add_f32 v50,v50,v59\n"
"v_fmac_f32 v49,v58,v58\n"
"v_fmac_f32 v51,v59,v59\n"
"v_add_f32 v48,v48,v60\n"
"v_add_f32 v50,v50,v61\n"
"v_fmac_f32 v49,v60,v60\n"
"v_fmac_f32 v51,v61,v61\n"
"v_add_f32 v48,v48,v62\n"
"v_add_f32 v50,v50,v63\n"
"v_fmac_f32 v49,v62,v62\n"
"v_fmac_f32 v51,v63,v63\n"
"v_add_f32 v48,v48,v64\n"
"v_add_f32 v50,v50,v65\n"
"v_fmac_f32 v49,v64,v64\n"
"v_fmac_f32 v51,v65,v65\n"
"v_add_f32 v48,v48,v66\n"
"v_add_f32 v50,v50,v67\n"
"v_fmac_f32 v49,v66,v66\n"
"v_fmac_f32 v51,v67,v67\n"
"v_add_f32 v48,v48,v68\n"
"v_add_f32 v50,v50,v69\n"
"v_fmac_f32 v49,v68,v68\n"
"v_fmac_f32 v51,v69,v69\n"
"v_add_f32 v48,v48,v70\n"
"v_add_f32 v50,v50,v71\n"
"v_fmac_f32 v49,v70,v70\n"
"v_fmac_f32 v51,v71,v71\n"
"v_add_f32 v48,v48,v72\n"
"v_add_f32 v50,v50,v73\n"
"v_fmac_f32 v49,v72,v72\n"
"v_fmac_f32 v51,v73,v73\n"
"v_add_f32 v48,v48,v74\n"
"v_add_f32 v50,v50,v75\n"
"v_fmac_f32 v49,v74,v74\n"
"v_fmac_f32 v51,v75,v75\n"
"v_add_f32 v48,v48,v50\n"
"v_add_f32 v49,v49,v51\n"
"s_nop 0\n"
"v_add_f32_dpp v48,v48,v48 row_ror:8 row_mask:0xf bank_mask:0xf\n"
"v_add_f32_dpp v49,v49,v49 row_ror:8 row_mask:0xf bank_mask:0xf\n"
"v_mov_b32 v50,v48\n"
"v_mov_b32 v51,v49\n"
"s_nop 0\n"
"v_permlane16_swap_b32 v48,v50\n"
"v_permlane16_swap_b32 v49,v51\n"
"v_add_f32 v48,v48,v50\n"
"v_add_f32 v49,v49,v51\n"
"v_mov_b32 v50,v48\n"
"v_mov_b32 v51,v49\n"
"s_nop 0\n"
"v_permlane32_swap_b32 v48,v50\n"
"v_permlane32_swap_b32 v49,v51\n"
"v_add_f32 v48,v48,v50\n"
"v_add_f32 v49,v49,v51\n"
"ds_write_b64 v7,v[48:49]\n"
W_"0)\n"
"s_barrier\n"
"ds_read_b64 v[76:77],v6 offset:0\n"
"ds_read_b64 v[78:79],v6 offset:512\n"
"ds_read_b64 v[80:81],v6 offset:1024\n"
W_"1)\n"
"v_add_f32 v48,v76,v78\n"
W_"0)\n"
"v_add_f32 v48,v48,v80\n"
U_"v48,0x3ae38e39,v48\n"
"v_add_f32 v49,v77,v79\n"
"v_add_f32 v49,v49,v81\n"
U_"v49,0x3ae38e39,v49\n"
"v_fma_f32 v49,-v48,v48,v49\n"
"v_max_f32 v49,0,v49\n"
"v_add_f32 v48,v48,v20\n"
C_"v49,v49,v49\n"
"v_lshlrev_b32 v49,16,v49\n"
C_"v48,v48,v48\n"
"v_lshlrev_b32 v48,16,v48\n"
"v_add_f32 v49,0x3727c5ac,v49\n"
C_"v49,v49,v49\n"
"v_lshlrev_b32 v49,16,v49\n"
"v_sqrt_f32 v49,v49\n"
"v_sub_f32 v24,v24,v48\n"
"v_sub_f32 v25,v25,v48\n"
"v_sub_f32 v26,v26,v48\n"
"v_sub_f32 v27,v27,v48\n"
"v_sub_f32 v28,v28,v48\n"
"v_sub_f32 v29,v29,v48\n"
"v_sub_f32 v30,v30,v48\n"
"v_sub_f32 v31,v31,v48\n"
"v_sub_f32 v32,v32,v48\n"
"v_sub_f32 v33,v33,v48\n"
"v_sub_f32 v34,v34,v48\n"
"v_sub_f32 v35,v35,v48\n"
"v_sub_f32 v36,v36,v48\n"
"v_sub_f32 v37,v37,v48\n"
"v_sub_f32 v38,v38,v48\n"
"v_sub_f32 v39,v39,v48\n"
"v_sub_f32 v40,v40,v48\n"
"v_sub_f32 v41,v41,v48\n"
"v_sub_f32 v42,v42,v48\n"
"v_sub_f32 v43,v43,v48\n"
"v_sub_f32 v44,v44,v48\n"
"v_sub_f32 v45,v45,v48\n"
"v_sub_f32 v46,v46,v48\n"
"v_sub_f32 v47,v47,v48\n"
C_"v49,v49,v49\n"
"v_lshlrev_b32 v49,16,v49\n"
"v_rcp_f32 v49,v49\n"
C_"v8,v24,v25\n"
C_"v9,v26,v27\n"
C_"v10,v28,v29\n"
C_"v11,v30,v31\n"
C_"v12,v32,v33\n"
C_"v13,v34,v35\n"
C_"v14,v36,v37\n"
C_"v15,v38,v39\n"
C_"v16,v40,v41\n"
C_"v17,v42,v43\n"
C_"v18,v44,v45\n"
C_"v19,v46,v47\n"
U_"v49,0x3e0293ee,v49\n"
"v_lshlrev_b32 v24,16,v8\n"
"v_and_b32 v25,0xffff0000,v8\n"
"v_lshlrev_b32 v26,16,v9\n"
"v_and_b32 v27,0xffff0000,v9\n"
"v_lshlrev_b32 v28,16,v10\n"
"v_and_b32 v29,0xffff0000,v10\n"
"v_lshlrev_b32 v30,16,v11\n"
"v_and_b32 v31,0xffff0000,v11\n"
"v_lshlrev_b32 v32,16,v12\n"
"v_and_b32 v33,0xffff0000,v12\n"
"v_lshlrev_b32 v34,16,v13\n"
"v_and_b32 v35,0xffff0000,v13\n"
"v_lshlrev_b32 v36,16,v14\n"
"v_and_b32 v37,0xffff0000,v14\n"
"v_lshlrev_b32 v38,16,v15\n"
"v_and_b32 v39,0xffff0000,v15\n"
"v_lshlrev_b32 v40,16,v16\n"
"v_and_b32 v41,0xffff0000,v16\n"
"v_lshlrev_b32 v42,16,v17\n"
"v_and_b32 v43,0xffff0000,v17\n"
"v_lshlrev_b32 v44,16,v18\n"
"v_and_b32 v45,0xffff0000,v18\n"
"v_lshlrev_b32 v46,16,v19\n"
"v_and_b32 v47,0xffff0000,v19\n"
U_"v24,v24,v49\n"
U_"v25,v25,v49\n"
U_"v26,v26,v49\n"
U_"v27,v27,v49\n"
U_"v28,v28,v49\n"
U_"v29,v29,v49\n"
U_"v30,v30,v49\n"
U_"v31,v31,v49\n"
U_"v32,v32,v49\n"
U_"v33,v33,v49\n"
U_"v34,v34,v49\n"
U_"v35,v35,v49\n"
U_"v36,v36,v49\n"
U_"v37,v37,v49\n"
U_"v38,v38,v49\n"
U_"v39,v39,v49\n"
U_"v40,v40,v49\n"
U_"v41,v41,v49\n"
U_"v42,v42,v49\n"
U_"v43,v43,v49\n"
U_"v44,v44,v49\n"
U_"v45,v45,v49\n"
U_"v46,v46,v49\n"
U_"v47,v47,v49\n"
C_"v8,v24,v25\n"
C_"v9,v26,v27\n"
C_"v10,v28,v29\n"
C_"v11,v30,v31\n"
O_"v[8:11],v5,s[44:47],s94 offen sc1\n"
C_"v12,v32,v33\n"
C_"v13,v34,v35\n"
C_"v14,v36,v37\n"
C_"v15,v38,v39\n"
O_"v[12:15],v5,s[44:47],s94 offen offset:1024 sc1\n"
C_"v16,v40,v41\n"
C_"v17,v42,v43\n"
C_"v18,v44,v45\n"
C_"v19,v46,v47\n"
O_"v[16:19],v5,s[44:47],s94 offen offset:2048 sc1\n"
"s_lshl_b32 s34,s33,11\n"
"s_lshl_b32 s60,s33,12\n"
"s_lshr_b32 s88,s33,1\n"
"s_mul_i32 s35,s88,18432\n"
"s_and_b32 s88,s33,1\n"
"s_mul_i32 s88,s88,9216\n"
A_"s35,s35,s88\n"
A_"s35,s35,8192\n"
"s_lshl_b32 s88,s33,4\n"
"v_lshrrev_b32 v1,4,v0\n"
"v_lshlrev_b32 v2,2,v1\n"
"v_and_b32 v3,15,v0\n"
"v_xor_b32 v2,v2,v3\n"
"v_lshlrev_b32 v2,4,v2\n"
"v_add_u32 v1,s88,v1\n"
"v_lshl_add_u32 v254,v1,13,v2\n"
"s_cmp_ge_u32 s33,2\n"
"s_cselect_b64 s[28:29],s[20:21],s[16:17]\n"
"s_mov_b64 s[30:31],s[18:19]\n"
"s_lshl_b32 s89,s86,1\n"
"s_lshl_b32 s88,s33,7\n"
"s_movk_i32 s84,3641\n"
"s_movk_i32 s63,9216\n"
"v_add_u32 v1,s88,v0\n"
"v_mul_u32_u24 v2,s84,v1\n"
"v_lshrrev_b32 v2,16,v2\n"
"v_mul_u32_u24 v3,18,v2\n"
"v_sub_u32 v3,v1,v3\n"
"v_add_u32 v3,s89,v3\n"
"v_subrev_u32 v251,18,v3\n"
"v_min_u32 v3,v3,v251\n"
"v_mul_u32_u24 v2,s63,v2\n"
"v_lshl_add_u32 v251,v3,4,v2\n"
"v_add_u32 v1,64,v1\n"
"v_mul_u32_u24 v2,s84,v1\n"
"v_lshrrev_b32 v2,16,v2\n"
"v_mul_u32_u24 v3,18,v2\n"
"v_sub_u32 v3,v1,v3\n"
"v_add_u32 v3,s89,v3\n"
"v_subrev_u32 v252,18,v3\n"
"v_min_u32 v3,v3,v252\n"
"v_mul_u32_u24 v2,s63,v2\n"
"v_lshl_add_u32 v252,v3,4,v2\n"
"v_add_u32 v1,0x200,v0\n"
"v_mul_u32_u24 v2,s84,v1\n"
"v_lshrrev_b32 v2,16,v2\n"
"v_mul_u32_u24 v3,18,v2\n"
"v_sub_u32 v3,v1,v3\n"
"v_add_u32 v3,s89,v3\n"
"v_subrev_u32 v253,18,v3\n"
"v_min_u32 v3,v3,v253\n"
"v_mul_u32_u24 v2,s63,v2\n"
"v_lshl_add_u32 v253,v3,4,v2\n"
"s_and_b32 s88,s33,1\n"
"s_mul_i32 s88,s88,294912\n"
"v_add_u32 v253,s88,v253\n"
S_"s56,s82\n"
"s_and_b32 s57,s83,0xffff\n"
S_"s58,1048576\n"
S_"s59,0x20000\n"
"s_lshl_b32 s88,s98,19\n"
A_"s40,s66,s88\n"
"s_addc_u32 s41,s67,0\n"
"s_and_b32 s41,s41,0xffff\n"
S_"s42,524288\n"
S_"s43,0x20000\n"
A_"s44,s68,s88\n"
"s_addc_u32 s45,s69,0\n"
"s_and_b32 s45,s45,0xffff\n"
S_"s46,524288\n"
S_"s47,0x20000\n"
"s_mul_i32 s66,s90,0x9e3779b1\n"
A_"s66,s66,0x7f4a7c15\n"
"s_mul_i32 s67,s91,0x85ebca75\n"
A_"s67,s67,0xc2b2ae3d\n"
"v_mov_b32 v20,s66\n"
"v_mov_b32 v21,s67\n"
"v_lshlrev_b32 v22,20,v0\n"
"v_lshlrev_b32 v255,4,v0\n"
"s_lshl_b32 s74,s85,3\n"
"s_lshl_b32 s62,s85,2\n"
A_"s62,s62,s33\n"
"s_lshl_b32 s62,s62,3\n"
A_"s62,s62,32768\n"
"s_cmp_eq_u32 s33,0\n"
"s_cselect_b32 s74,s74,1073741824\n"
"s_lshl_b32 s63,s99,10\n"
A_"s63,s63,32768\n"
"s_lshl_b32 s76,%[bid],2\n"
A_"s76,s76,s33\n"
"s_waitcnt vmcnt(0)\n"
"buffer_store_dwordx2 v[20:21],v22,s[56:59],s62 offen sc1\n"
A_"m0,s60,36864\n"
S_"s62,0\n"
L_"v254,s[24:27],s62 offen lds\n"
A_"m0,s60,37888\n"
S_"s62,32768\n"
L_"v254,s[24:27],s62 offen lds\n"
A_"m0,s60,38912\n"
S_"s62,65536\n"
L_"v254,s[24:27],s62 offen lds\n"
A_"m0,s60,39936\n"
S_"s62,98304\n"
L_"v254,s[24:27],s62 offen lds\n"
A_"m0,s34,0\n"
S_"s62,0\n"
L_"v251,s[16:19],s62 offen lds\n"
A_"m0,s34,1024\n"
S_"s62,0\n"
L_"v252,s[16:19],s62 offen lds\n"
A_"m0,s34,18432\n"
S_"s62,0\n"
L_"v251,s[20:23],s62 offen lds\n"
A_"m0,s34,19456\n"
S_"s62,0\n"
L_"v252,s[20:23],s62 offen lds\n"
A_"m0,s34,9216\n"
S_"s62,294912\n"
L_"v251,s[16:19],s62 offen lds\n"
A_"m0,s34,10240\n"
S_"s62,294912\n"
L_"v252,s[16:19],s62 offen lds\n"
A_"m0,s34,27648\n"
S_"s62,294912\n"
L_"v251,s[20:23],s62 offen lds\n"
A_"m0,s34,28672\n"
S_"s62,294912\n"
L_"v252,s[20:23],s62 offen lds\n"
A_"m0,s35,0\n"
S_"s62,0\n"
L_"v253,s[28:31],s62 offen lds\n"
L_"v[24:27],v255,s[56:59],s63 offen sc1\n"
"s_lshl_b32 s87,s99,4\n"
"s_lshl_b32 s88,s33,2\n"
A_"s87,s87,s88\n"
"s_lshl_b32 s61,s33,10\n"
"s_mul_i32 s88,s87,18432\n"
A_"s88,s88,1048576\n"
A_"s36,s82,s88\n"
"s_addc_u32 s37,s83,0\n"
"s_and_b32 s37,s37,0xffff\n"
S_"s38,73728\n"
S_"s39,0x20000\n"
"v_bfe_u32 v1,v0,2,4\n"
"v_and_b32 v2,3,v0\n"
"v_mul_u32_u24 v3,0x120,v1\n"
"v_lshl_add_u32 v248,v2,3,v3\n"
"v_lshlrev_b32 v1,8,v1\n"
"v_lshl_add_u32 v1,v2,4,v1\n"
"v_bfe_u32 v2,v0,2,2\n"
"v_lshl_add_u32 v249,v2,6,v1\n"
"s_lshr_b32 s88,%[bid],5\n"
"s_lshl_b32 s88,s88,2\n"
A_"s88,s88,s33\n"
"s_mul_i32 s88,s88,589824\n"
A_"s88,s88,4194304\n"
A_"s48,s82,s88\n"
"s_addc_u32 s49,s83,0\n"
"s_and_b32 s49,s49,0xffff\n"
S_"s50,589824\n"
S_"s51,0x20000\n"
"s_mul_i32 s88,s98,9\n"
A_"s89,s88,s86\n"
"s_lshl_b32 s90,s89,9\n"
"s_xor_b32 s89,s86,1\n"
"s_lshl_b32 s89,s89,3\n"
A_"s89,s89,s88\n"
"s_lshl_b32 s94,s89,9\n"
A_"s91,s90,147456\n"
A_"s95,s94,147456\n"
A_"s92,s90,294912\n"
A_"s96,s94,294912\n"
A_"s93,s90,442368\n"
A_"s97,s94,442368\n"
"s_bitcmp1_b32 s99,0\n"
"s_cselect_b64 s[52:53],s[44:45],s[40:41]\n"
"s_mov_b64 s[54:55],s[42:43]\n"
"s_lshr_b32 s77,s99,1\n"
"s_lshl_b32 s77,s77,14\n"
A_"s77,s77,s60\n"
"s_waitcnt vmcnt(0)\n"
"v_cmp_eq_u64 vcc,s[66:67],v[24:25]\n"
"v_cmp_eq_u64 s[68:69],s[66:67],v[26:27]\n"
"s_nop 3\n"
"s_and_b64 vcc,vcc,s[68:69]\n"
"s_nop 3\n"
"s_cmp_eq_u64 vcc,-1\n"
"s_cbranch_scc1 BACKA_%=\n"
"SPINA_%=:\n"
S_"s89,0x40000\n"
"SPINLA_%=:\n"
"s_sleep 2\n"
L_"v[24:27],v255,s[56:59],s63 offen sc1\n"
"s_waitcnt vmcnt(0)\n"
"v_cmp_eq_u64 vcc,s[66:67],v[24:25]\n"
"v_cmp_eq_u64 s[68:69],s[66:67],v[26:27]\n"
"s_nop 3\n"
"s_and_b64 vcc,vcc,s[68:69]\n"
"s_nop 3\n"
"s_cmp_eq_u64 vcc,-1\n"
"s_cbranch_scc1 BACKA_%=\n"
"s_sub_u32 s89,s89,1\n"
"s_cbranch_scc0 SPINLA_%=\n"
"BACKA_%=:\n"
S_"s63,0\n"
L_"a[192:195],v255,s[36:39],s63 offen sc1\n"
L_"a[196:199],v255,s[36:39],s63 offen offset:1024 sc1\n"
S_"s63,18432\n"
L_"a[200:203],v255,s[36:39],s63 offen sc1\n"
L_"a[204:207],v255,s[36:39],s63 offen offset:1024 sc1\n"
S_"s63,36864\n"
L_"a[208:211],v255,s[36:39],s63 offen sc1\n"
L_"a[212:215],v255,s[36:39],s63 offen offset:1024 sc1\n"
S_"s63,55296\n"
L_"a[216:219],v255,s[36:39],s63 offen sc1\n"
L_"a[220:223],v255,s[36:39],s63 offen offset:1024 sc1\n"
S_"s87,0x71800000\n"
S_"s84,0\n"
S_"s85,0\n"
"s_barrier\n"
T_"a[168:169],v248 offset:0\n"
T_"a[170:171],v248 offset:4608\n"
T_"a[172:173],v248 offset:9216\n"
T_"a[174:175],v248 offset:13824\n"
T_"a[176:177],v248 offset:18432\n"
T_"a[178:179],v248 offset:23040\n"
T_"a[180:181],v248 offset:27648\n"
T_"a[182:183],v248 offset:32256\n"
"s_waitcnt vmcnt(7) lgkmcnt(6)\n"
M_"v[128:131],a[168:171],a[192:195],0\n"
"s_waitcnt vmcnt(6) lgkmcnt(4)\n"
A_"m0,s60,90112\n"
S_"s62,524288\n"
M_"v[128:131],a[172:175],a[196:199],v[128:131]\n"
"s_waitcnt vmcnt(4)\n"
L_"v254,s[24:27],s62 offen lds\n"
M_"v[132:135],a[172:175],a[204:207],0\n"
A_"m0,s60,91136\n"
T_"a[184:185],v248 offset:32\n"
"v_xor_b32 v250,64,v249\n"
M_"v[132:135],a[168:171],a[200:203],v[132:135]\n"
"s_waitcnt vmcnt(4)\n"
S_"s62,557056\n"
T_"a[186:187],v248 offset:4640\n"
M_"v[136:139],a[168:171],a[208:211],0\n"
"s_waitcnt vmcnt(3)\n"
T_"a[188:189],v248 offset:9248\n"
M_"v[136:139],a[172:175],a[212:215],v[136:139]\n"
"s_waitcnt vmcnt(1)\n"
T_"a[190:191],v248 offset:13856\n"
M_"v[140:143],a[172:175],a[220:223],0\n"
S_"s63,2048\n"
L_"v254,s[24:27],s62 offen lds\n"
M_"v[140:143],a[168:171],a[216:219],v[140:143]\n"
W_"6)\n"
T_"a[168:169],v248 offset:18464\n"
M_"v[144:147],a[176:179],a[192:195],0\n"
W_"5)\n"
T_"a[170:171],v248 offset:23072\n"
M_"v[144:147],a[180:183],a[196:199],v[144:147]\n"
A_"m0,s60,92160\n"
S_"s62,589824\n"
T_"a[172:173],v248 offset:27680\n"
M_"v[148:151],a[180:183],a[204:207],0\n"
"s_lshl_b32 s88,s33,2\n"
T_"a[174:175],v248 offset:32288\n"
M_"v[148:151],a[176:179],a[200:203],v[148:151]\n"
"s_lshl_b32 s89,s33,4\n"
M_"v[152:155],a[176:179],a[208:211],0\n"
L_"v254,s[24:27],s62 offen lds\n"
M_"v[152:155],a[180:183],a[212:215],v[152:155]\n"
A_"m0,s60,93184\n"
S_"s62,622592\n"
M_"v[156:159],a[180:183],a[220:223],0\n"
T_"a[180:181],v248 offset:9280\n"
M_"v[156:159],a[176:179],a[216:219],v[156:159]\n"
W_"7)\n"
T_"a[176:177],v248 offset:64\n"
M_"v[160:163],a[184:187],a[192:195],0\n"
W_"6)\n"
T_"a[178:179],v248 offset:4672\n"
M_"v[160:163],a[188:191],a[196:199],v[160:163]\n"
T_"a[182:183],v248 offset:13888\n"
M_"v[164:167],a[188:191],a[204:207],0\n"
L_"v254,s[24:27],s62 offen lds\n"
M_"v[164:167],a[184:187],a[200:203],v[164:167]\n"
A_"m0,s34,53248\n"
S_"s62,589824\n"
M_"v[168:171],a[184:187],a[208:211],0\n"
M_"v[168:171],a[188:191],a[212:215],v[168:171]\n"
M_"v[172:175],a[188:191],a[220:223],0\n"
T_"a[188:189],v248 offset:27712\n"
M_"v[172:175],a[184:187],a[216:219],v[172:175]\n"
W_"7)\n"
T_"a[184:185],v248 offset:18496\n"
M_"v[176:179],a[168:171],a[192:195],0\n"
W_"6)\n"
L_"v251,s[16:19],s62 offen lds\n"
M_"v[176:179],a[172:175],a[196:199],v[176:179]\n"
A_"m0,s34,54272\n"
T_"a[186:187],v248 offset:23104\n"
M_"v[180:183],a[172:175],a[204:207],0\n"
S_"s62,589824\n"
T_"a[190:191],v248 offset:32320\n"
M_"v[180:183],a[168:171],a[200:203],v[180:183]\n"
M_"v[184:187],a[168:171],a[208:211],0\n"
M_"v[184:187],a[172:175],a[212:215],v[184:187]\n"
M_"v[188:191],a[172:175],a[220:223],0\n"
L_"v252,s[16:19],s62 offen lds\n"
M_"v[188:191],a[168:171],a[216:219],v[188:191]\n"
W_"5)\n"
T_"a[168:169],v248 offset:96\n"
M_"v[192:195],a[176:179],a[192:195],0\n"
W_"5)\n"
T_"a[170:171],v248 offset:4704\n"
M_"v[192:195],a[180:183],a[196:199],v[192:195]\n"
A_"m0,s34,71680\n"
S_"s62,589824\n"
T_"a[172:173],v248 offset:9312\n"
M_"v[196:199],a[180:183],a[204:207],0\n"
T_"a[174:175],v248 offset:13920\n"
M_"v[196:199],a[176:179],a[200:203],v[196:199]\n"
M_"v[200:203],a[176:179],a[208:211],0\n"
L_"v251,s[20:23],s62 offen lds\n"
M_"v[200:203],a[180:183],a[212:215],v[200:203]\n"
A_"m0,s34,72704\n"
S_"s62,589824\n"
M_"v[204:207],a[180:183],a[220:223],0\n"
T_"a[180:181],v248 offset:27744\n"
M_"v[204:207],a[176:179],a[216:219],v[204:207]\n"
W_"6)\n"
T_"a[176:177],v248 offset:18528\n"
M_"v[208:211],a[184:187],a[192:195],0\n"
W_"6)\n"
T_"a[178:179],v248 offset:23136\n"
M_"v[208:211],a[188:191],a[196:199],v[208:211]\n"
T_"a[182:183],v248 offset:32352\n"
M_"v[212:215],a[188:191],a[204:207],0\n"
L_"v252,s[20:23],s62 offen lds\n"
M_"v[212:215],a[184:187],a[200:203],v[212:215]\n"
A_"m0,s34,62464\n"
S_"s62,884736\n"
M_"v[216:219],a[184:187],a[208:211],0\n"
M_"v[216:219],a[188:191],a[212:215],v[216:219]\n"
M_"v[220:223],a[188:191],a[220:223],0\n"
T_"a[188:189],v248 offset:9344\n"
M_"v[220:223],a[184:187],a[216:219],v[220:223]\n"
W_"7)\n"
T_"a[184:185],v248 offset:128\n"
M_"v[224:227],a[168:171],a[192:195],0\n"
W_"6)\n"
L_"v251,s[16:19],s62 offen lds\n"
M_"v[224:227],a[172:175],a[196:199],v[224:227]\n"
A_"m0,s34,63488\n"
T_"a[186:187],v248 offset:4736\n"
M_"v[228:231],a[172:175],a[204:207],0\n"
S_"s62,884736\n"
T_"a[190:191],v248 offset:13952\n"
M_"v[228:231],a[168:171],a[200:203],v[228:231]\n"
M_"v[232:235],a[168:171],a[208:211],0\n"
M_"v[232:235],a[172:175],a[212:215],v[232:235]\n"
M_"v[236:239],a[172:175],a[220:223],0\n"
L_"v252,s[16:19],s62 offen lds\n"
M_"v[236:239],a[168:171],a[216:219],v[236:239]\n"
W_"5)\n"
T_"a[168:169],v248 offset:18560\n"
M_"v[240:243],a[176:179],a[192:195],0\n"
W_"5)\n"
T_"a[170:171],v248 offset:23168\n"
M_"v[240:243],a[180:183],a[196:199],v[240:243]\n"
A_"m0,s34,80896\n"
S_"s62,884736\n"
T_"a[172:173],v248 offset:27776\n"
M_"v[244:247],a[180:183],a[204:207],0\n"
T_"a[174:175],v248 offset:32384\n"
M_"v[244:247],a[176:179],a[200:203],v[244:247]\n"
M_"a[128:131],a[176:179],a[208:211],0\n"
L_"v251,s[20:23],s62 offen lds\n"
M_"a[128:131],a[180:183],a[212:215],a[128:131]\n"
A_"m0,s34,81920\n"
S_"s62,884736\n"
M_"a[132:135],a[180:183],a[220:223],0\n"
T_"a[180:181],v248 offset:9376\n"
M_"a[132:135],a[176:179],a[216:219],a[132:135]\n"
W_"6)\n"
T_"a[176:177],v248 offset:160\n"
M_"a[136:139],a[184:187],a[192:195],0\n"
W_"6)\n"
T_"a[178:179],v248 offset:4768\n"
M_"a[136:139],a[188:191],a[196:199],a[136:139]\n"
T_"a[182:183],v248 offset:13984\n"
M_"a[140:143],a[188:191],a[204:207],0\n"
L_"v252,s[20:23],s62 offen lds\n"
M_"a[140:143],a[184:187],a[200:203],a[140:143]\n"
A_"m0,s35,53248\n"
S_"s62,589824\n"
M_"a[144:147],a[184:187],a[208:211],0\n"
M_"a[144:147],a[188:191],a[212:215],a[144:147]\n"
M_"a[148:151],a[188:191],a[220:223],0\n"
T_"a[188:189],v248 offset:27808\n"
M_"a[148:151],a[184:187],a[216:219],a[148:151]\n"
W_"7)\n"
T_"a[184:185],v248 offset:18592\n"
M_"a[152:155],a[168:171],a[192:195],0\n"
W_"6)\n"
L_"v253,s[28:31],s62 offen lds\n"
M_"a[152:155],a[172:175],a[196:199],a[152:155]\n"
A_"m0,s60,143360\n"
T_"a[186:187],v248 offset:23200\n"
M_"a[156:159],a[172:175],a[204:207],0\n"
S_"s62,1048576\n"
T_"a[190:191],v248 offset:32416\n"
M_"a[156:159],a[168:171],a[200:203],a[156:159]\n"
M_"a[160:163],a[168:171],a[208:211],0\n"
M_"a[160:163],a[172:175],a[212:215],a[160:163]\n"
M_"a[164:167],a[172:175],a[220:223],0\n"
L_"a[224:227],v255,s[36:39],s63 offen sc1\n"
M_"a[164:167],a[168:171],a[216:219],a[164:167]\n"
W_"5)\n"
T_"a[168:169],v248 offset:192\n"
M_"a[0:3],a[176:179],a[192:195],0\n"
W_"5)\n"
T_"a[170:171],v248 offset:4800\n"
M_"a[0:3],a[180:183],a[196:199],a[0:3]\n"
T_"a[172:173],v248 offset:9408\n"
M_"a[4:7],a[180:183],a[204:207],0\n"
T_"a[174:175],v248 offset:14016\n"
M_"a[4:7],a[176:179],a[200:203],a[4:7]\n"
L_"a[228:231],v255,s[36:39],s63 offen offset:1024 sc1\n"
M_"a[8:11],a[176:179],a[208:211],0\n"
S_"s63,20480\n"
M_"a[8:11],a[180:183],a[212:215],a[8:11]\n"
M_"a[12:15],a[180:183],a[220:223],0\n"
T_"a[180:181],v248 offset:27840\n"
M_"a[12:15],a[176:179],a[216:219],a[12:15]\n"
W_"6)\n"
T_"a[176:177],v248 offset:18624\n"
M_"a[16:19],a[184:187],a[192:195],0\n"
W_"6)\n"
L_"a[232:235],v255,s[36:39],s63 offen sc1\n"
M_"a[16:19],a[188:191],a[196:199],a[16:19]\n"
T_"a[178:179],v248 offset:23232\n"
M_"a[20:23],a[188:191],a[204:207],0\n"
T_"a[182:183],v248 offset:32448\n"
M_"a[20:23],a[184:187],a[200:203],a[20:23]\n"
M_"a[24:27],a[184:187],a[208:211],0\n"
M_"a[24:27],a[188:191],a[212:215],a[24:27]\n"
L_"a[236:239],v255,s[36:39],s63 offen offset:1024 sc1\n"
M_"a[28:31],a[188:191],a[220:223],0\n"
S_"s63,38912\n"
T_"a[188:189],v248 offset:9440\n"
M_"a[28:31],a[184:187],a[216:219],a[28:31]\n"
W_"7)\n"
T_"a[184:185],v248 offset:224\n"
M_"a[32:35],a[168:171],a[192:195],0\n"
W_"6)\n"
T_"a[186:187],v248 offset:4832\n"
M_"a[32:35],a[172:175],a[196:199],a[32:35]\n"
T_"a[190:191],v248 offset:14048\n"
M_"a[36:39],a[172:175],a[204:207],0\n"
L_"a[240:243],v255,s[36:39],s63 offen sc1\n"
M_"a[36:39],a[168:171],a[200:203],a[36:39]\n"
M_"a[40:43],a[168:171],a[208:211],0\n"
M_"a[40:43],a[172:175],a[212:215],a[40:43]\n"
M_"a[44:47],a[172:175],a[220:223],0\n"
T_"a[172:173],v248 offset:27872\n"
M_"a[44:47],a[168:171],a[216:219],a[44:47]\n"
W_"6)\n"
L_"a[244:247],v255,s[36:39],s63 offen offset:1024 sc1\n"
M_"a[48:51],a[176:179],a[192:195],0\n"
W_"5)\n"
T_"a[168:169],v248 offset:18656\n"
M_"a[48:51],a[180:183],a[196:199],a[48:51]\n"
S_"s63,57344\n"
T_"a[170:171],v248 offset:23264\n"
M_"a[52:55],a[180:183],a[204:207],0\n"
T_"a[174:175],v248 offset:32480\n"
M_"a[52:55],a[176:179],a[200:203],a[52:55]\n"
M_"a[56:59],a[176:179],a[208:211],0\n"
L_"a[248:251],v255,s[36:39],s63 offen sc1\n"
M_"a[56:59],a[180:183],a[212:215],a[56:59]\n"
M_"a[60:63],a[180:183],a[220:223],0\n"
T_"a[180:181],v248 offset:9472\n"
M_"a[60:63],a[176:179],a[216:219],a[60:63]\n"
W_"6)\n"
T_"a[176:177],v248 offset:256\n"
M_"a[64:67],a[184:187],a[192:195],0\n"
W_"6)\n"
T_"a[178:179],v248 offset:4864\n"
M_"a[64:67],a[188:191],a[196:199],a[64:67]\n"
L_"a[252:255],v255,s[36:39],s63 offen offset:1024 sc1\n"
M_"a[68:71],a[188:191],a[204:207],0\n"
S_"s63,4096\n"
T_"a[182:183],v248 offset:14080\n"
M_"a[68:71],a[184:187],a[200:203],a[68:71]\n"
M_"a[72:75],a[184:187],a[208:211],0\n"
M_"a[72:75],a[188:191],a[212:215],a[72:75]\n"
M_"a[76:79],a[188:191],a[220:223],0\n"
L_"v254,s[24:27],s62 offen lds\n"
M_"a[76:79],a[184:187],a[216:219],a[76:79]\n"
W_"5)\n"
T_"a[184:185],v248 offset:18688\n"
M_"a[80:83],a[168:171],a[192:195],0\n"
W_"5)\n"
T_"a[186:187],v248 offset:23296\n"
M_"a[80:83],a[172:175],a[196:199],a[80:83]\n"
A_"m0,s60,144384\n"
T_"a[188:189],v248 offset:27904\n"
M_"a[84:87],a[172:175],a[204:207],0\n"
S_"s62,1081344\n"
T_"a[190:191],v248 offset:32512\n"
M_"a[84:87],a[168:171],a[200:203],a[84:87]\n"
"v_add_u32 v248,53248,v248\n"
M_"a[88:91],a[168:171],a[208:211],0\n"
M_"a[88:91],a[172:175],a[212:215],a[88:91]\n"
M_"a[92:95],a[172:175],a[220:223],0\n"
L_"v254,s[24:27],s62 offen lds\n"
M_"a[92:95],a[168:171],a[216:219],a[92:95]\n"
W_"5)\n"
T_"a[168:169],v249 offset:36864\n"
M_"a[96:99],a[176:179],a[192:195],0\n"
W_"5)\n"
T_"a[170:171],v249 offset:40960\n"
M_"a[96:99],a[180:183],a[196:199],a[96:99]\n"
A_"m0,s60,145408\n"
T_"a[172:173],v249 offset:45056\n"
M_"a[100:103],a[180:183],a[204:207],0\n"
S_"s62,1114112\n"
T_"a[174:175],v249 offset:49152\n"
M_"a[100:103],a[176:179],a[200:203],a[100:103]\n"
M_"a[104:107],a[176:179],a[208:211],0\n"
M_"a[104:107],a[180:183],a[212:215],a[104:107]\n"
M_"a[108:111],a[180:183],a[220:223],0\n"
L_"v254,s[24:27],s62 offen lds\n"
M_"a[108:111],a[176:179],a[216:219],a[108:111]\n"
W_"6)\n"
T_"a[176:177],v249 offset:36872\n"
M_"a[112:115],a[184:187],a[192:195],0\n"
W_"5)\n"
T_"a[178:179],v249 offset:40968\n"
M_"a[112:115],a[188:191],a[196:199],a[112:115]\n"
A_"m0,s60,146432\n"
T_"a[180:181],v249 offset:45064\n"
M_"a[116:119],a[188:191],a[204:207],0\n"
S_"s62,1146880\n"
T_"a[182:183],v249 offset:49160\n"
M_"a[116:119],a[184:187],a[200:203],a[116:119]\n"
M_"a[120:123],a[184:187],a[208:211],0\n"
M_"a[120:123],a[188:191],a[212:215],a[120:123]\n"
M_"a[124:127],a[188:191],a[220:223],0\n"
L_"v254,s[24:27],s62 offen lds\n"
M_"a[124:127],a[184:187],a[216:219],a[124:127]\n"
W_"6)\n"
T_"a[184:185],v250 offset:36864\n"
M_"v[0:3],a[168:171],a[192:195],0\n"
W_"5)\n"
T_"a[186:187],v250 offset:40960\n"
M_"v[0:3],a[172:175],a[196:199],v[0:3]\n"
A_"m0,s34,106496\n"
T_"a[188:189],v250 offset:45056\n"
M_"v[4:7],a[172:175],a[204:207],0\n"
S_"s62,1179648\n"
T_"a[190:191],v250 offset:49152\n"
M_"v[4:7],a[168:171],a[200:203],v[4:7]\n"
M_"v[8:11],a[168:171],a[208:211],0\n"
M_"v[8:11],a[172:175],a[212:215],v[8:11]\n"
M_"v[12:15],a[172:175],a[220:223],0\n"
L_"v251,s[16:19],s62 offen lds\n"
M_"v[12:15],a[168:171],a[216:219],v[12:15]\n"
W_"6)\n"
T_"a[168:169],v250 offset:36872\n"
M_"v[64:67],a[176:179],a[192:195],0\n"
W_"5)\n"
T_"a[170:171],v250 offset:40968\n"
M_"v[64:67],a[180:183],a[196:199],v[64:67]\n"
A_"m0,s34,107520\n"
T_"a[172:173],v250 offset:45064\n"
M_"v[68:71],a[180:183],a[204:207],0\n"
S_"s62,1179648\n"
T_"a[174:175],v250 offset:49160\n"
M_"v[68:71],a[176:179],a[200:203],v[68:71]\n"
"v_xor_b32 v250,128,v249\n"
M_"v[72:75],a[176:179],a[208:211],0\n"
M_"v[72:75],a[180:183],a[212:215],v[72:75]\n"
M_"v[76:79],a[180:183],a[220:223],0\n"
L_"v252,s[16:19],s62 offen lds\n"
M_"v[76:79],a[176:179],a[216:219],v[76:79]\n"
W_"6)\n"
T_"a[176:177],v250 offset:36864\n"
M_"v[16:19],a[184:187],a[192:195],0\n"
W_"5)\n"
T_"a[178:179],v250 offset:40960\n"
M_"v[16:19],a[188:191],a[196:199],v[16:19]\n"
A_"m0,s34,124928\n"
T_"a[180:181],v250 offset:45056\n"
M_"v[20:23],a[188:191],a[204:207],0\n"
S_"s62,1179648\n"
T_"a[182:183],v250 offset:49152\n"
M_"v[20:23],a[184:187],a[200:203],v[20:23]\n"
M_"v[24:27],a[184:187],a[208:211],0\n"
M_"v[24:27],a[188:191],a[212:215],v[24:27]\n"
M_"v[28:31],a[188:191],a[220:223],0\n"
L_"v251,s[20:23],s62 offen lds\n"
M_"v[28:31],a[184:187],a[216:219],v[28:31]\n"
W_"6)\n"
T_"a[184:185],v250 offset:36872\n"
M_"v[80:83],a[168:171],a[192:195],0\n"
W_"5)\n"
T_"a[186:187],v250 offset:40968\n"
M_"v[80:83],a[172:175],a[196:199],v[80:83]\n"
A_"m0,s34,125952\n"
T_"a[188:189],v250 offset:45064\n"
M_"v[84:87],a[172:175],a[204:207],0\n"
S_"s62,1179648\n"
T_"a[190:191],v250 offset:49160\n"
M_"v[84:87],a[168:171],a[200:203],v[84:87]\n"
"v_xor_b32 v250,192,v249\n"
"v_add_u32 v249,53248,v249\n"
M_"v[88:91],a[168:171],a[208:211],0\n"
M_"v[88:91],a[172:175],a[212:215],v[88:91]\n"
M_"v[92:95],a[172:175],a[220:223],0\n"
L_"v252,s[20:23],s62 offen lds\n"
M_"v[92:95],a[168:171],a[216:219],v[92:95]\n"
W_"6)\n"
T_"a[168:169],v250 offset:36864\n"
M_"v[32:35],a[176:179],a[192:195],0\n"
W_"5)\n"
T_"a[170:171],v250 offset:40960\n"
M_"v[32:35],a[180:183],a[196:199],v[32:35]\n"
A_"m0,s34,115712\n"
T_"a[172:173],v250 offset:45056\n"
M_"v[36:39],a[180:183],a[204:207],0\n"
S_"s62,1474560\n"
T_"a[174:175],v250 offset:49152\n"
M_"v[36:39],a[176:179],a[200:203],v[36:39]\n"
M_"v[40:43],a[176:179],a[208:211],0\n"
M_"v[40:43],a[180:183],a[212:215],v[40:43]\n"
M_"v[44:47],a[180:183],a[220:223],0\n"
L_"v251,s[16:19],s62 offen lds\n"
M_"v[44:47],a[176:179],a[216:219],v[44:47]\n"
W_"6)\n"
T_"a[176:177],v250 offset:36872\n"
M_"v[96:99],a[184:187],a[192:195],0\n"
W_"5)\n"
T_"a[178:179],v250 offset:40968\n"
M_"v[96:99],a[188:191],a[196:199],v[96:99]\n"
A_"m0,s34,116736\n"
T_"a[180:181],v250 offset:45064\n"
M_"v[100:103],a[188:191],a[204:207],0\n"
S_"s62,1474560\n"
T_"a[182:183],v250 offset:49160\n"
M_"v[100:103],a[184:187],a[200:203],v[100:103]\n"
"v_xor_b32 v250,64,v249\n"
M_"v[104:107],a[184:187],a[208:211],0\n"
M_"v[104:107],a[188:191],a[212:215],v[104:107]\n"
M_"v[108:111],a[188:191],a[220:223],0\n"
L_"v252,s[16:19],s62 offen lds\n"
M_"v[108:111],a[184:187],a[216:219],v[108:111]\n"
W_"6)\n"
A_"m0,s34,134144\n"
S_"s62,1474560\n"
M_"v[48:51],a[168:171],a[192:195],0\n"
W_"4)\n"
M_"v[48:51],a[172:175],a[196:199],v[48:51]\n"
M_"v[52:55],a[172:175],a[204:207],0\n"
"s_waitcnt vmcnt(18) lgkmcnt(0)\n"
"s_barrier\n"
T_"a[184:185],v248 offset:0\n"
M_"v[52:55],a[168:171],a[200:203],v[52:55]\n"
T_"a[186:187],v248 offset:4608\n"
M_"v[56:59],a[168:171],a[208:211],0\n"
T_"a[188:189],v248 offset:9216\n"
M_"v[56:59],a[172:175],a[212:215],v[56:59]\n"
T_"a[190:191],v248 offset:13824\n"
M_"v[60:63],a[172:175],a[220:223],0\n"
L_"v251,s[20:23],s62 offen lds\n"
M_"v[60:63],a[168:171],a[216:219],v[60:63]\n"
A_"m0,s34,135168\n"
T_"a[168:169],v248 offset:18432\n"
M_"v[112:115],a[176:179],a[192:195],0\n"
S_"s62,1474560\n"
T_"a[170:171],v248 offset:23040\n"
M_"v[112:115],a[180:183],a[196:199],v[112:115]\n"
T_"a[172:173],v248 offset:27648\n"
M_"v[116:119],a[180:183],a[204:207],0\n"
T_"a[174:175],v248 offset:32256\n"
M_"v[116:119],a[176:179],a[200:203],v[116:119]\n"
M_"v[120:123],a[176:179],a[208:211],0\n"
M_"v[120:123],a[180:183],a[212:215],v[120:123]\n"
M_"v[124:127],a[180:183],a[220:223],0\n"
L_"v252,s[20:23],s62 offen lds\n"
M_"v[124:127],a[176:179],a[216:219],v[124:127]\n"
"s_waitcnt vmcnt(19) lgkmcnt(6)\n"
T_"a[176:177],v248 offset:32\n"
M_"v[128:131],a[184:187],a[224:227],v[128:131]\n"
"s_waitcnt vmcnt(18) lgkmcnt(5)\n"
T_"a[178:179],v248 offset:4640\n"
M_"v[128:131],a[188:191],a[228:231],v[128:131]\n"
"s_waitcnt vmcnt(16)\n"
T_"a[180:181],v248 offset:9248\n"
M_"v[132:135],a[188:191],a[236:239],v[132:135]\n"
A_"m0,s35,106496\n"
T_"a[182:183],v248 offset:13856\n"
M_"v[132:135],a[184:187],a[232:235],v[132:135]\n"
"s_waitcnt vmcnt(15)\n"
S_"s62,1179648\n"
M_"v[136:139],a[184:187],a[240:243],v[136:139]\n"
"s_waitcnt vmcnt(14)\n"
M_"v[136:139],a[188:191],a[244:247],v[136:139]\n"
"s_waitcnt vmcnt(12)\n"
M_"v[140:143],a[188:191],a[252:255],v[140:143]\n"
L_"v253,s[28:31],s62 offen lds\n"
M_"v[140:143],a[184:187],a[248:251],v[140:143]\n"
W_"6)\n"
T_"a[184:185],v248 offset:18464\n"
M_"v[144:147],a[168:171],a[224:227],v[144:147]\n"
W_"5)\n"
T_"a[186:187],v248 offset:23072\n"
M_"v[144:147],a[172:175],a[228:231],v[144:147]\n"
A_"m0,s60,36864\n"
T_"a[188:189],v248 offset:27680\n"
M_"v[148:151],a[172:175],a[236:239],v[148:151]\n"
S_"s62,1572864\n"
T_"a[190:191],v248 offset:32288\n"
M_"v[148:151],a[168:171],a[232:235],v[148:151]\n"
M_"v[152:155],a[168:171],a[240:243],v[152:155]\n"
M_"v[152:155],a[172:175],a[244:247],v[152:155]\n"
M_"v[156:159],a[172:175],a[252:255],v[156:159]\n"
L_"a[192:195],v255,s[36:39],s63 offen sc1\n"
M_"v[156:159],a[168:171],a[248:251],v[156:159]\n"
W_"6)\n"
T_"a[168:169],v248 offset:64\n"
M_"v[160:163],a[176:179],a[224:227],v[160:163]\n"
W_"5)\n"
T_"a[170:171],v248 offset:4672\n"
M_"v[160:163],a[180:183],a[228:231],v[160:163]\n"
T_"a[172:173],v248 offset:9280\n"
M_"v[164:167],a[180:183],a[236:239],v[164:167]\n"
T_"a[174:175],v248 offset:13888\n"
M_"v[164:167],a[176:179],a[232:235],v[164:167]\n"
M_"v[168:171],a[176:179],a[240:243],v[168:171]\n"
M_"v[168:171],a[180:183],a[244:247],v[168:171]\n"
M_"v[172:175],a[180:183],a[252:255],v[172:175]\n"
T_"a[180:181],v248 offset:27712\n"
M_"v[172:175],a[176:179],a[248:251],v[172:175]\n"
W_"7)\n"
L_"a[196:199],v255,s[36:39],s63 offen offset:1024 sc1\n"
M_"v[176:179],a[184:187],a[224:227],v[176:179]\n"
W_"5)\n"
T_"a[176:177],v248 offset:18496\n"
M_"v[176:179],a[188:191],a[228:231],v[176:179]\n"
S_"s63,22528\n"
T_"a[178:179],v248 offset:23104\n"
M_"v[180:183],a[188:191],a[236:239],v[180:183]\n"
T_"a[182:183],v248 offset:32320\n"
M_"v[180:183],a[184:187],a[232:235],v[180:183]\n"
M_"v[184:187],a[184:187],a[240:243],v[184:187]\n"
M_"v[184:187],a[188:191],a[244:247],v[184:187]\n"
M_"v[188:191],a[188:191],a[252:255],v[188:191]\n"
T_"a[188:189],v248 offset:9312\n"
M_"v[188:191],a[184:187],a[248:251],v[188:191]\n"
W_"7)\n"
T_"a[184:185],v248 offset:96\n"
M_"v[192:195],a[168:171],a[224:227],v[192:195]\n"
W_"6)\n"
L_"a[200:203],v255,s[36:39],s63 offen sc1\n"
M_"v[192:195],a[172:175],a[228:231],v[192:195]\n"
T_"a[186:187],v248 offset:4704\n"
M_"v[196:199],a[172:175],a[236:239],v[196:199]\n"
T_"a[190:191],v248 offset:13920\n"
M_"v[196:199],a[168:171],a[232:235],v[196:199]\n"
M_"v[200:203],a[168:171],a[240:243],v[200:203]\n"
M_"v[200:203],a[172:175],a[244:247],v[200:203]\n"
M_"v[204:207],a[172:175],a[252:255],v[204:207]\n"
T_"a[172:173],v248 offset:27744\n"
M_"v[204:207],a[168:171],a[248:251],v[204:207]\n"
W_"6)\n"
T_"a[168:169],v248 offset:18528\n"
M_"v[208:211],a[176:179],a[224:227],v[208:211]\n"
W_"6)\n"
T_"a[170:171],v248 offset:23136\n"
M_"v[208:211],a[180:183],a[228:231],v[208:211]\n"
L_"a[204:207],v255,s[36:39],s63 offen offset:1024 sc1\n"
M_"v[212:215],a[180:183],a[236:239],v[212:215]\n"
S_"s63,40960\n"
T_"a[174:175],v248 offset:32352\n"
M_"v[212:215],a[176:179],a[232:235],v[212:215]\n"
M_"v[216:219],a[176:179],a[240:243],v[216:219]\n"
M_"v[216:219],a[180:183],a[244:247],v[216:219]\n"
M_"v[220:223],a[180:183],a[252:255],v[220:223]\n"
T_"a[180:181],v248 offset:9344\n"
M_"v[220:223],a[176:179],a[248:251],v[220:223]\n"
W_"6)\n"
T_"a[176:177],v248 offset:128\n"
M_"v[224:227],a[184:187],a[224:227],v[224:227]\n"
W_"6)\n"
T_"a[178:179],v248 offset:4736\n"
M_"v[224:227],a[188:191],a[228:231],v[224:227]\n"
T_"a[182:183],v248 offset:13952\n"
M_"v[228:231],a[188:191],a[236:239],v[228:231]\n"
L_"a[208:211],v255,s[36:39],s63 offen sc1\n"
M_"v[228:231],a[184:187],a[232:235],v[228:231]\n"
M_"v[232:235],a[184:187],a[240:243],v[232:235]\n"
M_"v[232:235],a[188:191],a[244:247],v[232:235]\n"
M_"v[236:239],a[188:191],a[252:255],v[236:239]\n"
T_"a[188:189],v248 offset:27776\n"
M_"v[236:239],a[184:187],a[248:251],v[236:239]\n"
W_"6)\n"
T_"a[184:185],v248 offset:18560\n"
M_"v[240:243],a[168:171],a[224:227],v[240:243]\n"
W_"6)\n"
T_"a[186:187],v248 offset:23168\n"
M_"v[240:243],a[172:175],a[228:231],v[240:243]\n"
T_"a[190:191],v248 offset:32384\n"
M_"v[244:247],a[172:175],a[236:239],v[244:247]\n"
M_"v[244:247],a[168:171],a[232:235],v[244:247]\n"
L_"a[212:215],v255,s[36:39],s63 offen offset:1024 sc1\n"
M_"a[128:131],a[168:171],a[240:243],a[128:131]\n"
S_"s63,59392\n"
M_"a[128:131],a[172:175],a[244:247],a[128:131]\n"
M_"a[132:135],a[172:175],a[252:255],a[132:135]\n"
T_"a[172:173],v248 offset:9376\n"
M_"a[132:135],a[168:171],a[248:251],a[132:135]\n"
W_"6)\n"
T_"a[168:169],v248 offset:160\n"
M_"a[136:139],a[176:179],a[224:227],a[136:139]\n"
W_"6)\n"
T_"a[170:171],v248 offset:4768\n"
M_"a[136:139],a[180:183],a[228:231],a[136:139]\n"
T_"a[174:175],v248 offset:13984\n"
M_"a[140:143],a[180:183],a[236:239],a[140:143]\n"
M_"a[140:143],a[176:179],a[232:235],a[140:143]\n"
M_"a[144:147],a[176:179],a[240:243],a[144:147]\n"
L_"a[216:219],v255,s[36:39],s63 offen sc1\n"
M_"a[144:147],a[180:183],a[244:247],a[144:147]\n"
M_"a[148:151],a[180:183],a[252:255],a[148:151]\n"
T_"a[180:181],v248 offset:27808\n"
M_"a[148:151],a[176:179],a[248:251],a[148:151]\n"
W_"6)\n"
T_"a[176:177],v248 offset:18592\n"
M_"a[152:155],a[184:187],a[224:227],a[152:155]\n"
W_"6)\n"
T_"a[178:179],v248 offset:23200\n"
M_"a[152:155],a[188:191],a[228:231],a[152:155]\n"
T_"a[182:183],v248 offset:32416\n"
M_"a[156:159],a[188:191],a[236:239],a[156:159]\n"
M_"a[156:159],a[184:187],a[232:235],a[156:159]\n"
M_"a[160:163],a[184:187],a[240:243],a[160:163]\n"
M_"a[160:163],a[188:191],a[244:247],a[160:163]\n"
L_"a[220:223],v255,s[36:39],s63 offen offset:1024 sc1\n"
M_"a[164:167],a[188:191],a[252:255],a[164:167]\n"
S_"s63,6144\n"
T_"a[188:189],v248 offset:9408\n"
M_"a[164:167],a[184:187],a[248:251],a[164:167]\n"
W_"6)\n"
T_"a[184:185],v248 offset:192\n"
M_"a[0:3],a[168:171],a[224:227],a[0:3]\n"
W_"6)\n"
T_"a[186:187],v248 offset:4800\n"
M_"a[0:3],a[172:175],a[228:231],a[0:3]\n"
T_"a[190:191],v248 offset:14016\n"
M_"a[4:7],a[172:175],a[236:239],a[4:7]\n"
M_"a[4:7],a[168:171],a[232:235],a[4:7]\n"
M_"a[8:11],a[168:171],a[240:243],a[8:11]\n"
M_"a[8:11],a[172:175],a[244:247],a[8:11]\n"
M_"a[12:15],a[172:175],a[252:255],a[12:15]\n"
L_"v254,s[24:27],s62 offen lds\n"
M_"a[12:15],a[168:171],a[248:251],a[12:15]\n"
W_"5)\n"
T_"a[168:169],v248 offset:18624\n"
M_"a[16:19],a[176:179],a[224:227],a[16:19]\n"
W_"5)\n"
T_"a[170:171],v248 offset:23232\n"
M_"a[16:19],a[180:183],a[228:231],a[16:19]\n"
A_"m0,s60,37888\n"
T_"a[172:173],v248 offset:27840\n"
M_"a[20:23],a[180:183],a[236:239],a[20:23]\n"
S_"s62,1605632\n"
T_"a[174:175],v248 offset:32448\n"
M_"a[20:23],a[176:179],a[232:235],a[20:23]\n"
M_"a[24:27],a[176:179],a[240:243],a[24:27]\n"
M_"a[24:27],a[180:183],a[244:247],a[24:27]\n"
M_"a[28:31],a[180:183],a[252:255],a[28:31]\n"
L_"v254,s[24:27],s62 offen lds\n"
M_"a[28:31],a[176:179],a[248:251],a[28:31]\n"
W_"5)\n"
T_"a[176:177],v248 offset:224\n"
M_"a[32:35],a[184:187],a[224:227],a[32:35]\n"
W_"5)\n"
T_"a[178:179],v248 offset:4832\n"
M_"a[32:35],a[188:191],a[228:231],a[32:35]\n"
A_"m0,s60,38912\n"
T_"a[180:181],v248 offset:9440\n"
M_"a[36:39],a[188:191],a[236:239],a[36:39]\n"
S_"s62,1638400\n"
T_"a[182:183],v248 offset:14048\n"
M_"a[36:39],a[184:187],a[232:235],a[36:39]\n"
M_"a[40:43],a[184:187],a[240:243],a[40:43]\n"
M_"a[40:43],a[188:191],a[244:247],a[40:43]\n"
M_"a[44:47],a[188:191],a[252:255],a[44:47]\n"
L_"v254,s[24:27],s62 offen lds\n"
M_"a[44:47],a[184:187],a[248:251],a[44:47]\n"
W_"6)\n"
T_"a[184:185],v248 offset:18656\n"
M_"a[48:51],a[168:171],a[224:227],a[48:51]\n"
W_"5)\n"
T_"a[186:187],v248 offset:23264\n"
M_"a[48:51],a[172:175],a[228:231],a[48:51]\n"
A_"m0,s60,39936\n"
T_"a[188:189],v248 offset:27872\n"
M_"a[52:55],a[172:175],a[236:239],a[52:55]\n"
S_"s62,1671168\n"
T_"a[190:191],v248 offset:32480\n"
M_"a[52:55],a[168:171],a[232:235],a[52:55]\n"
M_"a[56:59],a[168:171],a[240:243],a[56:59]\n"
M_"a[56:59],a[172:175],a[244:247],a[56:59]\n"
M_"a[60:63],a[172:175],a[252:255],a[60:63]\n"
L_"v254,s[24:27],s62 offen lds\n"
M_"a[60:63],a[168:171],a[248:251],a[60:63]\n"
W_"6)\n"
T_"a[168:169],v248 offset:256\n"
M_"a[64:67],a[176:179],a[224:227],a[64:67]\n"
W_"5)\n"
T_"a[170:171],v248 offset:4864\n"
M_"a[64:67],a[180:183],a[228:231],a[64:67]\n"
A_"m0,s34,0\n"
T_"a[172:173],v248 offset:9472\n"
M_"a[68:71],a[180:183],a[236:239],a[68:71]\n"
S_"s62,1769472\n"
T_"a[174:175],v248 offset:14080\n"
M_"a[68:71],a[176:179],a[232:235],a[68:71]\n"
M_"a[72:75],a[176:179],a[240:243],a[72:75]\n"
M_"a[72:75],a[180:183],a[244:247],a[72:75]\n"
M_"a[76:79],a[180:183],a[252:255],a[76:79]\n"
L_"v251,s[16:19],s62 offen lds\n"
M_"a[76:79],a[176:179],a[248:251],a[76:79]\n"
W_"6)\n"
T_"a[176:177],v248 offset:18688\n"
M_"a[80:83],a[184:187],a[224:227],a[80:83]\n"
W_"5)\n"
T_"a[178:179],v248 offset:23296\n"
M_"a[80:83],a[188:191],a[228:231],a[80:83]\n"
A_"m0,s34,1024\n"
T_"a[180:181],v248 offset:27904\n"
M_"a[84:87],a[188:191],a[236:239],a[84:87]\n"
S_"s62,1769472\n"
T_"a[182:183],v248 offset:32512\n"
M_"a[84:87],a[184:187],a[232:235],a[84:87]\n"
"v_add_u32 v248,53248,v248\n"
M_"a[88:91],a[184:187],a[240:243],a[88:91]\n"
M_"a[88:91],a[188:191],a[244:247],a[88:91]\n"
M_"a[92:95],a[188:191],a[252:255],a[92:95]\n"
L_"v252,s[16:19],s62 offen lds\n"
M_"a[92:95],a[184:187],a[248:251],a[92:95]\n"
W_"6)\n"
T_"a[184:185],v249 offset:36864\n"
M_"a[96:99],a[168:171],a[224:227],a[96:99]\n"
W_"5)\n"
T_"a[186:187],v249 offset:40960\n"
M_"a[96:99],a[172:175],a[228:231],a[96:99]\n"
A_"m0,s34,18432\n"
T_"a[188:189],v249 offset:45056\n"
M_"a[100:103],a[172:175],a[236:239],a[100:103]\n"
S_"s62,1769472\n"
T_"a[190:191],v249 offset:49152\n"
M_"a[100:103],a[168:171],a[232:235],a[100:103]\n"
M_"a[104:107],a[168:171],a[240:243],a[104:107]\n"
M_"a[104:107],a[172:175],a[244:247],a[104:107]\n"
M_"a[108:111],a[172:175],a[252:255],a[108:111]\n"
L_"v251,s[20:23],s62 offen lds\n"
M_"a[108:111],a[168:171],a[248:251],a[108:111]\n"
W_"6)\n"
T_"a[168:169],v249 offset:36872\n"
M_"a[112:115],a[176:179],a[224:227],a[112:115]\n"
W_"5)\n"
T_"a[170:171],v249 offset:40968\n"
M_"a[112:115],a[180:183],a[228:231],a[112:115]\n"
A_"m0,s34,19456\n"
T_"a[172:173],v249 offset:45064\n"
M_"a[116:119],a[180:183],a[236:239],a[116:119]\n"
S_"s62,1769472\n"
T_"a[174:175],v249 offset:49160\n"
M_"a[116:119],a[176:179],a[232:235],a[116:119]\n"
M_"a[120:123],a[176:179],a[240:243],a[120:123]\n"
M_"a[120:123],a[180:183],a[244:247],a[120:123]\n"
M_"a[124:127],a[180:183],a[252:255],a[124:127]\n"
L_"v252,s[20:23],s62 offen lds\n"
M_"a[124:127],a[176:179],a[248:251],a[124:127]\n"
W_"6)\n"
T_"a[176:177],v250 offset:36864\n"
M_"v[0:3],a[184:187],a[224:227],v[0:3]\n"
W_"5)\n"
T_"a[178:179],v250 offset:40960\n"
M_"v[0:3],a[188:191],a[228:231],v[0:3]\n"
A_"m0,s34,9216\n"
T_"a[180:181],v250 offset:45056\n"
M_"v[4:7],a[188:191],a[236:239],v[4:7]\n"
S_"s62,2064384\n"
T_"a[182:183],v250 offset:49152\n"
M_"v[4:7],a[184:187],a[232:235],v[4:7]\n"
M_"v[8:11],a[184:187],a[240:243],v[8:11]\n"
M_"v[8:11],a[188:191],a[244:247],v[8:11]\n"
M_"v[12:15],a[188:191],a[252:255],v[12:15]\n"
L_"v251,s[16:19],s62 offen lds\n"
M_"v[12:15],a[184:187],a[248:251],v[12:15]\n"
W_"6)\n"
T_"a[184:185],v250 offset:36872\n"
M_"v[64:67],a[168:171],a[224:227],v[64:67]\n"
W_"5)\n"
T_"a[186:187],v250 offset:40968\n"
M_"v[64:67],a[172:175],a[228:231],v[64:67]\n"
A_"m0,s34,10240\n"
T_"a[188:189],v250 offset:45064\n"
M_"v[68:71],a[172:175],a[236:239],v[68:71]\n"
S_"s62,2064384\n"
T_"a[190:191],v250 offset:49160\n"
M_"v[68:71],a[168:171],a[232:235],v[68:71]\n"
"v_xor_b32 v250,128,v249\n"
M_"v[72:75],a[168:171],a[240:243],v[72:75]\n"
M_"v[72:75],a[172:175],a[244:247],v[72:75]\n"
M_"v[76:79],a[172:175],a[252:255],v[76:79]\n"
L_"v252,s[16:19],s62 offen lds\n"
M_"v[76:79],a[168:171],a[248:251],v[76:79]\n"
W_"6)\n"
T_"a[168:169],v250 offset:36864\n"
M_"v[16:19],a[176:179],a[224:227],v[16:19]\n"
W_"5)\n"
T_"a[170:171],v250 offset:40960\n"
M_"v[16:19],a[180:183],a[228:231],v[16:19]\n"
A_"m0,s34,27648\n"
T_"a[172:173],v250 offset:45056\n"
M_"v[20:23],a[180:183],a[236:239],v[20:23]\n"
S_"s62,2064384\n"
T_"a[174:175],v250 offset:49152\n"
M_"v[20:23],a[176:179],a[232:235],v[20:23]\n"
M_"v[24:27],a[176:179],a[240:243],v[24:27]\n"
M_"v[24:27],a[180:183],a[244:247],v[24:27]\n"
M_"v[28:31],a[180:183],a[252:255],v[28:31]\n"
L_"v251,s[20:23],s62 offen lds\n"
M_"v[28:31],a[176:179],a[248:251],v[28:31]\n"
W_"6)\n"
T_"a[176:177],v250 offset:36872\n"
M_"v[80:83],a[184:187],a[224:227],v[80:83]\n"
W_"5)\n"
T_"a[178:179],v250 offset:40968\n"
M_"v[80:83],a[188:191],a[228:231],v[80:83]\n"
A_"m0,s34,28672\n"
T_"a[180:181],v250 offset:45064\n"
M_"v[84:87],a[188:191],a[236:239],v[84:87]\n"
S_"s62,2064384\n"
T_"a[182:183],v250 offset:49160\n"
M_"v[84:87],a[184:187],a[232:235],v[84:87]\n"
"v_xor_b32 v250,192,v249\n"
"v_add_u32 v249,53248,v249\n"
M_"v[88:91],a[184:187],a[240:243],v[88:91]\n"
M_"v[88:91],a[188:191],a[244:247],v[88:91]\n"
M_"v[92:95],a[188:191],a[252:255],v[92:95]\n"
L_"v252,s[20:23],s62 offen lds\n"
M_"v[92:95],a[184:187],a[248:251],v[92:95]\n"
W_"6)\n"
T_"a[184:185],v250 offset:36864\n"
M_"v[32:35],a[168:171],a[224:227],v[32:35]\n"
W_"5)\n"
T_"a[186:187],v250 offset:40960\n"
M_"v[32:35],a[172:175],a[228:231],v[32:35]\n"
A_"m0,s35,0\n"
T_"a[188:189],v250 offset:45056\n"
M_"v[36:39],a[172:175],a[236:239],v[36:39]\n"
S_"s62,1769472\n"
T_"a[190:191],v250 offset:49152\n"
M_"v[36:39],a[168:171],a[232:235],v[36:39]\n"
M_"v[40:43],a[168:171],a[240:243],v[40:43]\n"
M_"v[40:43],a[172:175],a[244:247],v[40:43]\n"
M_"v[44:47],a[172:175],a[252:255],v[44:47]\n"
L_"v253,s[28:31],s62 offen lds\n"
M_"v[44:47],a[168:171],a[248:251],v[44:47]\n"
W_"6)\n"
T_"a[168:169],v250 offset:36872\n"
M_"v[96:99],a[176:179],a[224:227],v[96:99]\n"
W_"5)\n"
T_"a[170:171],v250 offset:40968\n"
M_"v[96:99],a[180:183],a[228:231],v[96:99]\n"
S_"m0,159744\n"
T_"a[172:173],v250 offset:45064\n"
M_"v[100:103],a[180:183],a[236:239],v[100:103]\n"
S_"s62,2097152\n"
T_"a[174:175],v250 offset:49160\n"
M_"v[100:103],a[176:179],a[232:235],v[100:103]\n"
"v_xor_b32 v250,64,v249\n"
M_"v[104:107],a[176:179],a[240:243],v[104:107]\n"
M_"v[104:107],a[180:183],a[244:247],v[104:107]\n"
M_"v[108:111],a[180:183],a[252:255],v[108:111]\n"
M_"v[108:111],a[176:179],a[248:251],v[108:111]\n"
W_"6)\n"
M_"v[48:51],a[184:187],a[224:227],v[48:51]\n"
W_"4)\n"
M_"v[48:51],a[188:191],a[228:231],v[48:51]\n"
M_"v[52:55],a[188:191],a[236:239],v[52:55]\n"
"s_waitcnt vmcnt(21) lgkmcnt(0)\n"
"s_barrier\n"
T_"a[176:177],v248 offset:0\n"
M_"v[52:55],a[184:187],a[232:235],v[52:55]\n"
T_"a[178:179],v248 offset:4608\n"
M_"v[56:59],a[184:187],a[240:243],v[56:59]\n"
T_"a[180:181],v248 offset:9216\n"
M_"v[56:59],a[188:191],a[244:247],v[56:59]\n"
T_"a[182:183],v248 offset:13824\n"
M_"v[60:63],a[188:191],a[252:255],v[60:63]\n"
T_"a[188:189],v248 offset:27648\n"
M_"v[60:63],a[184:187],a[248:251],v[60:63]\n"
T_"a[184:185],v248 offset:18432\n"
M_"v[112:115],a[168:171],a[224:227],v[112:115]\n"
T_"a[186:187],v248 offset:23040\n"
M_"v[112:115],a[172:175],a[228:231],v[112:115]\n"
T_"a[190:191],v248 offset:32256\n"
M_"v[116:119],a[172:175],a[236:239],v[116:119]\n"
M_"v[116:119],a[168:171],a[232:235],v[116:119]\n"
M_"v[120:123],a[168:171],a[240:243],v[120:123]\n"
M_"v[120:123],a[172:175],a[244:247],v[120:123]\n"
M_"v[124:127],a[172:175],a[252:255],v[124:127]\n"
T_"a[172:173],v248 offset:9248\n"
M_"v[124:127],a[168:171],a[248:251],v[124:127]\n"
"s_waitcnt vmcnt(20) lgkmcnt(7)\n"
T_"a[168:169],v248 offset:32\n"
M_"v[128:131],a[176:179],a[192:195],v[128:131]\n"
"s_waitcnt vmcnt(19) lgkmcnt(6)\n"
L_"a[224:227],v255,s[36:39],s63 offen sc1\n"
M_"v[128:131],a[180:183],a[196:199],v[128:131]\n"
"s_waitcnt vmcnt(18)\n"
T_"a[170:171],v248 offset:4640\n"
M_"v[132:135],a[180:183],a[204:207],v[132:135]\n"
T_"a[174:175],v248 offset:13856\n"
M_"v[132:135],a[176:179],a[200:203],v[132:135]\n"
"s_waitcnt vmcnt(17)\n"
M_"v[136:139],a[176:179],a[208:211],v[136:139]\n"
"s_waitcnt vmcnt(16)\n"
M_"v[136:139],a[180:183],a[212:215],v[136:139]\n"
"s_waitcnt vmcnt(14)\n"
M_"v[140:143],a[180:183],a[220:223],v[140:143]\n"
T_"a[180:181],v248 offset:27680\n"
M_"v[140:143],a[176:179],a[216:219],v[140:143]\n"
W_"6)\n"
T_"a[176:177],v248 offset:18464\n"
M_"v[144:147],a[184:187],a[192:195],v[144:147]\n"
W_"6)\n"
T_"a[178:179],v248 offset:23072\n"
M_"v[144:147],a[188:191],a[196:199],v[144:147]\n"
L_"a[228:231],v255,s[36:39],s63 offen offset:1024 sc1\n"
M_"v[148:151],a[188:191],a[204:207],v[148:151]\n"
S_"s63,24576\n"
T_"a[182:183],v248 offset:32288\n"
M_"v[148:151],a[184:187],a[200:203],v[148:151]\n"
M_"v[152:155],a[184:187],a[208:211],v[152:155]\n"
M_"v[152:155],a[188:191],a[212:215],v[152:155]\n"
M_"v[156:159],a[188:191],a[220:223],v[156:159]\n"
T_"a[188:189],v248 offset:9280\n"
M_"v[156:159],a[184:187],a[216:219],v[156:159]\n"
W_"6)\n"
T_"a[184:185],v248 offset:64\n"
M_"v[160:163],a[168:171],a[192:195],v[160:163]\n"
W_"6)\n"
T_"a[186:187],v248 offset:4672\n"
M_"v[160:163],a[172:175],a[196:199],v[160:163]\n"
T_"a[190:191],v248 offset:13888\n"
M_"v[164:167],a[172:175],a[204:207],v[164:167]\n"
L_"a[232:235],v255,s[36:39],s63 offen sc1\n"
M_"v[164:167],a[168:171],a[200:203],v[164:167]\n"
M_"v[168:171],a[168:171],a[208:211],v[168:171]\n"
M_"v[168:171],a[172:175],a[212:215],v[168:171]\n"
M_"v[172:175],a[172:175],a[220:223],v[172:175]\n"
T_"a[172:173],v248 offset:27712\n"
M_"v[172:175],a[168:171],a[216:219],v[172:175]\n"
W_"6)\n"
T_"a[168:169],v248 offset:18496\n"
M_"v[176:179],a[176:179],a[192:195],v[176:179]\n"
W_"6)\n"
T_"a[170:171],v248 offset:23104\n"
M_"v[176:179],a[180:183],a[196:199],v[176:179]\n"
T_"a[174:175],v248 offset:32320\n"
M_"v[180:183],a[180:183],a[204:207],v[180:183]\n"
M_"v[180:183],a[176:179],a[200:203],v[180:183]\n"
L_"a[236:239],v255,s[36:39],s63 offen offset:1024 sc1\n"
M_"v[184:187],a[176:179],a[208:211],v[184:187]\n"
S_"s63,43008\n"
M_"v[184:187],a[180:183],a[212:215],v[184:187]\n"
M_"v[188:191],a[180:183],a[220:223],v[188:191]\n"
T_"a[180:181],v248 offset:9312\n"
M_"v[188:191],a[176:179],a[216:219],v[188:191]\n"
W_"6)\n"
T_"a[176:177],v248 offset:96\n"
M_"v[192:195],a[184:187],a[192:195],v[192:195]\n"
W_"6)\n"
T_"a[178:179],v248 offset:4704\n"
M_"v[192:195],a[188:191],a[196:199],v[192:195]\n"
T_"a[182:183],v248 offset:13920\n"
M_"v[196:199],a[188:191],a[204:207],v[196:199]\n"
M_"v[196:199],a[184:187],a[200:203],v[196:199]\n"
M_"v[200:203],a[184:187],a[208:211],v[200:203]\n"
L_"a[240:243],v255,s[36:39],s63 offen sc1\n"
M_"v[200:203],a[188:191],a[212:215],v[200:203]\n"
M_"v[204:207],a[188:191],a[220:223],v[204:207]\n"
T_"a[188:189],v248 offset:27744\n"
M_"v[204:207],a[184:187],a[216:219],v[204:207]\n"
W_"6)\n"
T_"a[184:185],v248 offset:18528\n"
M_"v[208:211],a[168:171],a[192:195],v[208:211]\n"
W_"6)\n"
T_"a[186:187],v248 offset:23136\n"
M_"v[208:211],a[172:175],a[196:199],v[208:211]\n"
T_"a[190:191],v248 offset:32352\n"
M_"v[212:215],a[172:175],a[204:207],v[212:215]\n"
M_"v[212:215],a[168:171],a[200:203],v[212:215]\n"
M_"v[216:219],a[168:171],a[208:211],v[216:219]\n"
M_"v[216:219],a[172:175],a[212:215],v[216:219]\n"
L_"a[244:247],v255,s[36:39],s63 offen offset:1024 sc1\n"
M_"v[220:223],a[172:175],a[220:223],v[220:223]\n"
S_"s63,61440\n"
T_"a[172:173],v248 offset:9344\n"
M_"v[220:223],a[168:171],a[216:219],v[220:223]\n"
W_"6)\n"
T_"a[168:169],v248 offset:128\n"
M_"v[224:227],a[176:179],a[192:195],v[224:227]\n"
W_"6)\n"
T_"a[170:171],v248 offset:4736\n"
M_"v[224:227],a[180:183],a[196:199],v[224:227]\n"
T_"a[174:175],v248 offset:13952\n"
M_"v[228:231],a[180:183],a[204:207],v[228:231]\n"
M_"v[228:231],a[176:179],a[200:203],v[228:231]\n"
M_"v[232:235],a[176:179],a[208:211],v[232:235]\n"
M_"v[232:235],a[180:183],a[212:215],v[232:235]\n"
M_"v[236:239],a[180:183],a[220:223],v[236:239]\n"
L_"a[248:251],v255,s[36:39],s63 offen sc1\n"
M_"v[236:239],a[176:179],a[216:219],v[236:239]\n"
W_"5)\n"
T_"a[176:177],v248 offset:18560\n"
M_"v[240:243],a[184:187],a[192:195],v[240:243]\n"
W_"5)\n"
T_"a[178:179],v248 offset:23168\n"
M_"v[240:243],a[188:191],a[196:199],v[240:243]\n"
T_"a[180:181],v248 offset:27776\n"
M_"v[244:247],a[188:191],a[204:207],v[244:247]\n"
T_"a[182:183],v248 offset:32384\n"
M_"v[244:247],a[184:187],a[200:203],v[244:247]\n"
M_"a[128:131],a[184:187],a[208:211],a[128:131]\n"
M_"a[128:131],a[188:191],a[212:215],a[128:131]\n"
M_"a[132:135],a[188:191],a[220:223],a[132:135]\n"
T_"a[188:189],v248 offset:9376\n"
M_"a[132:135],a[184:187],a[216:219],a[132:135]\n"
W_"6)\n"
L_"a[252:255],v255,s[36:39],s63 offen offset:1024 sc1\n"
M_"a[136:139],a[168:171],a[192:195],a[136:139]\n"
W_"5)\n"
T_"a[184:185],v248 offset:160\n"
M_"a[136:139],a[172:175],a[196:199],a[136:139]\n"
S_"s63,8192\n"
T_"a[186:187],v248 offset:4768\n"
M_"a[140:143],a[172:175],a[204:207],a[140:143]\n"
T_"a[190:191],v248 offset:13984\n"
M_"a[140:143],a[168:171],a[200:203],a[140:143]\n"
M_"a[144:147],a[168:171],a[208:211],a[144:147]\n"
M_"a[144:147],a[172:175],a[212:215],a[144:147]\n"
M_"a[148:151],a[172:175],a[220:223],a[148:151]\n"
T_"a[172:173],v248 offset:27808\n"
M_"a[148:151],a[168:171],a[216:219],a[148:151]\n"
W_"7)\n"
T_"a[168:169],v248 offset:18592\n"
M_"a[152:155],a[176:179],a[192:195],a[152:155]\n"
W_"6)\n"
L_"v255,s[52:55],s77 offen lds\n"
M_"a[152:155],a[180:183],a[196:199],a[152:155]\n"
A_"m0,s60,90112\n"
T_"a[170:171],v248 offset:23200\n"
M_"a[156:159],a[180:183],a[204:207],a[156:159]\n"
T_"a[174:175],v248 offset:32416\n"
M_"a[156:159],a[176:179],a[200:203],a[156:159]\n"
M_"a[160:163],a[176:179],a[208:211],a[160:163]\n"
M_"a[160:163],a[180:183],a[212:215],a[160:163]\n"
M_"a[164:167],a[180:183],a[220:223],a[164:167]\n"
T_"a[180:181],v248 offset:9408\n"
M_"a[164:167],a[176:179],a[216:219],a[164:167]\n"
W_"6)\n"
T_"a[176:177],v248 offset:192\n"
M_"a[0:3],a[184:187],a[192:195],a[0:3]\n"
W_"6)\n"
T_"a[178:179],v248 offset:4800\n"
M_"a[0:3],a[188:191],a[196:199],a[0:3]\n"
L_"v254,s[24:27],s62 offen lds\n"
M_"a[4:7],a[188:191],a[204:207],a[4:7]\n"
A_"m0,s60,91136\n"
T_"a[182:183],v248 offset:14016\n"
M_"a[4:7],a[184:187],a[200:203],a[4:7]\n"
S_"s62,2129920\n"
M_"a[8:11],a[184:187],a[208:211],a[8:11]\n"
M_"a[8:11],a[188:191],a[212:215],a[8:11]\n"
M_"a[12:15],a[188:191],a[220:223],a[12:15]\n"
T_"a[188:189],v248 offset:27840\n"
M_"a[12:15],a[184:187],a[216:219],a[12:15]\n"
W_"6)\n"
T_"a[184:185],v248 offset:18624\n"
M_"a[16:19],a[168:171],a[192:195],a[16:19]\n"
W_"6)\n"
T_"a[186:187],v248 offset:23232\n"
M_"a[16:19],a[172:175],a[196:199],a[16:19]\n"
L_"v254,s[24:27],s62 offen lds\n"
M_"a[20:23],a[172:175],a[204:207],a[20:23]\n"
A_"m0,s60,92160\n"
T_"a[190:191],v248 offset:32448\n"
M_"a[20:23],a[168:171],a[200:203],a[20:23]\n"
S_"s62,2162688\n"
M_"a[24:27],a[168:171],a[208:211],a[24:27]\n"
M_"a[24:27],a[172:175],a[212:215],a[24:27]\n"
M_"a[28:31],a[172:175],a[220:223],a[28:31]\n"
T_"a[172:173],v248 offset:9440\n"
M_"a[28:31],a[168:171],a[216:219],a[28:31]\n"
W_"6)\n"
T_"a[168:169],v248 offset:224\n"
M_"a[32:35],a[176:179],a[192:195],a[32:35]\n"
W_"6)\n"
T_"a[170:171],v248 offset:4832\n"
M_"a[32:35],a[180:183],a[196:199],a[32:35]\n"
L_"v254,s[24:27],s62 offen lds\n"
M_"a[36:39],a[180:183],a[204:207],a[36:39]\n"
A_"m0,s60,93184\n"
T_"a[174:175],v248 offset:14048\n"
M_"a[36:39],a[176:179],a[200:203],a[36:39]\n"
S_"s62,2195456\n"
M_"a[40:43],a[176:179],a[208:211],a[40:43]\n"
M_"a[40:43],a[180:183],a[212:215],a[40:43]\n"
M_"a[44:47],a[180:183],a[220:223],a[44:47]\n"
T_"a[180:181],v248 offset:27872\n"
M_"a[44:47],a[176:179],a[216:219],a[44:47]\n"
W_"6)\n"
T_"a[176:177],v248 offset:18656\n"
M_"a[48:51],a[184:187],a[192:195],a[48:51]\n"
W_"6)\n"
T_"a[178:179],v248 offset:23264\n"
M_"a[48:51],a[188:191],a[196:199],a[48:51]\n"
L_"v254,s[24:27],s62 offen lds\n"
M_"a[52:55],a[188:191],a[204:207],a[52:55]\n"
A_"m0,s34,53248\n"
T_"a[182:183],v248 offset:32480\n"
M_"a[52:55],a[184:187],a[200:203],a[52:55]\n"
S_"s62,2359296\n"
M_"a[56:59],a[184:187],a[208:211],a[56:59]\n"
M_"a[56:59],a[188:191],a[212:215],a[56:59]\n"
M_"a[60:63],a[188:191],a[220:223],a[60:63]\n"
T_"a[188:189],v248 offset:9472\n"
M_"a[60:63],a[184:187],a[216:219],a[60:63]\n"
W_"6)\n"
T_"a[184:185],v248 offset:256\n"
M_"a[64:67],a[168:171],a[192:195],a[64:67]\n"
W_"6)\n"
T_"a[186:187],v248 offset:4864\n"
M_"a[64:67],a[172:175],a[196:199],a[64:67]\n"
L_"v251,s[16:19],s62 offen lds\n"
M_"a[68:71],a[172:175],a[204:207],a[68:71]\n"
A_"m0,s34,54272\n"
T_"a[190:191],v248 offset:14080\n"
M_"a[68:71],a[168:171],a[200:203],a[68:71]\n"
S_"s62,2359296\n"
M_"a[72:75],a[168:171],a[208:211],a[72:75]\n"
M_"a[72:75],a[172:175],a[212:215],a[72:75]\n"
M_"a[76:79],a[172:175],a[220:223],a[76:79]\n"
T_"a[172:173],v248 offset:27904\n"
M_"a[76:79],a[168:171],a[216:219],a[76:79]\n"
W_"6)\n"
T_"a[168:169],v248 offset:18688\n"
M_"a[80:83],a[176:179],a[192:195],a[80:83]\n"
W_"6)\n"
T_"a[170:171],v248 offset:23296\n"
M_"a[80:83],a[180:183],a[196:199],a[80:83]\n"
L_"v252,s[16:19],s62 offen lds\n"
M_"a[84:87],a[180:183],a[204:207],a[84:87]\n"
A_"m0,s34,71680\n"
T_"a[174:175],v248 offset:32512\n"
M_"a[84:87],a[176:179],a[200:203],a[84:87]\n"
S_"s62,2359296\n"
"v_subrev_u32 v248,106496,v248\n"
M_"a[88:91],a[176:179],a[208:211],a[88:91]\n"
M_"a[88:91],a[180:183],a[212:215],a[88:91]\n"
M_"a[92:95],a[180:183],a[220:223],a[92:95]\n"
T_"a[180:181],v249 offset:45056\n"
M_"a[92:95],a[176:179],a[216:219],a[92:95]\n"
W_"6)\n"
T_"a[176:177],v249 offset:36864\n"
M_"a[96:99],a[184:187],a[192:195],a[96:99]\n"
W_"6)\n"
T_"a[178:179],v249 offset:40960\n"
M_"a[96:99],a[188:191],a[196:199],a[96:99]\n"
L_"v251,s[20:23],s62 offen lds\n"
M_"a[100:103],a[188:191],a[204:207],a[100:103]\n"
A_"m0,s34,72704\n"
T_"a[182:183],v249 offset:49152\n"
M_"a[100:103],a[184:187],a[200:203],a[100:103]\n"
S_"s62,2359296\n"
M_"a[104:107],a[184:187],a[208:211],a[104:107]\n"
M_"a[104:107],a[188:191],a[212:215],a[104:107]\n"
M_"a[108:111],a[188:191],a[220:223],a[108:111]\n"
T_"a[188:189],v249 offset:45064\n"
M_"a[108:111],a[184:187],a[216:219],a[108:111]\n"
W_"6)\n"
T_"a[184:185],v249 offset:36872\n"
M_"a[112:115],a[168:171],a[192:195],a[112:115]\n"
W_"6)\n"
T_"a[186:187],v249 offset:40968\n"
M_"a[112:115],a[172:175],a[196:199],a[112:115]\n"
L_"v252,s[20:23],s62 offen lds\n"
M_"a[116:119],a[172:175],a[204:207],a[116:119]\n"
A_"m0,s34,62464\n"
T_"a[190:191],v249 offset:49160\n"
M_"a[116:119],a[168:171],a[200:203],a[116:119]\n"
S_"s62,2654208\n"
M_"a[120:123],a[168:171],a[208:211],a[120:123]\n"
M_"a[120:123],a[172:175],a[212:215],a[120:123]\n"
M_"a[124:127],a[172:175],a[220:223],a[124:127]\n"
T_"a[172:173],v250 offset:45056\n"
M_"a[124:127],a[168:171],a[216:219],a[124:127]\n"
W_"6)\n"
T_"a[168:169],v250 offset:36864\n"
M_"v[0:3],a[176:179],a[192:195],v[0:3]\n"
W_"6)\n"
T_"a[170:171],v250 offset:40960\n"
M_"v[0:3],a[180:183],a[196:199],v[0:3]\n"
L_"v251,s[16:19],s62 offen lds\n"
M_"v[4:7],a[180:183],a[204:207],v[4:7]\n"
A_"m0,s34,63488\n"
T_"a[174:175],v250 offset:49152\n"
M_"v[4:7],a[176:179],a[200:203],v[4:7]\n"
S_"s62,2654208\n"
M_"v[8:11],a[176:179],a[208:211],v[8:11]\n"
M_"v[8:11],a[180:183],a[212:215],v[8:11]\n"
M_"v[12:15],a[180:183],a[220:223],v[12:15]\n"
T_"a[180:181],v250 offset:45064\n"
M_"v[12:15],a[176:179],a[216:219],v[12:15]\n"
W_"6)\n"
T_"a[176:177],v250 offset:36872\n"
M_"v[64:67],a[184:187],a[192:195],v[64:67]\n"
W_"6)\n"
T_"a[178:179],v250 offset:40968\n"
M_"v[64:67],a[188:191],a[196:199],v[64:67]\n"
L_"v252,s[16:19],s62 offen lds\n"
M_"v[68:71],a[188:191],a[204:207],v[68:71]\n"
A_"m0,s34,80896\n"
T_"a[182:183],v250 offset:49160\n"
M_"v[68:71],a[184:187],a[200:203],v[68:71]\n"
S_"s62,2654208\n"
"v_xor_b32 v250,128,v249\n"
M_"v[72:75],a[184:187],a[208:211],v[72:75]\n"
M_"v[72:75],a[188:191],a[212:215],v[72:75]\n"
M_"v[76:79],a[188:191],a[220:223],v[76:79]\n"
T_"a[188:189],v250 offset:45056\n"
M_"v[76:79],a[184:187],a[216:219],v[76:79]\n"
W_"6)\n"
T_"a[184:185],v250 offset:36864\n"
M_"v[16:19],a[168:171],a[192:195],v[16:19]\n"
W_"6)\n"
T_"a[186:187],v250 offset:40960\n"
M_"v[16:19],a[172:175],a[196:199],v[16:19]\n"
L_"v251,s[20:23],s62 offen lds\n"
M_"v[20:23],a[172:175],a[204:207],v[20:23]\n"
A_"m0,s34,81920\n"
T_"a[190:191],v250 offset:49152\n"
M_"v[20:23],a[168:171],a[200:203],v[20:23]\n"
S_"s62,2654208\n"
M_"v[24:27],a[168:171],a[208:211],v[24:27]\n"
M_"v[24:27],a[172:175],a[212:215],v[24:27]\n"
M_"v[28:31],a[172:175],a[220:223],v[28:31]\n"
T_"a[172:173],v250 offset:45064\n"
M_"v[28:31],a[168:171],a[216:219],v[28:31]\n"
W_"6)\n"
T_"a[168:169],v250 offset:36872\n"
M_"v[80:83],a[176:179],a[192:195],v[80:83]\n"
W_"6)\n"
T_"a[170:171],v250 offset:40968\n"
M_"v[80:83],a[180:183],a[196:199],v[80:83]\n"
L_"v252,s[20:23],s62 offen lds\n"
M_"v[84:87],a[180:183],a[204:207],v[84:87]\n"
A_"m0,s35,53248\n"
T_"a[174:175],v250 offset:49160\n"
M_"v[84:87],a[176:179],a[200:203],v[84:87]\n"
S_"s62,2359296\n"
"v_xor_b32 v250,192,v249\n"
"v_subrev_u32 v249,106496,v249\n"
M_"v[88:91],a[176:179],a[208:211],v[88:91]\n"
M_"v[88:91],a[180:183],a[212:215],v[88:91]\n"
M_"v[92:95],a[180:183],a[220:223],v[92:95]\n"
T_"a[180:181],v250 offset:45056\n"
M_"v[92:95],a[176:179],a[216:219],v[92:95]\n"
W_"6)\n"
T_"a[176:177],v250 offset:36864\n"
M_"v[32:35],a[184:187],a[192:195],v[32:35]\n"
W_"6)\n"
T_"a[178:179],v250 offset:40960\n"
M_"v[32:35],a[188:191],a[196:199],v[32:35]\n"
L_"v253,s[28:31],s62 offen lds\n"
M_"v[36:39],a[188:191],a[204:207],v[36:39]\n"
S_"m0,159744\n"
T_"a[182:183],v250 offset:49152\n"
M_"v[36:39],a[184:187],a[200:203],v[36:39]\n"
S_"s62,2621440\n"
M_"v[40:43],a[184:187],a[208:211],v[40:43]\n"
M_"v[40:43],a[188:191],a[212:215],v[40:43]\n"
M_"v[44:47],a[188:191],a[220:223],v[44:47]\n"
T_"a[188:189],v250 offset:45064\n"
M_"v[44:47],a[184:187],a[216:219],v[44:47]\n"
W_"6)\n"
T_"a[184:185],v250 offset:36872\n"
M_"v[96:99],a[168:171],a[192:195],v[96:99]\n"
W_"6)\n"
T_"a[186:187],v250 offset:40968\n"
M_"v[96:99],a[172:175],a[196:199],v[96:99]\n"
T_"a[190:191],v250 offset:49160\n"
M_"v[100:103],a[172:175],a[204:207],v[100:103]\n"
"v_xor_b32 v250,64,v249\n"
M_"v[100:103],a[168:171],a[200:203],v[100:103]\n"
M_"v[104:107],a[168:171],a[208:211],v[104:107]\n"
M_"v[104:107],a[172:175],a[212:215],v[104:107]\n"
M_"v[108:111],a[172:175],a[220:223],v[108:111]\n"
M_"v[108:111],a[168:171],a[216:219],v[108:111]\n"
W_"5)\n"
M_"v[48:51],a[176:179],a[192:195],v[48:51]\n"
W_"4)\n"
M_"v[48:51],a[180:183],a[196:199],v[48:51]\n"
M_"v[52:55],a[180:183],a[204:207],v[52:55]\n"
"s_waitcnt vmcnt(22) lgkmcnt(0)\n"
"s_barrier\n"
T_"a[168:169],v248 offset:0\n"
M_"v[52:55],a[176:179],a[200:203],v[52:55]\n"
T_"a[170:171],v248 offset:4608\n"
M_"v[56:59],a[176:179],a[208:211],v[56:59]\n"
T_"a[172:173],v248 offset:9216\n"
M_"v[56:59],a[180:183],a[212:215],v[56:59]\n"
T_"a[174:175],v248 offset:13824\n"
M_"v[60:63],a[180:183],a[220:223],v[60:63]\n"
T_"a[180:181],v248 offset:27648\n"
M_"v[60:63],a[176:179],a[216:219],v[60:63]\n"
T_"a[176:177],v248 offset:18432\n"
M_"v[112:115],a[184:187],a[192:195],v[112:115]\n"
T_"a[178:179],v248 offset:23040\n"
M_"v[112:115],a[188:191],a[196:199],v[112:115]\n"
T_"a[182:183],v248 offset:32256\n"
M_"v[116:119],a[188:191],a[204:207],v[116:119]\n"
M_"v[116:119],a[184:187],a[200:203],v[116:119]\n"
M_"v[120:123],a[184:187],a[208:211],v[120:123]\n"
M_"v[120:123],a[188:191],a[212:215],v[120:123]\n"
M_"v[124:127],a[188:191],a[220:223],v[124:127]\n"
T_"a[188:189],v248 offset:9248\n"
M_"v[124:127],a[184:187],a[216:219],v[124:127]\n"
"s_waitcnt vmcnt(21) lgkmcnt(7)\n"
T_"a[184:185],v248 offset:32\n"
M_"v[128:131],a[168:171],a[224:227],v[128:131]\n"
"s_waitcnt vmcnt(20) lgkmcnt(6)\n"
L_"a[192:195],v255,s[36:39],s63 offen sc1\n"
M_"v[128:131],a[172:175],a[228:231],v[128:131]\n"
"s_waitcnt vmcnt(19)\n"
T_"a[186:187],v248 offset:4640\n"
M_"v[132:135],a[172:175],a[236:239],v[132:135]\n"
T_"a[190:191],v248 offset:13856\n"
M_"v[132:135],a[168:171],a[232:235],v[132:135]\n"
"s_waitcnt vmcnt(18)\n"
M_"v[136:139],a[168:171],a[240:243],v[136:139]\n"
"s_waitcnt vmcnt(17)\n"
M_"v[136:139],a[172:175],a[244:247],v[136:139]\n"
"s_waitcnt vmcnt(15)\n"
M_"v[140:143],a[172:175],a[252:255],v[140:143]\n"
T_"a[172:173],v248 offset:27680\n"
M_"v[140:143],a[168:171],a[248:251],v[140:143]\n"
W_"6)\n"
T_"a[168:169],v248 offset:18464\n"
M_"v[144:147],a[176:179],a[224:227],v[144:147]\n"
W_"6)\n"
T_"a[170:171],v248 offset:23072\n"
M_"v[144:147],a[180:183],a[228:231],v[144:147]\n"
L_"a[196:199],v255,s[36:39],s63 offen offset:1024 sc1\n"
M_"v[148:151],a[180:183],a[236:239],v[148:151]\n"
S_"s63,26624\n"
T_"a[174:175],v248 offset:32288\n"
M_"v[148:151],a[176:179],a[232:235],v[148:151]\n"
M_"v[152:155],a[176:179],a[240:243],v[152:155]\n"
M_"v[152:155],a[180:183],a[244:247],v[152:155]\n"
M_"v[156:159],a[180:183],a[252:255],v[156:159]\n"
T_"a[180:181],v248 offset:9280\n"
M_"v[156:159],a[176:179],a[248:251],v[156:159]\n"
W_"6)\n"
T_"a[176:177],v248 offset:64\n"
M_"v[160:163],a[184:187],a[224:227],v[160:163]\n"
W_"6)\n"
T_"a[178:179],v248 offset:4672\n"
M_"v[160:163],a[188:191],a[228:231],v[160:163]\n"
T_"a[182:183],v248 offset:13888\n"
M_"v[164:167],a[188:191],a[236:239],v[164:167]\n"
L_"a[200:203],v255,s[36:39],s63 offen sc1\n"
M_"v[164:167],a[184:187],a[232:235],v[164:167]\n"
M_"v[168:171],a[184:187],a[240:243],v[168:171]\n"
M_"v[168:171],a[188:191],a[244:247],v[168:171]\n"
M_"v[172:175],a[188:191],a[252:255],v[172:175]\n"
T_"a[188:189],v248 offset:27712\n"
M_"v[172:175],a[184:187],a[248:251],v[172:175]\n"
W_"6)\n"
T_"a[184:185],v248 offset:18496\n"
M_"v[176:179],a[168:171],a[224:227],v[176:179]\n"
W_"6)\n"
T_"a[186:187],v248 offset:23104\n"
M_"v[176:179],a[172:175],a[228:231],v[176:179]\n"
T_"a[190:191],v248 offset:32320\n"
M_"v[180:183],a[172:175],a[236:239],v[180:183]\n"
M_"v[180:183],a[168:171],a[232:235],v[180:183]\n"
L_"a[204:207],v255,s[36:39],s63 offen offset:1024 sc1\n"
M_"v[184:187],a[168:171],a[240:243],v[184:187]\n"
S_"s63,45056\n"
M_"v[184:187],a[172:175],a[244:247],v[184:187]\n"
M_"v[188:191],a[172:175],a[252:255],v[188:191]\n"
T_"a[172:173],v248 offset:9312\n"
M_"v[188:191],a[168:171],a[248:251],v[188:191]\n"
W_"6)\n"
T_"a[168:169],v248 offset:96\n"
M_"v[192:195],a[176:179],a[224:227],v[192:195]\n"
W_"6)\n"
T_"a[170:171],v248 offset:4704\n"
M_"v[192:195],a[180:183],a[228:231],v[192:195]\n"
T_"a[174:175],v248 offset:13920\n"
M_"v[196:199],a[180:183],a[236:239],v[196:199]\n"
M_"v[196:199],a[176:179],a[232:235],v[196:199]\n"
M_"v[200:203],a[176:179],a[240:243],v[200:203]\n"
L_"a[208:211],v255,s[36:39],s63 offen sc1\n"
M_"v[200:203],a[180:183],a[244:247],v[200:203]\n"
M_"v[204:207],a[180:183],a[252:255],v[204:207]\n"
T_"a[180:181],v248 offset:27744\n"
M_"v[204:207],a[176:179],a[248:251],v[204:207]\n"
W_"6)\n"
T_"a[176:177],v248 offset:18528\n"
M_"v[208:211],a[184:187],a[224:227],v[208:211]\n"
W_"6)\n"
T_"a[178:179],v248 offset:23136\n"
M_"v[208:211],a[188:191],a[228:231],v[208:211]\n"
T_"a[182:183],v248 offset:32352\n"
M_"v[212:215],a[188:191],a[236:239],v[212:215]\n"
M_"v[212:215],a[184:187],a[232:235],v[212:215]\n"
M_"v[216:219],a[184:187],a[240:243],v[216:219]\n"
M_"v[216:219],a[188:191],a[244:247],v[216:219]\n"
L_"a[212:215],v255,s[36:39],s63 offen offset:1024 sc1\n"
M_"v[220:223],a[188:191],a[252:255],v[220:223]\n"
S_"s63,63488\n"
T_"a[188:189],v248 offset:9344\n"
M_"v[220:223],a[184:187],a[248:251],v[220:223]\n"
W_"6)\n"
T_"a[184:185],v248 offset:128\n"
M_"v[224:227],a[168:171],a[224:227],v[224:227]\n"
W_"6)\n"
T_"a[186:187],v248 offset:4736\n"
M_"v[224:227],a[172:175],a[228:231],v[224:227]\n"
T_"a[190:191],v248 offset:13952\n"
M_"v[228:231],a[172:175],a[236:239],v[228:231]\n"
M_"v[228:231],a[168:171],a[232:235],v[228:231]\n"
M_"v[232:235],a[168:171],a[240:243],v[232:235]\n"
M_"v[232:235],a[172:175],a[244:247],v[232:235]\n"
M_"v[236:239],a[172:175],a[252:255],v[236:239]\n"
L_"a[216:219],v255,s[36:39],s63 offen sc1\n"
M_"v[236:239],a[168:171],a[248:251],v[236:239]\n"
W_"5)\n"
T_"a[168:169],v248 offset:18560\n"
M_"v[240:243],a[176:179],a[224:227],v[240:243]\n"
W_"5)\n"
T_"a[170:171],v248 offset:23168\n"
M_"v[240:243],a[180:183],a[228:231],v[240:243]\n"
T_"a[172:173],v248 offset:27776\n"
M_"v[244:247],a[180:183],a[236:239],v[244:247]\n"
T_"a[174:175],v248 offset:32384\n"
M_"v[244:247],a[176:179],a[232:235],v[244:247]\n"
M_"a[128:131],a[176:179],a[240:243],a[128:131]\n"
M_"a[128:131],a[180:183],a[244:247],a[128:131]\n"
M_"a[132:135],a[180:183],a[252:255],a[132:135]\n"
T_"a[180:181],v248 offset:9376\n"
M_"a[132:135],a[176:179],a[248:251],a[132:135]\n"
W_"6)\n"
L_"a[220:223],v255,s[36:39],s63 offen offset:1024 sc1\n"
M_"a[136:139],a[184:187],a[224:227],a[136:139]\n"
W_"5)\n"
T_"a[176:177],v248 offset:160\n"
M_"a[136:139],a[188:191],a[228:231],a[136:139]\n"
S_"s63,10240\n"
T_"a[178:179],v248 offset:4768\n"
M_"a[140:143],a[188:191],a[236:239],a[140:143]\n"
T_"a[182:183],v248 offset:13984\n"
M_"a[140:143],a[184:187],a[232:235],a[140:143]\n"
M_"a[144:147],a[184:187],a[240:243],a[144:147]\n"
M_"a[144:147],a[188:191],a[244:247],a[144:147]\n"
M_"a[148:151],a[188:191],a[252:255],a[148:151]\n"
T_"a[188:189],v248 offset:27808\n"
M_"a[148:151],a[184:187],a[248:251],a[148:151]\n"
W_"7)\n"
T_"a[184:185],v248 offset:18592\n"
M_"a[152:155],a[168:171],a[224:227],a[152:155]\n"
W_"6)\n"
L_"v255,s[52:55],s77 offen offset:1024 lds\n"
M_"a[152:155],a[172:175],a[228:231],a[152:155]\n"
A_"m0,s60,143360\n"
T_"a[186:187],v248 offset:23200\n"
M_"a[156:159],a[172:175],a[236:239],a[156:159]\n"
T_"a[190:191],v248 offset:32416\n"
M_"a[156:159],a[168:171],a[232:235],a[156:159]\n"
M_"a[160:163],a[168:171],a[240:243],a[160:163]\n"
M_"a[160:163],a[172:175],a[244:247],a[160:163]\n"
M_"a[164:167],a[172:175],a[252:255],a[164:167]\n"
T_"a[172:173],v248 offset:9408\n"
M_"a[164:167],a[168:171],a[248:251],a[164:167]\n"
W_"6)\n"
T_"a[168:169],v248 offset:192\n"
M_"a[0:3],a[176:179],a[224:227],a[0:3]\n"
W_"6)\n"
T_"a[170:171],v248 offset:4800\n"
M_"a[0:3],a[180:183],a[228:231],a[0:3]\n"
L_"v254,s[24:27],s62 offen lds\n"
M_"a[4:7],a[180:183],a[236:239],a[4:7]\n"
A_"m0,s60,144384\n"
T_"a[174:175],v248 offset:14016\n"
M_"a[4:7],a[176:179],a[232:235],a[4:7]\n"
S_"s62,2654208\n"
M_"a[8:11],a[176:179],a[240:243],a[8:11]\n"
M_"a[8:11],a[180:183],a[244:247],a[8:11]\n"
M_"a[12:15],a[180:183],a[252:255],a[12:15]\n"
T_"a[180:181],v248 offset:27840\n"
M_"a[12:15],a[176:179],a[248:251],a[12:15]\n"
W_"6)\n"
T_"a[176:177],v248 offset:18624\n"
M_"a[16:19],a[184:187],a[224:227],a[16:19]\n"
W_"6)\n"
T_"a[178:179],v248 offset:23232\n"
M_"a[16:19],a[188:191],a[228:231],a[16:19]\n"
L_"v254,s[24:27],s62 offen lds\n"
M_"a[20:23],a[188:191],a[236:239],a[20:23]\n"
A_"m0,s60,145408\n"
T_"a[182:183],v248 offset:32448\n"
M_"a[20:23],a[184:187],a[232:235],a[20:23]\n"
S_"s62,2686976\n"
M_"a[24:27],a[184:187],a[240:243],a[24:27]\n"
M_"a[24:27],a[188:191],a[244:247],a[24:27]\n"
M_"a[28:31],a[188:191],a[252:255],a[28:31]\n"
T_"a[188:189],v248 offset:9440\n"
M_"a[28:31],a[184:187],a[248:251],a[28:31]\n"
W_"6)\n"
T_"a[184:185],v248 offset:224\n"
M_"a[32:35],a[168:171],a[224:227],a[32:35]\n"
W_"6)\n"
T_"a[186:187],v248 offset:4832\n"
M_"a[32:35],a[172:175],a[228:231],a[32:35]\n"
L_"v254,s[24:27],s62 offen lds\n"
M_"a[36:39],a[172:175],a[236:239],a[36:39]\n"
A_"m0,s60,146432\n"
T_"a[190:191],v248 offset:14048\n"
M_"a[36:39],a[168:171],a[232:235],a[36:39]\n"
S_"s62,2719744\n"
M_"a[40:43],a[168:171],a[240:243],a[40:43]\n"
M_"a[40:43],a[172:175],a[244:247],a[40:43]\n"
M_"a[44:47],a[172:175],a[252:255],a[44:47]\n"
T_"a[172:173],v248 offset:27872\n"
M_"a[44:47],a[168:171],a[248:251],a[44:47]\n"
W_"6)\n"
T_"a[168:169],v248 offset:18656\n"
M_"a[48:51],a[176:179],a[224:227],a[48:51]\n"
W_"6)\n"
T_"a[170:171],v248 offset:23264\n"
M_"a[48:51],a[180:183],a[228:231],a[48:51]\n"
L_"v254,s[24:27],s62 offen lds\n"
M_"a[52:55],a[180:183],a[236:239],a[52:55]\n"
A_"m0,s34,106496\n"
T_"a[174:175],v248 offset:32480\n"
M_"a[52:55],a[176:179],a[232:235],a[52:55]\n"
S_"s62,2949120\n"
M_"a[56:59],a[176:179],a[240:243],a[56:59]\n"
M_"a[56:59],a[180:183],a[244:247],a[56:59]\n"
M_"a[60:63],a[180:183],a[252:255],a[60:63]\n"
T_"a[180:181],v248 offset:9472\n"
M_"a[60:63],a[176:179],a[248:251],a[60:63]\n"
W_"6)\n"
T_"a[176:177],v248 offset:256\n"
M_"a[64:67],a[184:187],a[224:227],a[64:67]\n"
W_"6)\n"
T_"a[178:179],v248 offset:4864\n"
M_"a[64:67],a[188:191],a[228:231],a[64:67]\n"
L_"v251,s[16:19],s62 offen lds\n"
M_"a[68:71],a[188:191],a[236:239],a[68:71]\n"
A_"m0,s34,107520\n"
T_"a[182:183],v248 offset:14080\n"
M_"a[68:71],a[184:187],a[232:235],a[68:71]\n"
S_"s62,2949120\n"
M_"a[72:75],a[184:187],a[240:243],a[72:75]\n"
M_"a[72:75],a[188:191],a[244:247],a[72:75]\n"
M_"a[76:79],a[188:191],a[252:255],a[76:79]\n"
T_"a[188:189],v248 offset:27904\n"
M_"a[76:79],a[184:187],a[248:251],a[76:79]\n"
W_"6)\n"
T_"a[184:185],v248 offset:18688\n"
M_"a[80:83],a[168:171],a[224:227],a[80:83]\n"
W_"6)\n"
T_"a[186:187],v248 offset:23296\n"
M_"a[80:83],a[172:175],a[228:231],a[80:83]\n"
L_"v252,s[16:19],s62 offen lds\n"
M_"a[84:87],a[172:175],a[236:239],a[84:87]\n"
A_"m0,s34,124928\n"
T_"a[190:191],v248 offset:32512\n"
M_"a[84:87],a[168:171],a[232:235],a[84:87]\n"
S_"s62,2949120\n"
"v_add_u32 v248,53248,v248\n"
M_"a[88:91],a[168:171],a[240:243],a[88:91]\n"
M_"a[88:91],a[172:175],a[244:247],a[88:91]\n"
M_"a[92:95],a[172:175],a[252:255],a[92:95]\n"
T_"a[172:173],v249 offset:45056\n"
M_"a[92:95],a[168:171],a[248:251],a[92:95]\n"
W_"6)\n"
T_"a[168:169],v249 offset:36864\n"
M_"a[96:99],a[176:179],a[224:227],a[96:99]\n"
W_"6)\n"
T_"a[170:171],v249 offset:40960\n"
M_"a[96:99],a[180:183],a[228:231],a[96:99]\n"
L_"v251,s[20:23],s62 offen lds\n"
M_"a[100:103],a[180:183],a[236:239],a[100:103]\n"
A_"m0,s34,125952\n"
T_"a[174:175],v249 offset:49152\n"
M_"a[100:103],a[176:179],a[232:235],a[100:103]\n"
S_"s62,2949120\n"
M_"a[104:107],a[176:179],a[240:243],a[104:107]\n"
M_"a[104:107],a[180:183],a[244:247],a[104:107]\n"
M_"a[108:111],a[180:183],a[252:255],a[108:111]\n"
T_"a[180:181],v249 offset:45064\n"
M_"a[108:111],a[176:179],a[248:251],a[108:111]\n"
W_"6)\n"
T_"a[176:177],v249 offset:36872\n"
M_"a[112:115],a[184:187],a[224:227],a[112:115]\n"
W_"6)\n"
T_"a[178:179],v249 offset:40968\n"
M_"a[112:115],a[188:191],a[228:231],a[112:115]\n"
L_"v252,s[20:23],s62 offen lds\n"
M_"a[116:119],a[188:191],a[236:239],a[116:119]\n"
A_"m0,s34,115712\n"
T_"a[182:183],v249 offset:49160\n"
M_"a[116:119],a[184:187],a[232:235],a[116:119]\n"
S_"s62,3244032\n"
M_"a[120:123],a[184:187],a[240:243],a[120:123]\n"
M_"a[120:123],a[188:191],a[244:247],a[120:123]\n"
M_"a[124:127],a[188:191],a[252:255],a[124:127]\n"
T_"a[188:189],v250 offset:45056\n"
M_"a[124:127],a[184:187],a[248:251],a[124:127]\n"
W_"6)\n"
T_"a[184:185],v250 offset:36864\n"
M_"v[0:3],a[168:171],a[224:227],v[0:3]\n"
W_"6)\n"
T_"a[186:187],v250 offset:40960\n"
M_"v[0:3],a[172:175],a[228:231],v[0:3]\n"
L_"v251,s[16:19],s62 offen lds\n"
M_"v[4:7],a[172:175],a[236:239],v[4:7]\n"
A_"m0,s34,116736\n"
T_"a[190:191],v250 offset:49152\n"
M_"v[4:7],a[168:171],a[232:235],v[4:7]\n"
S_"s62,3244032\n"
M_"v[8:11],a[168:171],a[240:243],v[8:11]\n"
M_"v[8:11],a[172:175],a[244:247],v[8:11]\n"
M_"v[12:15],a[172:175],a[252:255],v[12:15]\n"
T_"a[172:173],v250 offset:45064\n"
M_"v[12:15],a[168:171],a[248:251],v[12:15]\n"
W_"6)\n"
T_"a[168:169],v250 offset:36872\n"
M_"v[64:67],a[176:179],a[224:227],v[64:67]\n"
W_"6)\n"
T_"a[170:171],v250 offset:40968\n"
M_"v[64:67],a[180:183],a[228:231],v[64:67]\n"
L_"v252,s[16:19],s62 offen lds\n"
M_"v[68:71],a[180:183],a[236:239],v[68:71]\n"
A_"m0,s34,134144\n"
T_"a[174:175],v250 offset:49160\n"
M_"v[68:71],a[176:179],a[232:235],v[68:71]\n"
S_"s62,3244032\n"
"v_xor_b32 v250,128,v249\n"
M_"v[72:75],a[176:179],a[240:243],v[72:75]\n"
M_"v[72:75],a[180:183],a[244:247],v[72:75]\n"
M_"v[76:79],a[180:183],a[252:255],v[76:79]\n"
T_"a[180:181],v250 offset:45056\n"
M_"v[76:79],a[176:179],a[248:251],v[76:79]\n"
W_"6)\n"
T_"a[176:177],v250 offset:36864\n"
M_"v[16:19],a[184:187],a[224:227],v[16:19]\n"
W_"6)\n"
T_"a[178:179],v250 offset:40960\n"
M_"v[16:19],a[188:191],a[228:231],v[16:19]\n"
L_"v251,s[20:23],s62 offen lds\n"
M_"v[20:23],a[188:191],a[236:239],v[20:23]\n"
A_"m0,s34,135168\n"
T_"a[182:183],v250 offset:49152\n"
M_"v[20:23],a[184:187],a[232:235],v[20:23]\n"
S_"s62,3244032\n"
M_"v[24:27],a[184:187],a[240:243],v[24:27]\n"
M_"v[24:27],a[188:191],a[244:247],v[24:27]\n"
M_"v[28:31],a[188:191],a[252:255],v[28:31]\n"
T_"a[188:189],v250 offset:45064\n"
M_"v[28:31],a[184:187],a[248:251],v[28:31]\n"
W_"6)\n"
T_"a[184:185],v250 offset:36872\n"
M_"v[80:83],a[168:171],a[224:227],v[80:83]\n"
W_"6)\n"
T_"a[186:187],v250 offset:40968\n"
M_"v[80:83],a[172:175],a[228:231],v[80:83]\n"
L_"v252,s[20:23],s62 offen lds\n"
M_"v[84:87],a[172:175],a[236:239],v[84:87]\n"
A_"m0,s35,106496\n"
T_"a[190:191],v250 offset:49160\n"
M_"v[84:87],a[168:171],a[232:235],v[84:87]\n"
S_"s62,2949120\n"
"v_xor_b32 v250,192,v249\n"
"v_add_u32 v249,53248,v249\n"
M_"v[88:91],a[168:171],a[240:243],v[88:91]\n"
M_"v[88:91],a[172:175],a[244:247],v[88:91]\n"
M_"v[92:95],a[172:175],a[252:255],v[92:95]\n"
T_"a[172:173],v250 offset:45056\n"
M_"v[92:95],a[168:171],a[248:251],v[92:95]\n"
W_"6)\n"
T_"a[168:169],v250 offset:36864\n"
M_"v[32:35],a[176:179],a[224:227],v[32:35]\n"
W_"6)\n"
T_"a[170:171],v250 offset:40960\n"
M_"v[32:35],a[180:183],a[228:231],v[32:35]\n"
L_"v253,s[28:31],s62 offen lds\n"
M_"v[36:39],a[180:183],a[236:239],v[36:39]\n"
S_"m0,159744\n"
T_"a[174:175],v250 offset:49152\n"
M_"v[36:39],a[176:179],a[232:235],v[36:39]\n"
S_"s62,3145728\n"
M_"v[40:43],a[176:179],a[240:243],v[40:43]\n"
M_"v[40:43],a[180:183],a[244:247],v[40:43]\n"
M_"v[44:47],a[180:183],a[252:255],v[44:47]\n"
T_"a[180:181],v250 offset:45064\n"
M_"v[44:47],a[176:179],a[248:251],v[44:47]\n"
W_"6)\n"
T_"a[176:177],v250 offset:36872\n"
M_"v[96:99],a[184:187],a[224:227],v[96:99]\n"
W_"6)\n"
T_"a[178:179],v250 offset:40968\n"
M_"v[96:99],a[188:191],a[228:231],v[96:99]\n"
T_"a[182:183],v250 offset:49160\n"
M_"v[100:103],a[188:191],a[236:239],v[100:103]\n"
"v_xor_b32 v250,64,v249\n"
M_"v[100:103],a[184:187],a[232:235],v[100:103]\n"
M_"v[104:107],a[184:187],a[240:243],v[104:107]\n"
M_"v[104:107],a[188:191],a[244:247],v[104:107]\n"
M_"v[108:111],a[188:191],a[252:255],v[108:111]\n"
M_"v[108:111],a[184:187],a[248:251],v[108:111]\n"
W_"5)\n"
M_"v[48:51],a[168:171],a[224:227],v[48:51]\n"
W_"4)\n"
M_"v[48:51],a[172:175],a[228:231],v[48:51]\n"
M_"v[52:55],a[172:175],a[236:239],v[52:55]\n"
"s_waitcnt vmcnt(22) lgkmcnt(0)\n"
"s_barrier\n"
T_"a[184:185],v248 offset:0\n"
M_"v[52:55],a[168:171],a[232:235],v[52:55]\n"
T_"a[186:187],v248 offset:4608\n"
M_"v[56:59],a[168:171],a[240:243],v[56:59]\n"
T_"a[188:189],v248 offset:9216\n"
M_"v[56:59],a[172:175],a[244:247],v[56:59]\n"
T_"a[190:191],v248 offset:13824\n"
M_"v[60:63],a[172:175],a[252:255],v[60:63]\n"
T_"a[172:173],v248 offset:27648\n"
M_"v[60:63],a[168:171],a[248:251],v[60:63]\n"
T_"a[168:169],v248 offset:18432\n"
M_"v[112:115],a[176:179],a[224:227],v[112:115]\n"
T_"a[170:171],v248 offset:23040\n"
M_"v[112:115],a[180:183],a[228:231],v[112:115]\n"
T_"a[174:175],v248 offset:32256\n"
M_"v[116:119],a[180:183],a[236:239],v[116:119]\n"
M_"v[116:119],a[176:179],a[232:235],v[116:119]\n"
M_"v[120:123],a[176:179],a[240:243],v[120:123]\n"
M_"v[120:123],a[180:183],a[244:247],v[120:123]\n"
M_"v[124:127],a[180:183],a[252:255],v[124:127]\n"
T_"a[180:181],v248 offset:9248\n"
M_"v[124:127],a[176:179],a[248:251],v[124:127]\n"
"s_waitcnt vmcnt(21) lgkmcnt(7)\n"
T_"a[176:177],v248 offset:32\n"
M_"v[128:131],a[184:187],a[192:195],v[128:131]\n"
"s_waitcnt vmcnt(20) lgkmcnt(6)\n"
L_"a[224:227],v255,s[36:39],s63 offen sc1\n"
M_"v[128:131],a[188:191],a[196:199],v[128:131]\n"
"s_waitcnt vmcnt(19)\n"
T_"a[178:179],v248 offset:4640\n"
M_"v[132:135],a[188:191],a[204:207],v[132:135]\n"
T_"a[182:183],v248 offset:13856\n"
M_"v[132:135],a[184:187],a[200:203],v[132:135]\n"
"s_waitcnt vmcnt(18)\n"
M_"v[136:139],a[184:187],a[208:211],v[136:139]\n"
"s_waitcnt vmcnt(17)\n"
M_"v[136:139],a[188:191],a[212:215],v[136:139]\n"
"s_waitcnt vmcnt(15)\n"
M_"v[140:143],a[188:191],a[220:223],v[140:143]\n"
T_"a[188:189],v248 offset:27680\n"
M_"v[140:143],a[184:187],a[216:219],v[140:143]\n"
W_"6)\n"
T_"a[184:185],v248 offset:18464\n"
M_"v[144:147],a[168:171],a[192:195],v[144:147]\n"
W_"6)\n"
T_"a[186:187],v248 offset:23072\n"
M_"v[144:147],a[172:175],a[196:199],v[144:147]\n"
L_"a[228:231],v255,s[36:39],s63 offen offset:1024 sc1\n"
M_"v[148:151],a[172:175],a[204:207],v[148:151]\n"
S_"s63,28672\n"
T_"a[190:191],v248 offset:32288\n"
M_"v[148:151],a[168:171],a[200:203],v[148:151]\n"
M_"v[152:155],a[168:171],a[208:211],v[152:155]\n"
M_"v[152:155],a[172:175],a[212:215],v[152:155]\n"
M_"v[156:159],a[172:175],a[220:223],v[156:159]\n"
T_"a[172:173],v248 offset:9280\n"
M_"v[156:159],a[168:171],a[216:219],v[156:159]\n"
W_"6)\n"
T_"a[168:169],v248 offset:64\n"
M_"v[160:163],a[176:179],a[192:195],v[160:163]\n"
W_"6)\n"
T_"a[170:171],v248 offset:4672\n"
M_"v[160:163],a[180:183],a[196:199],v[160:163]\n"
T_"a[174:175],v248 offset:13888\n"
M_"v[164:167],a[180:183],a[204:207],v[164:167]\n"
L_"a[232:235],v255,s[36:39],s63 offen sc1\n"
M_"v[164:167],a[176:179],a[200:203],v[164:167]\n"
M_"v[168:171],a[176:179],a[208:211],v[168:171]\n"
M_"v[168:171],a[180:183],a[212:215],v[168:171]\n"
M_"v[172:175],a[180:183],a[220:223],v[172:175]\n"
T_"a[180:181],v248 offset:27712\n"
M_"v[172:175],a[176:179],a[216:219],v[172:175]\n"
W_"6)\n"
T_"a[176:177],v248 offset:18496\n"
M_"v[176:179],a[184:187],a[192:195],v[176:179]\n"
W_"6)\n"
T_"a[178:179],v248 offset:23104\n"
M_"v[176:179],a[188:191],a[196:199],v[176:179]\n"
T_"a[182:183],v248 offset:32320\n"
M_"v[180:183],a[188:191],a[204:207],v[180:183]\n"
M_"v[180:183],a[184:187],a[200:203],v[180:183]\n"
L_"a[236:239],v255,s[36:39],s63 offen offset:1024 sc1\n"
M_"v[184:187],a[184:187],a[208:211],v[184:187]\n"
S_"s63,47104\n"
M_"v[184:187],a[188:191],a[212:215],v[184:187]\n"
M_"v[188:191],a[188:191],a[220:223],v[188:191]\n"
T_"a[188:189],v248 offset:9312\n"
M_"v[188:191],a[184:187],a[216:219],v[188:191]\n"
W_"6)\n"
T_"a[184:185],v248 offset:96\n"
M_"v[192:195],a[168:171],a[192:195],v[192:195]\n"
W_"6)\n"
T_"a[186:187],v248 offset:4704\n"
M_"v[192:195],a[172:175],a[196:199],v[192:195]\n"
T_"a[190:191],v248 offset:13920\n"
M_"v[196:199],a[172:175],a[204:207],v[196:199]\n"
M_"v[196:199],a[168:171],a[200:203],v[196:199]\n"
M_"v[200:203],a[168:171],a[208:211],v[200:203]\n"
L_"a[240:243],v255,s[36:39],s63 offen sc1\n"
M_"v[200:203],a[172:175],a[212:215],v[200:203]\n"
M_"v[204:207],a[172:175],a[220:223],v[204:207]\n"
T_"a[172:173],v248 offset:27744\n"
M_"v[204:207],a[168:171],a[216:219],v[204:207]\n"
W_"6)\n"
T_"a[168:169],v248 offset:18528\n"
M_"v[208:211],a[176:179],a[192:195],v[208:211]\n"
W_"6)\n"
T_"a[170:171],v248 offset:23136\n"
M_"v[208:211],a[180:183],a[196:199],v[208:211]\n"
T_"a[174:175],v248 offset:32352\n"
M_"v[212:215],a[180:183],a[204:207],v[212:215]\n"
M_"v[212:215],a[176:179],a[200:203],v[212:215]\n"
M_"v[216:219],a[176:179],a[208:211],v[216:219]\n"
M_"v[216:219],a[180:183],a[212:215],v[216:219]\n"
L_"a[244:247],v255,s[36:39],s63 offen offset:1024 sc1\n"
M_"v[220:223],a[180:183],a[220:223],v[220:223]\n"
S_"s63,65536\n"
T_"a[180:181],v248 offset:9344\n"
M_"v[220:223],a[176:179],a[216:219],v[220:223]\n"
W_"6)\n"
T_"a[176:177],v248 offset:128\n"
M_"v[224:227],a[184:187],a[192:195],v[224:227]\n"
W_"6)\n"
T_"a[178:179],v248 offset:4736\n"
M_"v[224:227],a[188:191],a[196:199],v[224:227]\n"
T_"a[182:183],v248 offset:13952\n"
M_"v[228:231],a[188:191],a[204:207],v[228:231]\n"
M_"v[228:231],a[184:187],a[200:203],v[228:231]\n"
M_"v[232:235],a[184:187],a[208:211],v[232:235]\n"
M_"v[232:235],a[188:191],a[212:215],v[232:235]\n"
M_"v[236:239],a[188:191],a[220:223],v[236:239]\n"
L_"a[248:251],v255,s[36:39],s63 offen sc1\n"
M_"v[236:239],a[184:187],a[216:219],v[236:239]\n"
W_"5)\n"
T_"a[184:185],v248 offset:18560\n"
M_"v[240:243],a[168:171],a[192:195],v[240:243]\n"
W_"5)\n"
T_"a[186:187],v248 offset:23168\n"
M_"v[240:243],a[172:175],a[196:199],v[240:243]\n"
T_"a[188:189],v248 offset:27776\n"
M_"v[244:247],a[172:175],a[204:207],v[244:247]\n"
T_"a[190:191],v248 offset:32384\n"
M_"v[244:247],a[168:171],a[200:203],v[244:247]\n"
M_"a[128:131],a[168:171],a[208:211],a[128:131]\n"
M_"a[128:131],a[172:175],a[212:215],a[128:131]\n"
M_"a[132:135],a[172:175],a[220:223],a[132:135]\n"
T_"a[172:173],v248 offset:9376\n"
M_"a[132:135],a[168:171],a[216:219],a[132:135]\n"
W_"6)\n"
L_"a[252:255],v255,s[36:39],s63 offen offset:1024 sc1\n"
M_"a[136:139],a[176:179],a[192:195],a[136:139]\n"
W_"5)\n"
T_"a[168:169],v248 offset:160\n"
M_"a[136:139],a[180:183],a[196:199],a[136:139]\n"
S_"s63,12288\n"
T_"a[170:171],v248 offset:4768\n"
M_"a[140:143],a[180:183],a[204:207],a[140:143]\n"
T_"a[174:175],v248 offset:13984\n"
M_"a[140:143],a[176:179],a[200:203],a[140:143]\n"
M_"a[144:147],a[176:179],a[208:211],a[144:147]\n"
M_"a[144:147],a[180:183],a[212:215],a[144:147]\n"
M_"a[148:151],a[180:183],a[220:223],a[148:151]\n"
T_"a[180:181],v248 offset:27808\n"
M_"a[148:151],a[176:179],a[216:219],a[148:151]\n"
W_"7)\n"
T_"a[176:177],v248 offset:18592\n"
M_"a[152:155],a[184:187],a[192:195],a[152:155]\n"
W_"6)\n"
L_"v255,s[52:55],s77 offen offset:2048 lds\n"
M_"a[152:155],a[188:191],a[196:199],a[152:155]\n"
A_"m0,s60,36864\n"
T_"a[178:179],v248 offset:23200\n"
M_"a[156:159],a[188:191],a[204:207],a[156:159]\n"
T_"a[182:183],v248 offset:32416\n"
M_"a[156:159],a[184:187],a[200:203],a[156:159]\n"
M_"a[160:163],a[184:187],a[208:211],a[160:163]\n"
M_"a[160:163],a[188:191],a[212:215],a[160:163]\n"
M_"a[164:167],a[188:191],a[220:223],a[164:167]\n"
T_"a[188:189],v248 offset:9408\n"
M_"a[164:167],a[184:187],a[216:219],a[164:167]\n"
W_"6)\n"
T_"a[184:185],v248 offset:192\n"
M_"a[0:3],a[168:171],a[192:195],a[0:3]\n"
W_"6)\n"
T_"a[186:187],v248 offset:4800\n"
M_"a[0:3],a[172:175],a[196:199],a[0:3]\n"
L_"v254,s[24:27],s62 offen lds\n"
M_"a[4:7],a[172:175],a[204:207],a[4:7]\n"
A_"m0,s60,37888\n"
T_"a[190:191],v248 offset:14016\n"
M_"a[4:7],a[168:171],a[200:203],a[4:7]\n"
S_"s62,3178496\n"
M_"a[8:11],a[168:171],a[208:211],a[8:11]\n"
M_"a[8:11],a[172:175],a[212:215],a[8:11]\n"
M_"a[12:15],a[172:175],a[220:223],a[12:15]\n"
T_"a[172:173],v248 offset:27840\n"
M_"a[12:15],a[168:171],a[216:219],a[12:15]\n"
W_"6)\n"
T_"a[168:169],v248 offset:18624\n"
M_"a[16:19],a[176:179],a[192:195],a[16:19]\n"
W_"6)\n"
T_"a[170:171],v248 offset:23232\n"
M_"a[16:19],a[180:183],a[196:199],a[16:19]\n"
L_"v254,s[24:27],s62 offen lds\n"
M_"a[20:23],a[180:183],a[204:207],a[20:23]\n"
A_"m0,s60,38912\n"
T_"a[174:175],v248 offset:32448\n"
M_"a[20:23],a[176:179],a[200:203],a[20:23]\n"
S_"s62,3211264\n"
M_"a[24:27],a[176:179],a[208:211],a[24:27]\n"
M_"a[24:27],a[180:183],a[212:215],a[24:27]\n"
M_"a[28:31],a[180:183],a[220:223],a[28:31]\n"
T_"a[180:181],v248 offset:9440\n"
M_"a[28:31],a[176:179],a[216:219],a[28:31]\n"
W_"6)\n"
T_"a[176:177],v248 offset:224\n"
M_"a[32:35],a[184:187],a[192:195],a[32:35]\n"
W_"6)\n"
T_"a[178:179],v248 offset:4832\n"
M_"a[32:35],a[188:191],a[196:199],a[32:35]\n"
L_"v254,s[24:27],s62 offen lds\n"
M_"a[36:39],a[188:191],a[204:207],a[36:39]\n"
A_"m0,s60,39936\n"
T_"a[182:183],v248 offset:14048\n"
M_"a[36:39],a[184:187],a[200:203],a[36:39]\n"
S_"s62,3244032\n"
M_"a[40:43],a[184:187],a[208:211],a[40:43]\n"
M_"a[40:43],a[188:191],a[212:215],a[40:43]\n"
M_"a[44:47],a[188:191],a[220:223],a[44:47]\n"
T_"a[188:189],v248 offset:27872\n"
M_"a[44:47],a[184:187],a[216:219],a[44:47]\n"
W_"6)\n"
T_"a[184:185],v248 offset:18656\n"
M_"a[48:51],a[168:171],a[192:195],a[48:51]\n"
W_"6)\n"
T_"a[186:187],v248 offset:23264\n"
M_"a[48:51],a[172:175],a[196:199],a[48:51]\n"
L_"v254,s[24:27],s62 offen lds\n"
M_"a[52:55],a[172:175],a[204:207],a[52:55]\n"
A_"m0,s34,0\n"
T_"a[190:191],v248 offset:32480\n"
M_"a[52:55],a[168:171],a[200:203],a[52:55]\n"
S_"s62,3538944\n"
M_"a[56:59],a[168:171],a[208:211],a[56:59]\n"
M_"a[56:59],a[172:175],a[212:215],a[56:59]\n"
M_"a[60:63],a[172:175],a[220:223],a[60:63]\n"
T_"a[172:173],v248 offset:9472\n"
M_"a[60:63],a[168:171],a[216:219],a[60:63]\n"
W_"6)\n"
T_"a[168:169],v248 offset:256\n"
M_"a[64:67],a[176:179],a[192:195],a[64:67]\n"
W_"6)\n"
T_"a[170:171],v248 offset:4864\n"
M_"a[64:67],a[180:183],a[196:199],a[64:67]\n"
L_"v251,s[16:19],s62 offen lds\n"
M_"a[68:71],a[180:183],a[204:207],a[68:71]\n"
A_"m0,s34,1024\n"
T_"a[174:175],v248 offset:14080\n"
M_"a[68:71],a[176:179],a[200:203],a[68:71]\n"
S_"s62,3538944\n"
M_"a[72:75],a[176:179],a[208:211],a[72:75]\n"
M_"a[72:75],a[180:183],a[212:215],a[72:75]\n"
M_"a[76:79],a[180:183],a[220:223],a[76:79]\n"
T_"a[180:181],v248 offset:27904\n"
M_"a[76:79],a[176:179],a[216:219],a[76:79]\n"
W_"6)\n"
T_"a[176:177],v248 offset:18688\n"
M_"a[80:83],a[184:187],a[192:195],a[80:83]\n"
W_"6)\n"
T_"a[178:179],v248 offset:23296\n"
M_"a[80:83],a[188:191],a[196:199],a[80:83]\n"
L_"v252,s[16:19],s62 offen lds\n"
M_"a[84:87],a[188:191],a[204:207],a[84:87]\n"
A_"m0,s34,18432\n"
T_"a[182:183],v248 offset:32512\n"
M_"a[84:87],a[184:187],a[200:203],a[84:87]\n"
S_"s62,3538944\n"
"v_add_u32 v248,53248,v248\n"
M_"a[88:91],a[184:187],a[208:211],a[88:91]\n"
M_"a[88:91],a[188:191],a[212:215],a[88:91]\n"
M_"a[92:95],a[188:191],a[220:223],a[92:95]\n"
T_"a[188:189],v249 offset:45056\n"
M_"a[92:95],a[184:187],a[216:219],a[92:95]\n"
W_"6)\n"
T_"a[184:185],v249 offset:36864\n"
M_"a[96:99],a[168:171],a[192:195],a[96:99]\n"
W_"6)\n"
T_"a[186:187],v249 offset:40960\n"
M_"a[96:99],a[172:175],a[196:199],a[96:99]\n"
L_"v251,s[20:23],s62 offen lds\n"
M_"a[100:103],a[172:175],a[204:207],a[100:103]\n"
A_"m0,s34,19456\n"
T_"a[190:191],v249 offset:49152\n"
M_"a[100:103],a[168:171],a[200:203],a[100:103]\n"
S_"s62,3538944\n"
M_"a[104:107],a[168:171],a[208:211],a[104:107]\n"
M_"a[104:107],a[172:175],a[212:215],a[104:107]\n"
M_"a[108:111],a[172:175],a[220:223],a[108:111]\n"
T_"a[172:173],v249 offset:45064\n"
M_"a[108:111],a[168:171],a[216:219],a[108:111]\n"
W_"6)\n"
T_"a[168:169],v249 offset:36872\n"
M_"a[112:115],a[176:179],a[192:195],a[112:115]\n"
W_"6)\n"
T_"a[170:171],v249 offset:40968\n"
M_"a[112:115],a[180:183],a[196:199],a[112:115]\n"
L_"v252,s[20:23],s62 offen lds\n"
M_"a[116:119],a[180:183],a[204:207],a[116:119]\n"
A_"m0,s34,9216\n"
T_"a[174:175],v249 offset:49160\n"
M_"a[116:119],a[176:179],a[200:203],a[116:119]\n"
S_"s62,3833856\n"
M_"a[120:123],a[176:179],a[208:211],a[120:123]\n"
M_"a[120:123],a[180:183],a[212:215],a[120:123]\n"
M_"a[124:127],a[180:183],a[220:223],a[124:127]\n"
T_"a[180:181],v250 offset:45056\n"
M_"a[124:127],a[176:179],a[216:219],a[124:127]\n"
W_"6)\n"
T_"a[176:177],v250 offset:36864\n"
M_"v[0:3],a[184:187],a[192:195],v[0:3]\n"
W_"6)\n"
T_"a[178:179],v250 offset:40960\n"
M_"v[0:3],a[188:191],a[196:199],v[0:3]\n"
L_"v251,s[16:19],s62 offen lds\n"
M_"v[4:7],a[188:191],a[204:207],v[4:7]\n"
A_"m0,s34,10240\n"
T_"a[182:183],v250 offset:49152\n"
M_"v[4:7],a[184:187],a[200:203],v[4:7]\n"
S_"s62,3833856\n"
M_"v[8:11],a[184:187],a[208:211],v[8:11]\n"
M_"v[8:11],a[188:191],a[212:215],v[8:11]\n"
M_"v[12:15],a[188:191],a[220:223],v[12:15]\n"
T_"a[188:189],v250 offset:45064\n"
M_"v[12:15],a[184:187],a[216:219],v[12:15]\n"
W_"6)\n"
T_"a[184:185],v250 offset:36872\n"
M_"v[64:67],a[168:171],a[192:195],v[64:67]\n"
W_"6)\n"
T_"a[186:187],v250 offset:40968\n"
M_"v[64:67],a[172:175],a[196:199],v[64:67]\n"
L_"v252,s[16:19],s62 offen lds\n"
M_"v[68:71],a[172:175],a[204:207],v[68:71]\n"
A_"m0,s34,27648\n"
T_"a[190:191],v250 offset:49160\n"
M_"v[68:71],a[168:171],a[200:203],v[68:71]\n"
S_"s62,3833856\n"
"v_xor_b32 v250,128,v249\n"
M_"v[72:75],a[168:171],a[208:211],v[72:75]\n"
M_"v[72:75],a[172:175],a[212:215],v[72:75]\n"
M_"v[76:79],a[172:175],a[220:223],v[76:79]\n"
T_"a[172:173],v250 offset:45056\n"
M_"v[76:79],a[168:171],a[216:219],v[76:79]\n"
W_"6)\n"
T_"a[168:169],v250 offset:36864\n"
M_"v[16:19],a[176:179],a[192:195],v[16:19]\n"
W_"6)\n"
T_"a[170:171],v250 offset:40960\n"
M_"v[16:19],a[180:183],a[196:199],v[16:19]\n"
L_"v251,s[20:23],s62 offen lds\n"
M_"v[20:23],a[180:183],a[204:207],v[20:23]\n"
A_"m0,s34,28672\n"
T_"a[174:175],v250 offset:49152\n"
M_"v[20:23],a[176:179],a[200:203],v[20:23]\n"
S_"s62,3833856\n"
M_"v[24:27],a[176:179],a[208:211],v[24:27]\n"
M_"v[24:27],a[180:183],a[212:215],v[24:27]\n"
M_"v[28:31],a[180:183],a[220:223],v[28:31]\n"
T_"a[180:181],v250 offset:45064\n"
M_"v[28:31],a[176:179],a[216:219],v[28:31]\n"
W_"6)\n"
T_"a[176:177],v250 offset:36872\n"
M_"v[80:83],a[184:187],a[192:195],v[80:83]\n"
W_"6)\n"
T_"a[178:179],v250 offset:40968\n"
M_"v[80:83],a[188:191],a[196:199],v[80:83]\n"
L_"v252,s[20:23],s62 offen lds\n"
M_"v[84:87],a[188:191],a[204:207],v[84:87]\n"
A_"m0,s35,0\n"
T_"a[182:183],v250 offset:49160\n"
M_"v[84:87],a[184:187],a[200:203],v[84:87]\n"
S_"s62,3538944\n"
"v_xor_b32 v250,192,v249\n"
"v_add_u32 v249,53248,v249\n"
M_"v[88:91],a[184:187],a[208:211],v[88:91]\n"
M_"v[88:91],a[188:191],a[212:215],v[88:91]\n"
M_"v[92:95],a[188:191],a[220:223],v[92:95]\n"
T_"a[188:189],v250 offset:45056\n"
M_"v[92:95],a[184:187],a[216:219],v[92:95]\n"
W_"6)\n"
T_"a[184:185],v250 offset:36864\n"
M_"v[32:35],a[168:171],a[192:195],v[32:35]\n"
W_"6)\n"
T_"a[186:187],v250 offset:40960\n"
M_"v[32:35],a[172:175],a[196:199],v[32:35]\n"
L_"v253,s[28:31],s62 offen lds\n"
M_"v[36:39],a[172:175],a[204:207],v[36:39]\n"
S_"m0,159744\n"
T_"a[190:191],v250 offset:49152\n"
M_"v[36:39],a[168:171],a[200:203],v[36:39]\n"
S_"s62,3670016\n"
M_"v[40:43],a[168:171],a[208:211],v[40:43]\n"
M_"v[40:43],a[172:175],a[212:215],v[40:43]\n"
M_"v[44:47],a[172:175],a[220:223],v[44:47]\n"
T_"a[172:173],v250 offset:45064\n"
M_"v[44:47],a[168:171],a[216:219],v[44:47]\n"
W_"6)\n"
T_"a[168:169],v250 offset:36872\n"
M_"v[96:99],a[176:179],a[192:195],v[96:99]\n"
W_"6)\n"
T_"a[170:171],v250 offset:40968\n"
M_"v[96:99],a[180:183],a[196:199],v[96:99]\n"
T_"a[174:175],v250 offset:49160\n"
M_"v[100:103],a[180:183],a[204:207],v[100:103]\n"
"v_xor_b32 v250,64,v249\n"
M_"v[100:103],a[176:179],a[200:203],v[100:103]\n"
M_"v[104:107],a[176:179],a[208:211],v[104:107]\n"
M_"v[104:107],a[180:183],a[212:215],v[104:107]\n"
M_"v[108:111],a[180:183],a[220:223],v[108:111]\n"
M_"v[108:111],a[176:179],a[216:219],v[108:111]\n"
W_"5)\n"
M_"v[48:51],a[184:187],a[192:195],v[48:51]\n"
W_"4)\n"
M_"v[48:51],a[188:191],a[196:199],v[48:51]\n"
M_"v[52:55],a[188:191],a[204:207],v[52:55]\n"
"s_waitcnt vmcnt(22) lgkmcnt(0)\n"
"s_barrier\n"
T_"a[176:177],v248 offset:0\n"
M_"v[52:55],a[184:187],a[200:203],v[52:55]\n"
T_"a[178:179],v248 offset:4608\n"
M_"v[56:59],a[184:187],a[208:211],v[56:59]\n"
T_"a[180:181],v248 offset:9216\n"
M_"v[56:59],a[188:191],a[212:215],v[56:59]\n"
T_"a[182:183],v248 offset:13824\n"
M_"v[60:63],a[188:191],a[220:223],v[60:63]\n"
T_"a[188:189],v248 offset:27648\n"
M_"v[60:63],a[184:187],a[216:219],v[60:63]\n"
T_"a[184:185],v248 offset:18432\n"
M_"v[112:115],a[168:171],a[192:195],v[112:115]\n"
T_"a[186:187],v248 offset:23040\n"
M_"v[112:115],a[172:175],a[196:199],v[112:115]\n"
T_"a[190:191],v248 offset:32256\n"
M_"v[116:119],a[172:175],a[204:207],v[116:119]\n"
M_"v[116:119],a[168:171],a[200:203],v[116:119]\n"
M_"v[120:123],a[168:171],a[208:211],v[120:123]\n"
M_"v[120:123],a[172:175],a[212:215],v[120:123]\n"
M_"v[124:127],a[172:175],a[220:223],v[124:127]\n"
T_"a[172:173],v248 offset:9248\n"
M_"v[124:127],a[168:171],a[216:219],v[124:127]\n"
"s_waitcnt vmcnt(21) lgkmcnt(7)\n"
T_"a[168:169],v248 offset:32\n"
M_"v[128:131],a[176:179],a[224:227],v[128:131]\n"
"s_waitcnt vmcnt(20) lgkmcnt(6)\n"
L_"a[192:195],v255,s[36:39],s63 offen sc1\n"
M_"v[128:131],a[180:183],a[228:231],v[128:131]\n"
"s_waitcnt vmcnt(19)\n"
T_"a[170:171],v248 offset:4640\n"
M_"v[132:135],a[180:183],a[236:239],v[132:135]\n"
T_"a[174:175],v248 offset:13856\n"
M_"v[132:135],a[176:179],a[232:235],v[132:135]\n"
"s_waitcnt vmcnt(18)\n"
M_"v[136:139],a[176:179],a[240:243],v[136:139]\n"
"s_waitcnt vmcnt(17)\n"
M_"v[136:139],a[180:183],a[244:247],v[136:139]\n"
"s_waitcnt vmcnt(15)\n"
M_"v[140:143],a[180:183],a[252:255],v[140:143]\n"
T_"a[180:181],v248 offset:27680\n"
M_"v[140:143],a[176:179],a[248:251],v[140:143]\n"
W_"6)\n"
T_"a[176:177],v248 offset:18464\n"
M_"v[144:147],a[184:187],a[224:227],v[144:147]\n"
W_"6)\n"
T_"a[178:179],v248 offset:23072\n"
M_"v[144:147],a[188:191],a[228:231],v[144:147]\n"
L_"a[196:199],v255,s[36:39],s63 offen offset:1024 sc1\n"
M_"v[148:151],a[188:191],a[236:239],v[148:151]\n"
S_"s63,30720\n"
T_"a[182:183],v248 offset:32288\n"
M_"v[148:151],a[184:187],a[232:235],v[148:151]\n"
M_"v[152:155],a[184:187],a[240:243],v[152:155]\n"
M_"v[152:155],a[188:191],a[244:247],v[152:155]\n"
M_"v[156:159],a[188:191],a[252:255],v[156:159]\n"
T_"a[188:189],v248 offset:9280\n"
M_"v[156:159],a[184:187],a[248:251],v[156:159]\n"
W_"6)\n"
T_"a[184:185],v248 offset:64\n"
M_"v[160:163],a[168:171],a[224:227],v[160:163]\n"
W_"6)\n"
T_"a[186:187],v248 offset:4672\n"
M_"v[160:163],a[172:175],a[228:231],v[160:163]\n"
T_"a[190:191],v248 offset:13888\n"
M_"v[164:167],a[172:175],a[236:239],v[164:167]\n"
L_"a[200:203],v255,s[36:39],s63 offen sc1\n"
M_"v[164:167],a[168:171],a[232:235],v[164:167]\n"
M_"v[168:171],a[168:171],a[240:243],v[168:171]\n"
M_"v[168:171],a[172:175],a[244:247],v[168:171]\n"
M_"v[172:175],a[172:175],a[252:255],v[172:175]\n"
T_"a[172:173],v248 offset:27712\n"
M_"v[172:175],a[168:171],a[248:251],v[172:175]\n"
W_"6)\n"
T_"a[168:169],v248 offset:18496\n"
M_"v[176:179],a[176:179],a[224:227],v[176:179]\n"
W_"6)\n"
T_"a[170:171],v248 offset:23104\n"
M_"v[176:179],a[180:183],a[228:231],v[176:179]\n"
T_"a[174:175],v248 offset:32320\n"
M_"v[180:183],a[180:183],a[236:239],v[180:183]\n"
M_"v[180:183],a[176:179],a[232:235],v[180:183]\n"
L_"a[204:207],v255,s[36:39],s63 offen offset:1024 sc1\n"
M_"v[184:187],a[176:179],a[240:243],v[184:187]\n"
S_"s63,49152\n"
M_"v[184:187],a[180:183],a[244:247],v[184:187]\n"
M_"v[188:191],a[180:183],a[252:255],v[188:191]\n"
T_"a[180:181],v248 offset:9312\n"
M_"v[188:191],a[176:179],a[248:251],v[188:191]\n"
W_"6)\n"
T_"a[176:177],v248 offset:96\n"
M_"v[192:195],a[184:187],a[224:227],v[192:195]\n"
W_"6)\n"
T_"a[178:179],v248 offset:4704\n"
M_"v[192:195],a[188:191],a[228:231],v[192:195]\n"
T_"a[182:183],v248 offset:13920\n"
M_"v[196:199],a[188:191],a[236:239],v[196:199]\n"
M_"v[196:199],a[184:187],a[232:235],v[196:199]\n"
M_"v[200:203],a[184:187],a[240:243],v[200:203]\n"
L_"a[208:211],v255,s[36:39],s63 offen sc1\n"
M_"v[200:203],a[188:191],a[244:247],v[200:203]\n"
M_"v[204:207],a[188:191],a[252:255],v[204:207]\n"
T_"a[188:189],v248 offset:27744\n"
M_"v[204:207],a[184:187],a[248:251],v[204:207]\n"
W_"6)\n"
T_"a[184:185],v248 offset:18528\n"
M_"v[208:211],a[168:171],a[224:227],v[208:211]\n"
W_"6)\n"
T_"a[186:187],v248 offset:23136\n"
M_"v[208:211],a[172:175],a[228:231],v[208:211]\n"
T_"a[190:191],v248 offset:32352\n"
M_"v[212:215],a[172:175],a[236:239],v[212:215]\n"
M_"v[212:215],a[168:171],a[232:235],v[212:215]\n"
M_"v[216:219],a[168:171],a[240:243],v[216:219]\n"
M_"v[216:219],a[172:175],a[244:247],v[216:219]\n"
L_"a[212:215],v255,s[36:39],s63 offen offset:1024 sc1\n"
M_"v[220:223],a[172:175],a[252:255],v[220:223]\n"
S_"s63,67584\n"
T_"a[172:173],v248 offset:9344\n"
M_"v[220:223],a[168:171],a[248:251],v[220:223]\n"
W_"6)\n"
T_"a[168:169],v248 offset:128\n"
M_"v[224:227],a[176:179],a[224:227],v[224:227]\n"
W_"6)\n"
T_"a[170:171],v248 offset:4736\n"
M_"v[224:227],a[180:183],a[228:231],v[224:227]\n"
T_"a[174:175],v248 offset:13952\n"
M_"v[228:231],a[180:183],a[236:239],v[228:231]\n"
M_"v[228:231],a[176:179],a[232:235],v[228:231]\n"
M_"v[232:235],a[176:179],a[240:243],v[232:235]\n"
M_"v[232:235],a[180:183],a[244:247],v[232:235]\n"
M_"v[236:239],a[180:183],a[252:255],v[236:239]\n"
L_"a[216:219],v255,s[36:39],s63 offen sc1\n"
M_"v[236:239],a[176:179],a[248:251],v[236:239]\n"
W_"5)\n"
T_"a[176:177],v248 offset:18560\n"
M_"v[240:243],a[184:187],a[224:227],v[240:243]\n"
W_"5)\n"
T_"a[178:179],v248 offset:23168\n"
M_"v[240:243],a[188:191],a[228:231],v[240:243]\n"
T_"a[180:181],v248 offset:27776\n"
M_"v[244:247],a[188:191],a[236:239],v[244:247]\n"
T_"a[182:183],v248 offset:32384\n"
M_"v[244:247],a[184:187],a[232:235],v[244:247]\n"
M_"a[128:131],a[184:187],a[240:243],a[128:131]\n"
M_"a[128:131],a[188:191],a[244:247],a[128:131]\n"
M_"a[132:135],a[188:191],a[252:255],a[132:135]\n"
T_"a[188:189],v248 offset:9376\n"
M_"a[132:135],a[184:187],a[248:251],a[132:135]\n"
W_"6)\n"
L_"a[220:223],v255,s[36:39],s63 offen offset:1024 sc1\n"
M_"a[136:139],a[168:171],a[224:227],a[136:139]\n"
W_"5)\n"
T_"a[184:185],v248 offset:160\n"
M_"a[136:139],a[172:175],a[228:231],a[136:139]\n"
S_"s63,14336\n"
T_"a[186:187],v248 offset:4768\n"
M_"a[140:143],a[172:175],a[236:239],a[140:143]\n"
T_"a[190:191],v248 offset:13984\n"
M_"a[140:143],a[168:171],a[232:235],a[140:143]\n"
M_"a[144:147],a[168:171],a[240:243],a[144:147]\n"
M_"a[144:147],a[172:175],a[244:247],a[144:147]\n"
M_"a[148:151],a[172:175],a[252:255],a[148:151]\n"
T_"a[172:173],v248 offset:27808\n"
M_"a[148:151],a[168:171],a[248:251],a[148:151]\n"
W_"7)\n"
T_"a[168:169],v248 offset:18592\n"
M_"a[152:155],a[176:179],a[224:227],a[152:155]\n"
W_"6)\n"
L_"v255,s[52:55],s77 offen offset:3072 lds\n"
M_"a[152:155],a[180:183],a[228:231],a[152:155]\n"
A_"m0,s60,90112\n"
T_"a[170:171],v248 offset:23200\n"
M_"a[156:159],a[180:183],a[236:239],a[156:159]\n"
A_"s77,s76,9216\n"
T_"a[174:175],v248 offset:32416\n"
M_"a[156:159],a[176:179],a[232:235],a[156:159]\n"
"s_lshl_b32 s77,s77,10\n"
M_"a[160:163],a[176:179],a[240:243],a[160:163]\n"
M_"a[160:163],a[180:183],a[244:247],a[160:163]\n"
M_"a[164:167],a[180:183],a[252:255],a[164:167]\n"
T_"a[180:181],v248 offset:9408\n"
M_"a[164:167],a[176:179],a[248:251],a[164:167]\n"
W_"6)\n"
T_"a[176:177],v248 offset:192\n"
M_"a[0:3],a[184:187],a[224:227],a[0:3]\n"
W_"6)\n"
T_"a[178:179],v248 offset:4800\n"
M_"a[0:3],a[188:191],a[228:231],a[0:3]\n"
L_"v254,s[24:27],s62 offen lds\n"
M_"a[4:7],a[188:191],a[236:239],a[4:7]\n"
A_"m0,s60,91136\n"
T_"a[182:183],v248 offset:14016\n"
M_"a[4:7],a[184:187],a[232:235],a[4:7]\n"
S_"s62,3702784\n"
M_"a[8:11],a[184:187],a[240:243],a[8:11]\n"
M_"a[8:11],a[188:191],a[244:247],a[8:11]\n"
M_"a[12:15],a[188:191],a[252:255],a[12:15]\n"
T_"a[188:189],v248 offset:27840\n"
M_"a[12:15],a[184:187],a[248:251],a[12:15]\n"
W_"6)\n"
T_"a[184:185],v248 offset:18624\n"
M_"a[16:19],a[168:171],a[224:227],a[16:19]\n"
W_"6)\n"
T_"a[186:187],v248 offset:23232\n"
M_"a[16:19],a[172:175],a[228:231],a[16:19]\n"
L_"v254,s[24:27],s62 offen lds\n"
M_"a[20:23],a[172:175],a[236:239],a[20:23]\n"
A_"m0,s60,92160\n"
T_"a[190:191],v248 offset:32448\n"
M_"a[20:23],a[168:171],a[232:235],a[20:23]\n"
S_"s62,3735552\n"
M_"a[24:27],a[168:171],a[240:243],a[24:27]\n"
M_"a[24:27],a[172:175],a[244:247],a[24:27]\n"
M_"a[28:31],a[172:175],a[252:255],a[28:31]\n"
T_"a[172:173],v248 offset:9440\n"
M_"a[28:31],a[168:171],a[248:251],a[28:31]\n"
W_"6)\n"
T_"a[168:169],v248 offset:224\n"
M_"a[32:35],a[176:179],a[224:227],a[32:35]\n"
W_"6)\n"
T_"a[170:171],v248 offset:4832\n"
M_"a[32:35],a[180:183],a[228:231],a[32:35]\n"
L_"v254,s[24:27],s62 offen lds\n"
M_"a[36:39],a[180:183],a[236:239],a[36:39]\n"
A_"m0,s60,93184\n"
T_"a[174:175],v248 offset:14048\n"
M_"a[36:39],a[176:179],a[232:235],a[36:39]\n"
S_"s62,3768320\n"
M_"a[40:43],a[176:179],a[240:243],a[40:43]\n"
M_"a[40:43],a[180:183],a[244:247],a[40:43]\n"
M_"a[44:47],a[180:183],a[252:255],a[44:47]\n"
T_"a[180:181],v248 offset:27872\n"
M_"a[44:47],a[176:179],a[248:251],a[44:47]\n"
W_"6)\n"
T_"a[176:177],v248 offset:18656\n"
M_"a[48:51],a[184:187],a[224:227],a[48:51]\n"
W_"6)\n"
T_"a[178:179],v248 offset:23264\n"
M_"a[48:51],a[188:191],a[228:231],a[48:51]\n"
L_"v254,s[24:27],s62 offen lds\n"
M_"a[52:55],a[188:191],a[236:239],a[52:55]\n"
A_"m0,s34,53248\n"
T_"a[182:183],v248 offset:32480\n"
M_"a[52:55],a[184:187],a[232:235],a[52:55]\n"
S_"s62,4128768\n"
M_"a[56:59],a[184:187],a[240:243],a[56:59]\n"
M_"a[56:59],a[188:191],a[244:247],a[56:59]\n"
M_"a[60:63],a[188:191],a[252:255],a[60:63]\n"
T_"a[188:189],v248 offset:9472\n"
M_"a[60:63],a[184:187],a[248:251],a[60:63]\n"
W_"6)\n"
T_"a[184:185],v248 offset:256\n"
M_"a[64:67],a[168:171],a[224:227],a[64:67]\n"
W_"6)\n"
T_"a[186:187],v248 offset:4864\n"
M_"a[64:67],a[172:175],a[228:231],a[64:67]\n"
L_"v251,s[16:19],s62 offen lds\n"
M_"a[68:71],a[172:175],a[236:239],a[68:71]\n"
A_"m0,s34,54272\n"
T_"a[190:191],v248 offset:14080\n"
M_"a[68:71],a[168:171],a[232:235],a[68:71]\n"
S_"s62,4128768\n"
M_"a[72:75],a[168:171],a[240:243],a[72:75]\n"
M_"a[72:75],a[172:175],a[244:247],a[72:75]\n"
M_"a[76:79],a[172:175],a[252:255],a[76:79]\n"
T_"a[172:173],v248 offset:27904\n"
M_"a[76:79],a[168:171],a[248:251],a[76:79]\n"
W_"6)\n"
T_"a[168:169],v248 offset:18688\n"
M_"a[80:83],a[176:179],a[224:227],a[80:83]\n"
W_"6)\n"
T_"a[170:171],v248 offset:23296\n"
M_"a[80:83],a[180:183],a[228:231],a[80:83]\n"
L_"v252,s[16:19],s62 offen lds\n"
M_"a[84:87],a[180:183],a[236:239],a[84:87]\n"
A_"m0,s34,71680\n"
T_"a[174:175],v248 offset:32512\n"
M_"a[84:87],a[176:179],a[232:235],a[84:87]\n"
S_"s62,4128768\n"
"v_subrev_u32 v248,106496,v248\n"
M_"a[88:91],a[176:179],a[240:243],a[88:91]\n"
M_"a[88:91],a[180:183],a[244:247],a[88:91]\n"
M_"a[92:95],a[180:183],a[252:255],a[92:95]\n"
T_"a[180:181],v249 offset:45056\n"
M_"a[92:95],a[176:179],a[248:251],a[92:95]\n"
W_"6)\n"
T_"a[176:177],v249 offset:36864\n"
M_"a[96:99],a[184:187],a[224:227],a[96:99]\n"
W_"6)\n"
T_"a[178:179],v249 offset:40960\n"
M_"a[96:99],a[188:191],a[228:231],a[96:99]\n"
L_"v251,s[20:23],s62 offen lds\n"
M_"a[100:103],a[188:191],a[236:239],a[100:103]\n"
A_"m0,s34,72704\n"
T_"a[182:183],v249 offset:49152\n"
M_"a[100:103],a[184:187],a[232:235],a[100:103]\n"
S_"s62,4128768\n"
M_"a[104:107],a[184:187],a[240:243],a[104:107]\n"
M_"a[104:107],a[188:191],a[244:247],a[104:107]\n"
M_"a[108:111],a[188:191],a[252:255],a[108:111]\n"
T_"a[188:189],v249 offset:45064\n"
M_"a[108:111],a[184:187],a[248:251],a[108:111]\n"
W_"6)\n"
T_"a[184:185],v249 offset:36872\n"
M_"a[112:115],a[168:171],a[224:227],a[112:115]\n"
W_"6)\n"
T_"a[186:187],v249 offset:40968\n"
M_"a[112:115],a[172:175],a[228:231],a[112:115]\n"
L_"v252,s[20:23],s62 offen lds\n"
M_"a[116:119],a[172:175],a[236:239],a[116:119]\n"
A_"m0,s34,62464\n"
T_"a[190:191],v249 offset:49160\n"
M_"a[116:119],a[168:171],a[232:235],a[116:119]\n"
S_"s62,4423680\n"
M_"a[120:123],a[168:171],a[240:243],a[120:123]\n"
M_"a[120:123],a[172:175],a[244:247],a[120:123]\n"
M_"a[124:127],a[172:175],a[252:255],a[124:127]\n"
T_"a[172:173],v250 offset:45056\n"
M_"a[124:127],a[168:171],a[248:251],a[124:127]\n"
W_"6)\n"
T_"a[168:169],v250 offset:36864\n"
M_"v[0:3],a[176:179],a[224:227],v[0:3]\n"
W_"6)\n"
T_"a[170:171],v250 offset:40960\n"
M_"v[0:3],a[180:183],a[228:231],v[0:3]\n"
L_"v251,s[16:19],s62 offen lds\n"
M_"v[4:7],a[180:183],a[236:239],v[4:7]\n"
A_"m0,s34,63488\n"
T_"a[174:175],v250 offset:49152\n"
M_"v[4:7],a[176:179],a[232:235],v[4:7]\n"
S_"s62,4423680\n"
M_"v[8:11],a[176:179],a[240:243],v[8:11]\n"
M_"v[8:11],a[180:183],a[244:247],v[8:11]\n"
M_"v[12:15],a[180:183],a[252:255],v[12:15]\n"
T_"a[180:181],v250 offset:45064\n"
M_"v[12:15],a[176:179],a[248:251],v[12:15]\n"
W_"6)\n"
T_"a[176:177],v250 offset:36872\n"
M_"v[64:67],a[184:187],a[224:227],v[64:67]\n"
W_"6)\n"
T_"a[178:179],v250 offset:40968\n"
M_"v[64:67],a[188:191],a[228:231],v[64:67]\n"
L_"v252,s[16:19],s62 offen lds\n"
M_"v[68:71],a[188:191],a[236:239],v[68:71]\n"
A_"m0,s34,80896\n"
T_"a[182:183],v250 offset:49160\n"
M_"v[68:71],a[184:187],a[232:235],v[68:71]\n"
S_"s62,4423680\n"
"v_xor_b32 v250,128,v249\n"
M_"v[72:75],a[184:187],a[240:243],v[72:75]\n"
M_"v[72:75],a[188:191],a[244:247],v[72:75]\n"
M_"v[76:79],a[188:191],a[252:255],v[76:79]\n"
T_"a[188:189],v250 offset:45056\n"
M_"v[76:79],a[184:187],a[248:251],v[76:79]\n"
W_"6)\n"
T_"a[184:185],v250 offset:36864\n"
M_"v[16:19],a[168:171],a[224:227],v[16:19]\n"
W_"6)\n"
T_"a[186:187],v250 offset:40960\n"
M_"v[16:19],a[172:175],a[228:231],v[16:19]\n"
L_"v251,s[20:23],s62 offen lds\n"
M_"v[20:23],a[172:175],a[236:239],v[20:23]\n"
A_"m0,s34,81920\n"
T_"a[190:191],v250 offset:49152\n"
M_"v[20:23],a[168:171],a[232:235],v[20:23]\n"
S_"s62,4423680\n"
M_"v[24:27],a[168:171],a[240:243],v[24:27]\n"
M_"v[24:27],a[172:175],a[244:247],v[24:27]\n"
M_"v[28:31],a[172:175],a[252:255],v[28:31]\n"
T_"a[172:173],v250 offset:45064\n"
M_"v[28:31],a[168:171],a[248:251],v[28:31]\n"
W_"6)\n"
T_"a[168:169],v250 offset:36872\n"
M_"v[80:83],a[176:179],a[224:227],v[80:83]\n"
W_"6)\n"
T_"a[170:171],v250 offset:40968\n"
M_"v[80:83],a[180:183],a[228:231],v[80:83]\n"
L_"v252,s[20:23],s62 offen lds\n"
M_"v[84:87],a[180:183],a[236:239],v[84:87]\n"
A_"m0,s35,53248\n"
T_"a[174:175],v250 offset:49160\n"
M_"v[84:87],a[176:179],a[232:235],v[84:87]\n"
S_"s62,4128768\n"
"v_xor_b32 v250,192,v249\n"
"v_subrev_u32 v249,106496,v249\n"
M_"v[88:91],a[176:179],a[240:243],v[88:91]\n"
M_"v[88:91],a[180:183],a[244:247],v[88:91]\n"
M_"v[92:95],a[180:183],a[252:255],v[92:95]\n"
T_"a[180:181],v250 offset:45056\n"
M_"v[92:95],a[176:179],a[248:251],v[92:95]\n"
W_"6)\n"
T_"a[176:177],v250 offset:36864\n"
M_"v[32:35],a[184:187],a[224:227],v[32:35]\n"
W_"6)\n"
T_"a[178:179],v250 offset:40960\n"
M_"v[32:35],a[188:191],a[228:231],v[32:35]\n"
L_"v253,s[28:31],s62 offen lds\n"
M_"v[36:39],a[188:191],a[236:239],v[36:39]\n"
A_"m0,s60,143360\n"
T_"a[182:183],v250 offset:49152\n"
M_"v[36:39],a[184:187],a[232:235],v[36:39]\n"
S_"s62,4194304\n"
M_"v[40:43],a[184:187],a[240:243],v[40:43]\n"
M_"v[40:43],a[188:191],a[244:247],v[40:43]\n"
M_"v[44:47],a[188:191],a[252:255],v[44:47]\n"
T_"a[188:189],v250 offset:45064\n"
M_"v[44:47],a[184:187],a[248:251],v[44:47]\n"
W_"6)\n"
T_"a[184:185],v250 offset:36872\n"
M_"v[96:99],a[168:171],a[224:227],v[96:99]\n"
W_"6)\n"
T_"a[186:187],v250 offset:40968\n"
M_"v[96:99],a[172:175],a[228:231],v[96:99]\n"
T_"a[190:191],v250 offset:49160\n"
M_"v[100:103],a[172:175],a[236:239],v[100:103]\n"
"v_xor_b32 v250,64,v249\n"
M_"v[100:103],a[168:171],a[232:235],v[100:103]\n"
M_"v[104:107],a[168:171],a[240:243],v[104:107]\n"
M_"v[104:107],a[172:175],a[244:247],v[104:107]\n"
M_"v[108:111],a[172:175],a[252:255],v[108:111]\n"
M_"v[108:111],a[168:171],a[248:251],v[108:111]\n"
W_"5)\n"
M_"v[48:51],a[176:179],a[224:227],v[48:51]\n"
W_"4)\n"
M_"v[48:51],a[180:183],a[228:231],v[48:51]\n"
M_"v[52:55],a[180:183],a[236:239],v[52:55]\n"
"s_waitcnt vmcnt(22) lgkmcnt(0)\n"
"s_barrier\n"
T_"a[168:169],v248 offset:0\n"
M_"v[52:55],a[176:179],a[232:235],v[52:55]\n"
T_"a[170:171],v248 offset:4608\n"
M_"v[56:59],a[176:179],a[240:243],v[56:59]\n"
T_"a[172:173],v248 offset:9216\n"
M_"v[56:59],a[180:183],a[244:247],v[56:59]\n"
T_"a[174:175],v248 offset:13824\n"
M_"v[60:63],a[180:183],a[252:255],v[60:63]\n"
T_"a[180:181],v248 offset:27648\n"
M_"v[60:63],a[176:179],a[248:251],v[60:63]\n"
T_"a[176:177],v248 offset:18432\n"
M_"v[112:115],a[184:187],a[224:227],v[112:115]\n"
T_"a[178:179],v248 offset:23040\n"
M_"v[112:115],a[188:191],a[228:231],v[112:115]\n"
T_"a[182:183],v248 offset:32256\n"
M_"v[116:119],a[188:191],a[236:239],v[116:119]\n"
M_"v[116:119],a[184:187],a[232:235],v[116:119]\n"
M_"v[120:123],a[184:187],a[240:243],v[120:123]\n"
M_"v[120:123],a[188:191],a[244:247],v[120:123]\n"
M_"v[124:127],a[188:191],a[252:255],v[124:127]\n"
T_"a[188:189],v248 offset:9248\n"
M_"v[124:127],a[184:187],a[248:251],v[124:127]\n"
"s_waitcnt vmcnt(21) lgkmcnt(7)\n"
T_"a[184:185],v248 offset:32\n"
M_"v[128:131],a[168:171],a[192:195],v[128:131]\n"
"s_waitcnt vmcnt(20) lgkmcnt(6)\n"
L_"a[224:227],v255,s[36:39],s63 offen sc1\n"
M_"v[128:131],a[172:175],a[196:199],v[128:131]\n"
"s_waitcnt vmcnt(19)\n"
T_"a[186:187],v248 offset:4640\n"
M_"v[132:135],a[172:175],a[204:207],v[132:135]\n"
T_"a[190:191],v248 offset:13856\n"
M_"v[132:135],a[168:171],a[200:203],v[132:135]\n"
"s_waitcnt vmcnt(18)\n"
M_"v[136:139],a[168:171],a[208:211],v[136:139]\n"
"s_waitcnt vmcnt(17)\n"
M_"v[136:139],a[172:175],a[212:215],v[136:139]\n"
"s_waitcnt vmcnt(15)\n"
M_"v[140:143],a[172:175],a[220:223],v[140:143]\n"
T_"a[172:173],v248 offset:27680\n"
M_"v[140:143],a[168:171],a[216:219],v[140:143]\n"
W_"6)\n"
T_"a[168:169],v248 offset:18464\n"
M_"v[144:147],a[176:179],a[192:195],v[144:147]\n"
W_"6)\n"
T_"a[170:171],v248 offset:23072\n"
M_"v[144:147],a[180:183],a[196:199],v[144:147]\n"
L_"a[228:231],v255,s[36:39],s63 offen offset:1024 sc1\n"
M_"v[148:151],a[180:183],a[204:207],v[148:151]\n"
S_"s63,32768\n"
T_"a[174:175],v248 offset:32288\n"
M_"v[148:151],a[176:179],a[200:203],v[148:151]\n"
M_"v[152:155],a[176:179],a[208:211],v[152:155]\n"
M_"v[152:155],a[180:183],a[212:215],v[152:155]\n"
M_"v[156:159],a[180:183],a[220:223],v[156:159]\n"
T_"a[180:181],v248 offset:9280\n"
M_"v[156:159],a[176:179],a[216:219],v[156:159]\n"
W_"6)\n"
T_"a[176:177],v248 offset:64\n"
M_"v[160:163],a[184:187],a[192:195],v[160:163]\n"
W_"6)\n"
T_"a[178:179],v248 offset:4672\n"
M_"v[160:163],a[188:191],a[196:199],v[160:163]\n"
T_"a[182:183],v248 offset:13888\n"
M_"v[164:167],a[188:191],a[204:207],v[164:167]\n"
L_"a[232:235],v255,s[36:39],s63 offen sc1\n"
M_"v[164:167],a[184:187],a[200:203],v[164:167]\n"
M_"v[168:171],a[184:187],a[208:211],v[168:171]\n"
M_"v[168:171],a[188:191],a[212:215],v[168:171]\n"
M_"v[172:175],a[188:191],a[220:223],v[172:175]\n"
T_"a[188:189],v248 offset:27712\n"
M_"v[172:175],a[184:187],a[216:219],v[172:175]\n"
W_"6)\n"
T_"a[184:185],v248 offset:18496\n"
M_"v[176:179],a[168:171],a[192:195],v[176:179]\n"
W_"6)\n"
T_"a[186:187],v248 offset:23104\n"
M_"v[176:179],a[172:175],a[196:199],v[176:179]\n"
T_"a[190:191],v248 offset:32320\n"
M_"v[180:183],a[172:175],a[204:207],v[180:183]\n"
M_"v[180:183],a[168:171],a[200:203],v[180:183]\n"
L_"a[236:239],v255,s[36:39],s63 offen offset:1024 sc1\n"
M_"v[184:187],a[168:171],a[208:211],v[184:187]\n"
S_"s63,51200\n"
M_"v[184:187],a[172:175],a[212:215],v[184:187]\n"
M_"v[188:191],a[172:175],a[220:223],v[188:191]\n"
T_"a[172:173],v248 offset:9312\n"
M_"v[188:191],a[168:171],a[216:219],v[188:191]\n"
W_"6)\n"
T_"a[168:169],v248 offset:96\n"
M_"v[192:195],a[176:179],a[192:195],v[192:195]\n"
W_"6)\n"
T_"a[170:171],v248 offset:4704\n"
M_"v[192:195],a[180:183],a[196:199],v[192:195]\n"
T_"a[174:175],v248 offset:13920\n"
M_"v[196:199],a[180:183],a[204:207],v[196:199]\n"
M_"v[196:199],a[176:179],a[200:203],v[196:199]\n"
M_"v[200:203],a[176:179],a[208:211],v[200:203]\n"
L_"a[240:243],v255,s[36:39],s63 offen sc1\n"
M_"v[200:203],a[180:183],a[212:215],v[200:203]\n"
M_"v[204:207],a[180:183],a[220:223],v[204:207]\n"
T_"a[180:181],v248 offset:27744\n"
M_"v[204:207],a[176:179],a[216:219],v[204:207]\n"
W_"6)\n"
T_"a[176:177],v248 offset:18528\n"
M_"v[208:211],a[184:187],a[192:195],v[208:211]\n"
W_"6)\n"
T_"a[178:179],v248 offset:23136\n"
M_"v[208:211],a[188:191],a[196:199],v[208:211]\n"
T_"a[182:183],v248 offset:32352\n"
M_"v[212:215],a[188:191],a[204:207],v[212:215]\n"
M_"v[212:215],a[184:187],a[200:203],v[212:215]\n"
M_"v[216:219],a[184:187],a[208:211],v[216:219]\n"
M_"v[216:219],a[188:191],a[212:215],v[216:219]\n"
L_"a[244:247],v255,s[36:39],s63 offen offset:1024 sc1\n"
M_"v[220:223],a[188:191],a[220:223],v[220:223]\n"
S_"s63,69632\n"
T_"a[188:189],v248 offset:9344\n"
M_"v[220:223],a[184:187],a[216:219],v[220:223]\n"
W_"6)\n"
T_"a[184:185],v248 offset:128\n"
M_"v[224:227],a[168:171],a[192:195],v[224:227]\n"
W_"6)\n"
T_"a[186:187],v248 offset:4736\n"
M_"v[224:227],a[172:175],a[196:199],v[224:227]\n"
T_"a[190:191],v248 offset:13952\n"
M_"v[228:231],a[172:175],a[204:207],v[228:231]\n"
M_"v[228:231],a[168:171],a[200:203],v[228:231]\n"
M_"v[232:235],a[168:171],a[208:211],v[232:235]\n"
M_"v[232:235],a[172:175],a[212:215],v[232:235]\n"
M_"v[236:239],a[172:175],a[220:223],v[236:239]\n"
L_"a[248:251],v255,s[36:39],s63 offen sc1\n"
M_"v[236:239],a[168:171],a[216:219],v[236:239]\n"
W_"5)\n"
T_"a[168:169],v248 offset:18560\n"
M_"v[240:243],a[176:179],a[192:195],v[240:243]\n"
W_"5)\n"
T_"a[170:171],v248 offset:23168\n"
M_"v[240:243],a[180:183],a[196:199],v[240:243]\n"
T_"a[172:173],v248 offset:27776\n"
M_"v[244:247],a[180:183],a[204:207],v[244:247]\n"
T_"a[174:175],v248 offset:32384\n"
M_"v[244:247],a[176:179],a[200:203],v[244:247]\n"
M_"a[128:131],a[176:179],a[208:211],a[128:131]\n"
M_"a[128:131],a[180:183],a[212:215],a[128:131]\n"
M_"a[132:135],a[180:183],a[220:223],a[132:135]\n"
T_"a[180:181],v248 offset:9376\n"
M_"a[132:135],a[176:179],a[216:219],a[132:135]\n"
W_"6)\n"
L_"a[252:255],v255,s[36:39],s63 offen offset:1024 sc1\n"
M_"a[136:139],a[184:187],a[192:195],a[136:139]\n"
W_"5)\n"
T_"a[176:177],v248 offset:160\n"
M_"a[136:139],a[188:191],a[196:199],a[136:139]\n"
S_"s63,16384\n"
T_"a[178:179],v248 offset:4768\n"
M_"a[140:143],a[188:191],a[204:207],a[140:143]\n"
T_"a[182:183],v248 offset:13984\n"
M_"a[140:143],a[184:187],a[200:203],a[140:143]\n"
M_"a[144:147],a[184:187],a[208:211],a[144:147]\n"
M_"a[144:147],a[188:191],a[212:215],a[144:147]\n"
M_"a[148:151],a[188:191],a[220:223],a[148:151]\n"
T_"a[188:189],v248 offset:27808\n"
M_"a[148:151],a[184:187],a[216:219],a[148:151]\n"
W_"7)\n"
T_"a[184:185],v248 offset:18592\n"
M_"a[152:155],a[168:171],a[192:195],a[152:155]\n"
W_"6)\n"
L_"v254,s[24:27],s62 offen lds\n"
M_"a[152:155],a[172:175],a[196:199],a[152:155]\n"
A_"m0,s60,144384\n"
T_"a[186:187],v248 offset:23200\n"
M_"a[156:159],a[172:175],a[204:207],a[156:159]\n"
S_"s62,4227072\n"
T_"a[190:191],v248 offset:32416\n"
M_"a[156:159],a[168:171],a[200:203],a[156:159]\n"
M_"a[160:163],a[168:171],a[208:211],a[160:163]\n"
M_"a[160:163],a[172:175],a[212:215],a[160:163]\n"
M_"a[164:167],a[172:175],a[220:223],a[164:167]\n"
T_"a[172:173],v248 offset:9408\n"
M_"a[164:167],a[168:171],a[216:219],a[164:167]\n"
W_"6)\n"
T_"a[168:169],v248 offset:192\n"
M_"a[0:3],a[176:179],a[192:195],a[0:3]\n"
W_"6)\n"
L_"v254,s[24:27],s62 offen lds\n"
M_"a[0:3],a[180:183],a[196:199],a[0:3]\n"
A_"m0,s60,145408\n"
T_"a[170:171],v248 offset:4800\n"
M_"a[4:7],a[180:183],a[204:207],a[4:7]\n"
S_"s62,4259840\n"
T_"a[174:175],v248 offset:14016\n"
M_"a[4:7],a[176:179],a[200:203],a[4:7]\n"
M_"a[8:11],a[176:179],a[208:211],a[8:11]\n"
M_"a[8:11],a[180:183],a[212:215],a[8:11]\n"
M_"a[12:15],a[180:183],a[220:223],a[12:15]\n"
T_"a[180:181],v248 offset:27840\n"
M_"a[12:15],a[176:179],a[216:219],a[12:15]\n"
W_"6)\n"
T_"a[176:177],v248 offset:18624\n"
M_"a[16:19],a[184:187],a[192:195],a[16:19]\n"
W_"6)\n"
L_"v254,s[24:27],s62 offen lds\n"
M_"a[16:19],a[188:191],a[196:199],a[16:19]\n"
A_"m0,s60,146432\n"
T_"a[178:179],v248 offset:23232\n"
M_"a[20:23],a[188:191],a[204:207],a[20:23]\n"
S_"s62,4292608\n"
T_"a[182:183],v248 offset:32448\n"
M_"a[20:23],a[184:187],a[200:203],a[20:23]\n"
M_"a[24:27],a[184:187],a[208:211],a[24:27]\n"
M_"a[24:27],a[188:191],a[212:215],a[24:27]\n"
M_"a[28:31],a[188:191],a[220:223],a[28:31]\n"
T_"a[188:189],v248 offset:9440\n"
M_"a[28:31],a[184:187],a[216:219],a[28:31]\n"
W_"6)\n"
T_"a[184:185],v248 offset:224\n"
M_"a[32:35],a[168:171],a[192:195],a[32:35]\n"
W_"6)\n"
L_"v254,s[24:27],s62 offen lds\n"
M_"a[32:35],a[172:175],a[196:199],a[32:35]\n"
A_"m0,s34,106496\n"
T_"a[186:187],v248 offset:4832\n"
"v_bfe_u32 v254,v255,4,4\n"
M_"a[36:39],a[172:175],a[204:207],a[36:39]\n"
S_"s62,4718592\n"
T_"a[190:191],v248 offset:14048\n"
M_"a[36:39],a[168:171],a[200:203],a[36:39]\n"
A_"s24,s82,42991616\n"
"s_addc_u32 s25,s83,0\n"
S_"s26,10027008\n"
M_"a[40:43],a[168:171],a[208:211],a[40:43]\n"
"s_and_b32 s25,s25,0xffff\n"
S_"s27,0x20000\n"
M_"a[40:43],a[172:175],a[212:215],a[40:43]\n"
M_"a[44:47],a[172:175],a[220:223],a[44:47]\n"
T_"a[172:173],v248 offset:27872\n"
M_"a[44:47],a[168:171],a[216:219],a[44:47]\n"
W_"6)\n"
T_"a[168:169],v248 offset:18656\n"
M_"a[48:51],a[176:179],a[192:195],a[48:51]\n"
W_"6)\n"
L_"v251,s[16:19],s62 offen lds\n"
M_"a[48:51],a[180:183],a[196:199],a[48:51]\n"
A_"m0,s34,107520\n"
T_"a[170:171],v248 offset:23264\n"
M_"a[52:55],a[180:183],a[204:207],a[52:55]\n"
S_"s62,4718592\n"
T_"a[174:175],v248 offset:32480\n"
M_"a[52:55],a[176:179],a[200:203],a[52:55]\n"
M_"a[56:59],a[176:179],a[208:211],a[56:59]\n"
M_"a[56:59],a[180:183],a[212:215],a[56:59]\n"
M_"a[60:63],a[180:183],a[220:223],a[60:63]\n"
T_"a[180:181],v248 offset:9472\n"
M_"a[60:63],a[176:179],a[216:219],a[60:63]\n"
W_"6)\n"
T_"a[176:177],v248 offset:256\n"
M_"a[64:67],a[184:187],a[192:195],a[64:67]\n"
W_"6)\n"
L_"v252,s[16:19],s62 offen lds\n"
M_"a[64:67],a[188:191],a[196:199],a[64:67]\n"
A_"m0,s34,124928\n"
T_"a[178:179],v248 offset:4864\n"
M_"a[68:71],a[188:191],a[204:207],a[68:71]\n"
S_"s62,4718592\n"
T_"a[182:183],v248 offset:14080\n"
M_"a[68:71],a[184:187],a[200:203],a[68:71]\n"
M_"a[72:75],a[184:187],a[208:211],a[72:75]\n"
M_"a[72:75],a[188:191],a[212:215],a[72:75]\n"
M_"a[76:79],a[188:191],a[220:223],a[76:79]\n"
T_"a[188:189],v248 offset:27904\n"
M_"a[76:79],a[184:187],a[216:219],a[76:79]\n"
W_"6)\n"
T_"a[184:185],v248 offset:18688\n"
M_"a[80:83],a[168:171],a[192:195],a[80:83]\n"
W_"6)\n"
L_"v251,s[20:23],s62 offen lds\n"
M_"a[80:83],a[172:175],a[196:199],a[80:83]\n"
A_"m0,s34,125952\n"
T_"a[186:187],v248 offset:23296\n"
M_"a[84:87],a[172:175],a[204:207],a[84:87]\n"
S_"s62,4718592\n"
T_"a[190:191],v248 offset:32512\n"
M_"a[84:87],a[168:171],a[200:203],a[84:87]\n"
"v_add_u32 v248,53248,v248\n"
M_"a[88:91],a[168:171],a[208:211],a[88:91]\n"
M_"a[88:91],a[172:175],a[212:215],a[88:91]\n"
M_"a[92:95],a[172:175],a[220:223],a[92:95]\n"
T_"a[172:173],v249 offset:45056\n"
M_"a[92:95],a[168:171],a[216:219],a[92:95]\n"
W_"6)\n"
T_"a[168:169],v249 offset:36864\n"
M_"a[96:99],a[176:179],a[192:195],a[96:99]\n"
W_"6)\n"
L_"v252,s[20:23],s62 offen lds\n"
M_"a[96:99],a[180:183],a[196:199],a[96:99]\n"
A_"m0,s34,115712\n"
T_"a[170:171],v249 offset:40960\n"
M_"a[100:103],a[180:183],a[204:207],a[100:103]\n"
S_"s62,5013504\n"
T_"a[174:175],v249 offset:49152\n"
M_"a[100:103],a[176:179],a[200:203],a[100:103]\n"
M_"a[104:107],a[176:179],a[208:211],a[104:107]\n"
M_"a[104:107],a[180:183],a[212:215],a[104:107]\n"
M_"a[108:111],a[180:183],a[220:223],a[108:111]\n"
T_"a[180:181],v249 offset:45064\n"
M_"a[108:111],a[176:179],a[216:219],a[108:111]\n"
W_"6)\n"
T_"a[176:177],v249 offset:36872\n"
M_"a[112:115],a[184:187],a[192:195],a[112:115]\n"
W_"6)\n"
L_"v251,s[16:19],s62 offen lds\n"
M_"a[112:115],a[188:191],a[196:199],a[112:115]\n"
A_"m0,s34,116736\n"
T_"a[178:179],v249 offset:40968\n"
M_"a[116:119],a[188:191],a[204:207],a[116:119]\n"
S_"s62,5013504\n"
T_"a[182:183],v249 offset:49160\n"
M_"a[116:119],a[184:187],a[200:203],a[116:119]\n"
M_"a[120:123],a[184:187],a[208:211],a[120:123]\n"
M_"a[120:123],a[188:191],a[212:215],a[120:123]\n"
M_"a[124:127],a[188:191],a[220:223],a[124:127]\n"
T_"a[188:189],v250 offset:45056\n"
M_"a[124:127],a[184:187],a[216:219],a[124:127]\n"
W_"6)\n"
T_"a[184:185],v250 offset:36864\n"
M_"v[0:3],a[168:171],a[192:195],v[0:3]\n"
W_"6)\n"
L_"v252,s[16:19],s62 offen lds\n"
M_"v[0:3],a[172:175],a[196:199],v[0:3]\n"
A_"m0,s34,134144\n"
T_"a[186:187],v250 offset:40960\n"
M_"v[4:7],a[172:175],a[204:207],v[4:7]\n"
S_"s62,5013504\n"
T_"a[190:191],v250 offset:49152\n"
M_"v[4:7],a[168:171],a[200:203],v[4:7]\n"
S_"s19,0x20000\n"
M_"v[8:11],a[168:171],a[208:211],v[8:11]\n"
M_"v[8:11],a[172:175],a[212:215],v[8:11]\n"
M_"v[12:15],a[172:175],a[220:223],v[12:15]\n"
T_"a[172:173],v250 offset:45064\n"
M_"v[12:15],a[168:171],a[216:219],v[12:15]\n"
W_"6)\n"
T_"a[168:169],v250 offset:36872\n"
M_"v[64:67],a[176:179],a[192:195],v[64:67]\n"
W_"6)\n"
L_"v251,s[20:23],s62 offen lds\n"
M_"v[64:67],a[180:183],a[196:199],v[64:67]\n"
A_"m0,s34,135168\n"
S_"s62,5013504\n"
T_"a[170:171],v250 offset:40968\n"
M_"v[68:71],a[180:183],a[204:207],v[68:71]\n"
T_"a[174:175],v250 offset:49160\n"
M_"v[68:71],a[176:179],a[200:203],v[68:71]\n"
"v_xor_b32 v250,128,v249\n"
M_"v[72:75],a[176:179],a[208:211],v[72:75]\n"
M_"v[72:75],a[180:183],a[212:215],v[72:75]\n"
M_"v[76:79],a[180:183],a[220:223],v[76:79]\n"
T_"a[180:181],v250 offset:45056\n"
M_"v[76:79],a[176:179],a[216:219],v[76:79]\n"
W_"6)\n"
T_"a[176:177],v250 offset:36864\n"
M_"v[16:19],a[184:187],a[192:195],v[16:19]\n"
W_"6)\n"
L_"v252,s[20:23],s62 offen lds\n"
M_"v[16:19],a[188:191],a[196:199],v[16:19]\n"
A_"m0,s35,106496\n"
S_"s62,4718592\n"
T_"a[178:179],v250 offset:40960\n"
M_"v[20:23],a[188:191],a[204:207],v[20:23]\n"
"s_lshl_b32 s35,s33,12\n"
T_"a[182:183],v250 offset:49152\n"
M_"v[20:23],a[184:187],a[200:203],v[20:23]\n"
A_"s35,s35,114688\n"
S_"s22,35744\n"
S_"s23,0x20000\n"
M_"v[24:27],a[184:187],a[208:211],v[24:27]\n"
"s_cmp_lt_u32 s76,576\n"
"s_cselect_b32 s77,s77,1073741824\n"
"s_cselect_b32 s22,s22,0\n"
M_"v[24:27],a[188:191],a[212:215],v[24:27]\n"
M_"v[28:31],a[188:191],a[220:223],v[28:31]\n"
T_"a[188:189],v250 offset:45064\n"
M_"v[28:31],a[184:187],a[216:219],v[28:31]\n"
W_"6)\n"
T_"a[184:185],v250 offset:36872\n"
M_"v[80:83],a[168:171],a[192:195],v[80:83]\n"
W_"6)\n"
L_"v253,s[28:31],s62 offen lds\n"
M_"v[80:83],a[172:175],a[196:199],v[80:83]\n"
A_"m0,s61,0\n"
T_"a[186:187],v250 offset:40968\n"
"v_lshrrev_b32 v253,8,v255\n"
M_"v[84:87],a[172:175],a[204:207],v[84:87]\n"
A_"s62,s84,4096\n"
T_"a[190:191],v250 offset:49160\n"
"v_add_u32 v253,s88,v253\n"
M_"v[84:87],a[168:171],a[200:203],v[84:87]\n"
"s_lshr_b32 s88,s76,2\n"
"v_xor_b32 v250,192,v249\n"
"v_xor_b32 v254,v254,v253\n"
M_"v[88:91],a[168:171],a[208:211],v[88:91]\n"
"v_lshlrev_b32 v254,4,v254\n"
"v_add_u32 v249,53248,v249\n"
M_"v[88:91],a[172:175],a[212:215],v[88:91]\n"
"v_lshl_add_u32 v251,v253,8,v254\n"
"v_bfe_u32 v253,v255,8,1\n"
M_"v[92:95],a[172:175],a[220:223],v[92:95]\n"
T_"a[172:173],v250 offset:45056\n"
"v_lshlrev_b32 v253,1,v253\n"
M_"v[92:95],a[168:171],a[216:219],v[92:95]\n"
W_"6)\n"
T_"a[168:169],v250 offset:36864\n"
"v_bfe_u32 v254,v255,4,2\n"
M_"v[32:35],a[176:179],a[192:195],v[32:35]\n"
W_"6)\n"
T_"a[170:171],v250 offset:40960\n"
"v_xor_b32 v254,v254,v253\n"
M_"v[32:35],a[180:183],a[196:199],v[32:35]\n"
T_"a[174:175],v250 offset:49152\n"
"v_lshlrev_b32 v254,4,v254\n"
M_"v[36:39],a[180:183],a[204:207],v[36:39]\n"
"v_lshrrev_b32 v253,9,v255\n"
"v_lshl_add_u32 v254,v253,6,v254\n"
M_"v[36:39],a[176:179],a[200:203],v[36:39]\n"
"v_bfe_u32 v253,v255,6,3\n"
"v_add_u32 v253,s89,v253\n"
M_"v[40:43],a[176:179],a[208:211],v[40:43]\n"
"s_and_b32 s89,s76,3\n"
"v_lshl_add_u32 v252,v253,8,v254\n"
"v_add_u32 v254,53248,v248\n"
M_"v[40:43],a[180:183],a[212:215],v[40:43]\n"
"s_mul_i32 s89,s89,288\n"
"v_add_u32 v253,53248,v249\n"
M_"v[44:47],a[180:183],a[220:223],v[44:47]\n"
T_"a[180:181],v250 offset:45064\n"
M_"v[44:47],a[176:179],a[216:219],v[44:47]\n"
W_"6)\n"
T_"a[176:177],v250 offset:36872\n"
M_"v[96:99],a[184:187],a[192:195],v[96:99]\n"
W_"6)\n"
T_"a[178:179],v250 offset:40968\n"
M_"v[96:99],a[188:191],a[196:199],v[96:99]\n"
T_"a[182:183],v250 offset:49160\n"
M_"v[100:103],a[188:191],a[204:207],v[100:103]\n"
"v_xor_b32 v250,64,v249\n"
M_"v[100:103],a[184:187],a[200:203],v[100:103]\n"
"s_cmp_lt_u32 s88,144\n"
"s_cselect_b64 s[16:17],s[78:79],s[72:73]\n"
"s_cselect_b32 s18,0,144\n"
M_"v[104:107],a[184:187],a[208:211],v[104:107]\n"
M_"v[104:107],a[188:191],a[212:215],v[104:107]\n"
M_"v[108:111],a[188:191],a[220:223],v[108:111]\n"
M_"v[108:111],a[184:187],a[216:219],v[108:111]\n"
W_"5)\n"
M_"v[48:51],a[168:171],a[192:195],v[48:51]\n"
W_"4)\n"
M_"v[48:51],a[172:175],a[196:199],v[48:51]\n"
M_"v[52:55],a[172:175],a[204:207],v[52:55]\n"
"s_waitcnt vmcnt(21) lgkmcnt(0)\n"
"s_barrier\n"
T_"a[184:185],v248 offset:0\n"
M_"v[52:55],a[168:171],a[200:203],v[52:55]\n"
T_"a[186:187],v248 offset:4608\n"
M_"v[56:59],a[168:171],a[208:211],v[56:59]\n"
T_"a[188:189],v248 offset:9216\n"
M_"v[56:59],a[172:175],a[212:215],v[56:59]\n"
T_"a[190:191],v248 offset:13824\n"
M_"v[60:63],a[172:175],a[220:223],v[60:63]\n"
T_"a[172:173],v248 offset:27648\n"
M_"v[60:63],a[168:171],a[216:219],v[60:63]\n"
T_"a[168:169],v248 offset:18432\n"
M_"v[112:115],a[176:179],a[192:195],v[112:115]\n"
T_"a[170:171],v248 offset:23040\n"
M_"v[112:115],a[180:183],a[196:199],v[112:115]\n"
T_"a[174:175],v248 offset:32256\n"
M_"v[116:119],a[180:183],a[204:207],v[116:119]\n"
M_"v[116:119],a[176:179],a[200:203],v[116:119]\n"
M_"v[120:123],a[176:179],a[208:211],v[120:123]\n"
M_"v[120:123],a[180:183],a[212:215],v[120:123]\n"
M_"v[124:127],a[180:183],a[220:223],v[124:127]\n"
T_"a[180:181],v248 offset:9248\n"
M_"v[124:127],a[176:179],a[216:219],v[124:127]\n"
"s_waitcnt vmcnt(20) lgkmcnt(7)\n"
T_"a[176:177],v248 offset:32\n"
M_"v[128:131],a[184:187],a[224:227],v[128:131]\n"
"s_waitcnt vmcnt(19) lgkmcnt(6)\n"
L_"a[192:195],v255,s[36:39],s63 offen sc1\n"
M_"v[128:131],a[188:191],a[228:231],v[128:131]\n"
"s_waitcnt vmcnt(18)\n"
T_"a[178:179],v248 offset:4640\n"
M_"v[132:135],a[188:191],a[236:239],v[132:135]\n"
T_"a[182:183],v248 offset:13856\n"
M_"v[132:135],a[184:187],a[232:235],v[132:135]\n"
"s_waitcnt vmcnt(17)\n"
M_"v[136:139],a[184:187],a[240:243],v[136:139]\n"
"s_waitcnt vmcnt(16)\n"
L_"a[196:199],v255,s[36:39],s63 offen offset:1024 sc1\n"
M_"v[136:139],a[188:191],a[244:247],v[136:139]\n"
"s_waitcnt vmcnt(15)\n"
S_"s63,34816\n"
M_"v[140:143],a[188:191],a[252:255],v[140:143]\n"
T_"a[188:189],v248 offset:27680\n"
M_"v[140:143],a[184:187],a[248:251],v[140:143]\n"
W_"6)\n"
T_"a[184:185],v248 offset:18464\n"
M_"v[144:147],a[168:171],a[224:227],v[144:147]\n"
W_"6)\n"
L_"a[200:203],v255,s[36:39],s63 offen sc1\n"
M_"v[144:147],a[172:175],a[228:231],v[144:147]\n"
T_"a[186:187],v248 offset:23072\n"
M_"v[148:151],a[172:175],a[236:239],v[148:151]\n"
T_"a[190:191],v248 offset:32288\n"
M_"v[148:151],a[168:171],a[232:235],v[148:151]\n"
M_"v[152:155],a[168:171],a[240:243],v[152:155]\n"
L_"a[204:207],v255,s[36:39],s63 offen offset:1024 sc1\n"
M_"v[152:155],a[172:175],a[244:247],v[152:155]\n"
S_"s63,53248\n"
M_"v[156:159],a[172:175],a[252:255],v[156:159]\n"
T_"a[172:173],v248 offset:9344\n"
M_"v[156:159],a[168:171],a[248:251],v[156:159]\n"
W_"6)\n"
T_"a[168:169],v248 offset:128\n"
M_"v[160:163],a[176:179],a[224:227],v[160:163]\n"
W_"6)\n"
L_"a[208:211],v255,s[36:39],s63 offen sc1\n"
M_"v[160:163],a[180:183],a[228:231],v[160:163]\n"
T_"a[170:171],v248 offset:4736\n"
M_"v[164:167],a[180:183],a[236:239],v[164:167]\n"
T_"a[174:175],v248 offset:13952\n"
M_"v[164:167],a[176:179],a[232:235],v[164:167]\n"
M_"v[168:171],a[176:179],a[240:243],v[168:171]\n"
L_"a[212:215],v255,s[36:39],s63 offen offset:1024 sc1\n"
M_"v[168:171],a[180:183],a[244:247],v[168:171]\n"
S_"s63,71680\n"
M_"v[172:175],a[180:183],a[252:255],v[172:175]\n"
T_"a[180:181],v248 offset:27776\n"
M_"v[172:175],a[176:179],a[248:251],v[172:175]\n"
W_"6)\n"
T_"a[176:177],v248 offset:18560\n"
M_"v[176:179],a[184:187],a[224:227],v[176:179]\n"
W_"6)\n"
L_"a[216:219],v255,s[36:39],s63 offen sc1\n"
M_"v[176:179],a[188:191],a[228:231],v[176:179]\n"
T_"a[178:179],v248 offset:23168\n"
M_"v[180:183],a[188:191],a[236:239],v[180:183]\n"
T_"a[182:183],v248 offset:32384\n"
M_"v[180:183],a[184:187],a[232:235],v[180:183]\n"
M_"v[184:187],a[184:187],a[240:243],v[184:187]\n"
L_"a[220:223],v255,s[36:39],s63 offen offset:1024 sc1\n"
M_"v[184:187],a[188:191],a[244:247],v[184:187]\n"
A_"s63,s85,128\n"
M_"v[188:191],a[188:191],a[252:255],v[188:191]\n"
T_"a[188:189],v248 offset:9376\n"
M_"v[188:191],a[184:187],a[248:251],v[188:191]\n"
W_"6)\n"
T_"a[184:185],v248 offset:160\n"
M_"a[136:139],a[168:171],a[224:227],a[136:139]\n"
W_"6)\n"
L_"v251,s[40:43],s84 offen lds\n"
M_"a[136:139],a[172:175],a[228:231],a[136:139]\n"
A_"m0,s61,4096\n"
T_"a[186:187],v248 offset:4768\n"
M_"a[140:143],a[172:175],a[236:239],a[140:143]\n"
T_"a[190:191],v248 offset:13984\n"
M_"a[140:143],a[168:171],a[232:235],a[140:143]\n"
M_"a[144:147],a[168:171],a[240:243],a[144:147]\n"
M_"a[144:147],a[172:175],a[244:247],a[144:147]\n"
M_"a[148:151],a[172:175],a[252:255],a[148:151]\n"
T_"a[172:173],v248 offset:27808\n"
M_"a[148:151],a[168:171],a[248:251],a[148:151]\n"
W_"6)\n"
T_"a[168:169],v248 offset:18592\n"
M_"a[152:155],a[176:179],a[224:227],a[152:155]\n"
W_"6)\n"
T_"a[170:171],v248 offset:23200\n"
M_"a[152:155],a[180:183],a[228:231],a[152:155]\n"
L_"v251,s[40:43],s62 offen lds\n"
M_"a[156:159],a[180:183],a[236:239],a[156:159]\n"
A_"m0,s61,8192\n"
T_"a[174:175],v248 offset:32416\n"
M_"a[156:159],a[176:179],a[232:235],a[156:159]\n"
A_"s62,s84,8192\n"
M_"a[160:163],a[176:179],a[240:243],a[160:163]\n"
M_"a[160:163],a[180:183],a[244:247],a[160:163]\n"
M_"a[164:167],a[180:183],a[252:255],a[164:167]\n"
M_"a[164:167],a[176:179],a[248:251],a[164:167]\n"
W_"5)\n"
M_"a[0:3],a[184:187],a[224:227],a[0:3]\n"
W_"4)\n"
M_"a[0:3],a[188:191],a[228:231],a[0:3]\n"
M_"a[4:7],a[188:191],a[236:239],a[4:7]\n"
"s_waitcnt vmcnt(10) lgkmcnt(0)\n"
"s_barrier\n"
T_"a[176:177],v254 offset:0\n"
M_"a[4:7],a[184:187],a[232:235],a[4:7]\n"
L_"v251,s[40:43],s62 offen lds\n"
M_"a[8:11],a[184:187],a[240:243],a[8:11]\n"
A_"m0,s61,12288\n"
T_"a[178:179],v254 offset:4608\n"
M_"a[8:11],a[188:191],a[244:247],a[8:11]\n"
A_"s62,s84,12288\n"
T_"a[180:181],v254 offset:9216\n"
M_"a[12:15],a[188:191],a[252:255],a[12:15]\n"
A_"s84,s84,0x4000\n"
T_"a[182:183],v254 offset:13824\n"
M_"a[12:15],a[184:187],a[248:251],a[12:15]\n"
T_"a[184:185],v248 offset:192\n"
M_"a[16:19],a[168:171],a[224:227],a[16:19]\n"
T_"a[186:187],v248 offset:4800\n"
M_"a[16:19],a[172:175],a[228:231],a[16:19]\n"
T_"a[188:189],v248 offset:9408\n"
M_"a[20:23],a[172:175],a[236:239],a[20:23]\n"
T_"a[190:191],v248 offset:14016\n"
M_"a[20:23],a[168:171],a[232:235],a[20:23]\n"
L_"v251,s[40:43],s62 offen lds\n"
M_"a[24:27],a[168:171],a[240:243],a[24:27]\n"
A_"m0,s60,16384\n"
A_"s62,s84,4096\n"
M_"a[24:27],a[172:175],a[244:247],a[24:27]\n"
M_"a[28:31],a[172:175],a[252:255],a[28:31]\n"
T_"a[172:173],v254 offset:27648\n"
M_"a[28:31],a[168:171],a[248:251],a[28:31]\n"
"s_waitcnt vmcnt(11) lgkmcnt(7)\n"
T_"a[168:169],v254 offset:18432\n"
M_"v[128:131],a[176:179],a[192:195],v[128:131]\n"
"s_waitcnt vmcnt(10) lgkmcnt(6)\n"
T_"a[170:171],v254 offset:23040\n"
M_"v[128:131],a[180:183],a[196:199],v[128:131]\n"
"s_waitcnt vmcnt(8)\n"
T_"a[174:175],v254 offset:32256\n"
M_"v[132:135],a[180:183],a[204:207],v[132:135]\n"
M_"v[132:135],a[176:179],a[200:203],v[132:135]\n"
"s_waitcnt vmcnt(7)\n"
M_"v[136:139],a[176:179],a[208:211],v[136:139]\n"
"s_waitcnt vmcnt(6)\n"
L_"v252,s[44:47],s85 offen lds\n"
M_"v[136:139],a[180:183],a[212:215],v[136:139]\n"
"s_waitcnt vmcnt(5)\n"
M_"v[140:143],a[180:183],a[220:223],v[140:143]\n"
T_"a[180:181],v248 offset:27840\n"
M_"v[140:143],a[176:179],a[216:219],v[140:143]\n"
W_"7)\n"
T_"a[176:177],v248 offset:18624\n"
M_"a[32:35],a[184:187],a[224:227],a[32:35]\n"
W_"6)\n"
T_"a[178:179],v248 offset:23232\n"
M_"a[32:35],a[188:191],a[228:231],a[32:35]\n"
T_"a[182:183],v248 offset:32448\n"
M_"a[36:39],a[188:191],a[236:239],a[36:39]\n"
M_"a[36:39],a[184:187],a[232:235],a[36:39]\n"
M_"a[40:43],a[184:187],a[240:243],a[40:43]\n"
M_"a[40:43],a[188:191],a[244:247],a[40:43]\n"
L_"v252,s[44:47],s85 offen offset:2048 lds\n"
M_"a[44:47],a[188:191],a[252:255],a[44:47]\n"
A_"m0,s60,17408\n"
T_"a[188:189],v254 offset:9248\n"
M_"a[44:47],a[184:187],a[248:251],a[44:47]\n"
W_"6)\n"
T_"a[184:185],v254 offset:32\n"
M_"v[144:147],a[168:171],a[192:195],v[144:147]\n"
W_"6)\n"
T_"a[186:187],v254 offset:4640\n"
M_"v[144:147],a[172:175],a[196:199],v[144:147]\n"
A_"s85,s85,0x4000\n"
T_"a[190:191],v254 offset:13856\n"
M_"v[148:151],a[172:175],a[204:207],v[148:151]\n"
M_"v[148:151],a[168:171],a[200:203],v[148:151]\n"
M_"v[152:155],a[168:171],a[208:211],v[152:155]\n"
M_"v[152:155],a[172:175],a[212:215],v[152:155]\n"
M_"v[156:159],a[172:175],a[220:223],v[156:159]\n"
L_"v252,s[44:47],s63 offen lds\n"
M_"v[156:159],a[168:171],a[216:219],v[156:159]\n"
W_"5)\n"
T_"a[168:169],v248 offset:224\n"
U_"v128,v128,v144\n"
M_"a[48:51],a[176:179],a[224:227],a[48:51]\n"
W_"5)\n"
T_"a[170:171],v248 offset:4832\n"
U_"v129,v129,v145\n"
M_"a[48:51],a[180:183],a[228:231],a[48:51]\n"
T_"a[172:173],v248 offset:9440\n"
U_"v140,v140,v156\n"
M_"a[52:55],a[180:183],a[236:239],a[52:55]\n"
T_"a[174:175],v248 offset:14048\n"
U_"v141,v141,v157\n"
M_"a[52:55],a[176:179],a[232:235],a[52:55]\n"
"v_bfe_u32 v156,v255,4,4\n"
"v_lshrrev_b32 v157,8,v255\n"
M_"a[56:59],a[176:179],a[240:243],a[56:59]\n"
"v_xor_b32 v157,v157,v156\n"
U_"v132,v132,v148\n"
M_"a[56:59],a[180:183],a[244:247],a[56:59]\n"
"v_lshlrev_b32 v157,4,v157\n"
U_"v133,v133,v149\n"
M_"a[60:63],a[180:183],a[252:255],a[60:63]\n"
T_"a[180:181],v254 offset:27680\n"
U_"v136,v136,v152\n"
M_"a[60:63],a[176:179],a[248:251],a[60:63]\n"
W_"6)\n"
L_"v252,s[44:47],s63 offen offset:2048 lds\n"
M_"v[160:163],a[184:187],a[192:195],v[160:163]\n"
W_"5)\n"
T_"a[176:177],v254 offset:18464\n"
U_"v137,v137,v153\n"
M_"v[160:163],a[188:191],a[196:199],v[160:163]\n"
A_"m0,s61,32768\n"
T_"a[178:179],v254 offset:23072\n"
"v_lshl_add_u32 v144,v156,8,v157\n"
M_"v[164:167],a[188:191],a[204:207],v[164:167]\n"
A_"s63,s85,128\n"
T_"a[182:183],v254 offset:32288\n"
U_"v130,v130,v146\n"
M_"v[164:167],a[184:187],a[200:203],v[164:167]\n"
U_"v131,v131,v147\n"
C_"v128,v128,v129\n"
M_"v[168:171],a[184:187],a[208:211],v[168:171]\n"
U_"v134,v134,v150\n"
U_"v135,v135,v151\n"
M_"v[168:171],a[188:191],a[212:215],v[168:171]\n"
C_"v132,v132,v133\n"
U_"v138,v138,v154\n"
M_"v[172:175],a[188:191],a[220:223],v[172:175]\n"
T_"a[188:189],v248 offset:27872\n"
U_"v139,v139,v155\n"
M_"v[172:175],a[184:187],a[216:219],v[172:175]\n"
W_"7)\n"
T_"a[184:185],v248 offset:18656\n"
C_"v136,v136,v137\n"
M_"a[64:67],a[168:171],a[224:227],a[64:67]\n"
W_"6)\n"
L_"v251,s[40:43],s84 offen lds\n"
M_"a[64:67],a[172:175],a[228:231],a[64:67]\n"
A_"m0,s61,36864\n"
T_"a[186:187],v248 offset:23264\n"
U_"v142,v142,v158\n"
M_"a[68:71],a[172:175],a[236:239],a[68:71]\n"
T_"a[190:191],v248 offset:32480\n"
U_"v143,v143,v159\n"
M_"a[68:71],a[168:171],a[232:235],a[68:71]\n"
C_"v140,v140,v141\n"
"v_xor_b32 v145,64,v144\n"
M_"a[72:75],a[168:171],a[240:243],a[72:75]\n"
C_"v129,v130,v131\n"
C_"v133,v134,v135\n"
M_"a[72:75],a[172:175],a[244:247],a[72:75]\n"
C_"v137,v138,v139\n"
C_"v141,v142,v143\n"
M_"a[76:79],a[172:175],a[252:255],a[76:79]\n"
T_"a[172:173],v254 offset:9344\n"
"v_xor_b32 v146,128,v144\n"
M_"a[76:79],a[168:171],a[248:251],a[76:79]\n"
W_"6)\n"
T_"a[168:169],v254 offset:128\n"
"v_xor_b32 v147,192,v144\n"
M_"v[176:179],a[176:179],a[192:195],v[176:179]\n"
W_"6)\n"
T_"a[170:171],v254 offset:4736\n"
"v_bfe_u32 v156,v255,9,1\n"
M_"v[176:179],a[180:183],a[196:199],v[176:179]\n"
L_"v251,s[40:43],s62 offen lds\n"
M_"v[180:183],a[180:183],a[204:207],v[180:183]\n"
A_"m0,s61,40960\n"
T_"a[174:175],v254 offset:13952\n"
"v_lshlrev_b32 v156,11,v156\n"
M_"v[180:183],a[176:179],a[200:203],v[180:183]\n"
A_"s62,s84,8192\n"
"v_bfe_u32 v157,v255,6,3\n"
"v_lshrrev_b32 v158,1,v255\n"
M_"v[184:187],a[176:179],a[208:211],v[184:187]\n"
U_"v160,v160,v176\n"
U_"v161,v161,v177\n"
M_"v[184:187],a[180:183],a[212:215],v[184:187]\n"
U_"v162,v162,v178\n"
U_"v163,v163,v179\n"
M_"v[188:191],a[180:183],a[220:223],v[188:191]\n"
T_"a[180:181],v248 offset:9472\n"
U_"v164,v164,v180\n"
M_"v[188:191],a[176:179],a[216:219],v[188:191]\n"
W_"6)\n"
T_"a[176:177],v248 offset:256\n"
U_"v165,v165,v181\n"
M_"a[80:83],a[184:187],a[224:227],a[80:83]\n"
W_"6)\n"
T_"a[178:179],v248 offset:4864\n"
U_"v166,v166,v182\n"
M_"a[80:83],a[188:191],a[228:231],a[80:83]\n"
T_"a[182:183],v248 offset:14080\n"
U_"v167,v167,v183\n"
M_"a[84:87],a[188:191],a[236:239],a[84:87]\n"
L_"v251,s[40:43],s62 offen lds\n"
M_"a[84:87],a[184:187],a[232:235],a[84:87]\n"
A_"m0,s61,45056\n"
U_"v168,v168,v184\n"
U_"v169,v169,v185\n"
M_"a[88:91],a[184:187],a[240:243],a[88:91]\n"
A_"s62,s84,12288\n"
U_"v170,v170,v186\n"
U_"v171,v171,v187\n"
M_"a[88:91],a[188:191],a[244:247],a[88:91]\n"
A_"s84,s84,0x4000\n"
U_"v172,v172,v188\n"
U_"v173,v173,v189\n"
M_"a[92:95],a[188:191],a[252:255],a[92:95]\n"
T_"a[188:189],v254 offset:27776\n"
U_"v174,v174,v190\n"
M_"a[92:95],a[184:187],a[248:251],a[92:95]\n"
W_"6)\n"
T_"a[184:185],v254 offset:18560\n"
U_"v175,v175,v191\n"
M_"a[136:139],a[168:171],a[192:195],a[136:139]\n"
W_"6)\n"
T_"a[186:187],v254 offset:23168\n"
C_"v130,v160,v161\n"
M_"a[136:139],a[172:175],a[196:199],a[136:139]\n"
T_"a[190:191],v254 offset:32384\n"
C_"v131,v162,v163\n"
M_"a[140:143],a[172:175],a[204:207],a[140:143]\n"
O_"v[128:131],v255,s[48:51],s90 offen sc1\n"
M_"a[140:143],a[168:171],a[200:203],a[140:143]\n"
L_"v251,s[40:43],s62 offen lds\n"
M_"a[144:147],a[168:171],a[208:211],a[144:147]\n"
A_"m0,s61,65536\n"
C_"v134,v164,v165\n"
C_"v135,v166,v167\n"
M_"a[144:147],a[172:175],a[212:215],a[144:147]\n"
A_"s62,s84,4096\n"
O_"v[132:135],v255,s[48:51],s91 offen sc1\n"
M_"a[148:151],a[172:175],a[220:223],a[148:151]\n"
T_"a[172:173],v248 offset:27904\n"
C_"v138,v168,v169\n"
M_"a[148:151],a[168:171],a[216:219],a[148:151]\n"
W_"6)\n"
T_"a[168:169],v248 offset:18688\n"
C_"v139,v170,v171\n"
M_"a[96:99],a[176:179],a[224:227],a[96:99]\n"
W_"6)\n"
T_"a[170:171],v248 offset:23296\n"
C_"v142,v172,v173\n"
M_"a[96:99],a[180:183],a[228:231],a[96:99]\n"
T_"a[174:175],v248 offset:32512\n"
C_"v143,v174,v175\n"
M_"a[100:103],a[180:183],a[236:239],a[100:103]\n"
O_"v[136:139],v255,s[48:51],s92 offen sc1\n"
M_"a[100:103],a[176:179],a[232:235],a[100:103]\n"
O_"v[140:143],v255,s[48:51],s93 offen sc1\n"
M_"a[104:107],a[176:179],a[240:243],a[104:107]\n"
R_"v160,a136\n"
R_"v161,a137\n"
M_"a[104:107],a[180:183],a[244:247],a[104:107]\n"
R_"v162,a138\n"
R_"v163,a139\n"
M_"a[108:111],a[180:183],a[252:255],a[108:111]\n"
T_"a[180:181],v254 offset:9376\n"
R_"v168,a140\n"
M_"a[108:111],a[176:179],a[248:251],a[108:111]\n"
W_"6)\n"
T_"a[176:177],v254 offset:160\n"
R_"v169,a141\n"
M_"a[152:155],a[184:187],a[192:195],a[152:155]\n"
W_"6)\n"
T_"a[178:179],v254 offset:4768\n"
R_"v170,a142\n"
M_"a[152:155],a[188:191],a[196:199],a[152:155]\n"
T_"a[182:183],v254 offset:13984\n"
R_"v171,a143\n"
M_"a[156:159],a[188:191],a[204:207],a[156:159]\n"
R_"v176,a144\n"
R_"v177,a145\n"
M_"a[156:159],a[184:187],a[200:203],a[156:159]\n"
R_"v178,a146\n"
R_"v179,a147\n"
M_"a[160:163],a[184:187],a[208:211],a[160:163]\n"
R_"v164,a152\n"
R_"v165,a153\n"
M_"a[160:163],a[188:191],a[212:215],a[160:163]\n"
R_"v166,a154\n"
R_"v167,a155\n"
M_"a[164:167],a[188:191],a[220:223],a[164:167]\n"
T_"a[188:189],v248 offset:9280\n"
R_"v172,a156\n"
M_"a[164:167],a[184:187],a[216:219],a[164:167]\n"
W_"6)\n"
T_"a[184:185],v248 offset:64\n"
R_"v173,a157\n"
M_"a[112:115],a[168:171],a[224:227],a[112:115]\n"
W_"6)\n"
T_"a[186:187],v248 offset:4672\n"
R_"v174,a158\n"
M_"a[112:115],a[172:175],a[228:231],a[112:115]\n"
T_"a[190:191],v248 offset:13888\n"
R_"v175,a159\n"
M_"a[116:119],a[172:175],a[236:239],a[116:119]\n"
U_"v160,v160,v164\n"
U_"v161,v161,v165\n"
M_"a[116:119],a[168:171],a[232:235],a[116:119]\n"
U_"v162,v162,v166\n"
U_"v163,v163,v167\n"
M_"a[120:123],a[168:171],a[240:243],a[120:123]\n"
U_"v168,v168,v172\n"
U_"v169,v169,v173\n"
M_"a[120:123],a[172:175],a[244:247],a[120:123]\n"
U_"v170,v170,v174\n"
U_"v171,v171,v175\n"
M_"a[124:127],a[172:175],a[252:255],a[124:127]\n"
T_"a[172:173],v254 offset:27808\n"
C_"v160,v160,v161\n"
M_"a[124:127],a[168:171],a[248:251],a[124:127]\n"
W_"6)\n"
T_"a[168:169],v254 offset:18592\n"
R_"v180,a160\n"
M_"a[0:3],a[176:179],a[192:195],a[0:3]\n"
W_"6)\n"
T_"a[170:171],v254 offset:23200\n"
R_"v181,a161\n"
M_"a[0:3],a[180:183],a[196:199],a[0:3]\n"
T_"a[174:175],v254 offset:32416\n"
R_"v182,a162\n"
M_"a[4:7],a[180:183],a[204:207],a[4:7]\n"
R_"v183,a163\n"
R_"v184,a148\n"
M_"a[4:7],a[176:179],a[200:203],a[4:7]\n"
R_"v185,a149\n"
R_"v186,a150\n"
M_"a[8:11],a[176:179],a[208:211],a[8:11]\n"
R_"v187,a151\n"
R_"v188,a164\n"
M_"a[8:11],a[180:183],a[212:215],a[8:11]\n"
R_"v189,a165\n"
R_"v190,a166\n"
M_"a[12:15],a[180:183],a[220:223],a[12:15]\n"
T_"a[180:181],v248 offset:27712\n"
R_"v191,a167\n"
M_"a[12:15],a[176:179],a[216:219],a[12:15]\n"
W_"6)\n"
T_"a[176:177],v248 offset:18496\n"
R_"v164,a0\n"
M_"v[192:195],a[184:187],a[224:227],v[192:195]\n"
W_"6)\n"
T_"a[178:179],v248 offset:23104\n"
R_"v165,a1\n"
M_"v[192:195],a[188:191],a[228:231],v[192:195]\n"
T_"a[182:183],v248 offset:32320\n"
R_"v166,a2\n"
M_"v[196:199],a[188:191],a[236:239],v[196:199]\n"
R_"v167,a3\n"
C_"v161,v162,v163\n"
M_"v[196:199],a[184:187],a[232:235],v[196:199]\n"
U_"v176,v176,v180\n"
U_"v177,v177,v181\n"
M_"v[200:203],a[184:187],a[240:243],v[200:203]\n"
U_"v178,v178,v182\n"
U_"v179,v179,v183\n"
M_"v[200:203],a[188:191],a[244:247],v[200:203]\n"
U_"v184,v184,v188\n"
U_"v185,v185,v189\n"
M_"v[204:207],a[188:191],a[252:255],v[204:207]\n"
T_"a[188:189],v254 offset:9408\n"
U_"v186,v186,v190\n"
M_"v[204:207],a[184:187],a[248:251],v[204:207]\n"
W_"6)\n"
T_"a[184:185],v254 offset:192\n"
U_"v187,v187,v191\n"
M_"a[16:19],a[168:171],a[192:195],a[16:19]\n"
W_"6)\n"
T_"a[186:187],v254 offset:4800\n"
C_"v168,v168,v169\n"
M_"a[16:19],a[172:175],a[196:199],a[16:19]\n"
T_"a[190:191],v254 offset:14016\n"
R_"v180,a4\n"
M_"a[20:23],a[172:175],a[204:207],a[20:23]\n"
R_"v181,a5\n"
R_"v182,a6\n"
M_"a[20:23],a[168:171],a[200:203],a[20:23]\n"
R_"v183,a7\n"
C_"v169,v170,v171\n"
M_"a[24:27],a[168:171],a[208:211],a[24:27]\n"
R_"v172,a16\n"
R_"v173,a17\n"
M_"a[24:27],a[172:175],a[212:215],a[24:27]\n"
R_"v174,a18\n"
R_"v175,a19\n"
M_"a[28:31],a[172:175],a[220:223],a[28:31]\n"
T_"a[172:173],v248 offset:9312\n"
U_"v164,v164,v172\n"
M_"a[28:31],a[168:171],a[216:219],a[28:31]\n"
W_"6)\n"
T_"a[168:169],v248 offset:96\n"
U_"v165,v165,v173\n"
M_"v[208:211],a[176:179],a[224:227],v[208:211]\n"
W_"6)\n"
T_"a[170:171],v248 offset:4704\n"
U_"v166,v166,v174\n"
M_"v[208:211],a[180:183],a[228:231],v[208:211]\n"
T_"a[174:175],v248 offset:13920\n"
U_"v167,v167,v175\n"
M_"v[212:215],a[180:183],a[236:239],v[212:215]\n"
C_"v162,v164,v165\n"
C_"v163,v166,v167\n"
M_"v[212:215],a[176:179],a[232:235],v[212:215]\n"
O_"v[160:163],v255,s[48:51],s90 offen offset:2048 sc1\n"
M_"v[216:219],a[176:179],a[240:243],v[216:219]\n"
R_"v188,a20\n"
R_"v189,a21\n"
M_"v[216:219],a[180:183],a[244:247],v[216:219]\n"
R_"v190,a22\n"
R_"v191,a23\n"
M_"v[220:223],a[180:183],a[252:255],v[220:223]\n"
T_"a[180:181],v254 offset:27840\n"
U_"v180,v180,v188\n"
M_"v[220:223],a[176:179],a[248:251],v[220:223]\n"
W_"6)\n"
T_"a[176:177],v254 offset:18624\n"
U_"v181,v181,v189\n"
M_"a[32:35],a[184:187],a[192:195],a[32:35]\n"
W_"6)\n"
T_"a[178:179],v254 offset:23232\n"
U_"v182,v182,v190\n"
M_"a[32:35],a[188:191],a[196:199],a[32:35]\n"
T_"a[182:183],v254 offset:32448\n"
U_"v183,v183,v191\n"
M_"a[36:39],a[188:191],a[204:207],a[36:39]\n"
C_"v176,v176,v177\n"
C_"v170,v180,v181\n"
M_"a[36:39],a[184:187],a[200:203],a[36:39]\n"
C_"v171,v182,v183\n"
R_"v172,a8\n"
M_"a[40:43],a[184:187],a[208:211],a[40:43]\n"
O_"v[168:171],v255,s[48:51],s91 offen offset:2048 sc1\n"
M_"a[40:43],a[188:191],a[212:215],a[40:43]\n"
R_"v173,a9\n"
R_"v174,a10\n"
M_"a[44:47],a[188:191],a[220:223],a[44:47]\n"
T_"a[188:189],v248 offset:27744\n"
R_"v175,a11\n"
M_"a[44:47],a[184:187],a[216:219],a[44:47]\n"
W_"6)\n"
T_"a[184:185],v248 offset:18528\n"
R_"v164,a24\n"
M_"v[224:227],a[168:171],a[224:227],v[224:227]\n"
W_"6)\n"
T_"a[186:187],v248 offset:23136\n"
R_"v165,a25\n"
M_"v[224:227],a[172:175],a[228:231],v[224:227]\n"
T_"a[190:191],v248 offset:32352\n"
R_"v166,a26\n"
M_"v[228:231],a[172:175],a[236:239],v[228:231]\n"
R_"v167,a27\n"
C_"v177,v178,v179\n"
M_"v[228:231],a[168:171],a[232:235],v[228:231]\n"
C_"v184,v184,v185\n"
U_"v172,v172,v164\n"
M_"v[232:235],a[168:171],a[240:243],v[232:235]\n"
U_"v173,v173,v165\n"
U_"v174,v174,v166\n"
M_"v[232:235],a[172:175],a[244:247],v[232:235]\n"
U_"v175,v175,v167\n"
R_"v160,a12\n"
M_"v[236:239],a[172:175],a[252:255],v[236:239]\n"
T_"a[172:173],v254 offset:9440\n"
R_"v161,a13\n"
M_"v[236:239],a[168:171],a[248:251],v[236:239]\n"
W_"6)\n"
T_"a[168:169],v254 offset:224\n"
R_"v162,a14\n"
M_"a[48:51],a[176:179],a[192:195],a[48:51]\n"
W_"6)\n"
T_"a[170:171],v254 offset:4832\n"
R_"v163,a15\n"
M_"a[48:51],a[180:183],a[196:199],a[48:51]\n"
T_"a[174:175],v254 offset:14048\n"
R_"v188,a28\n"
M_"a[52:55],a[180:183],a[204:207],a[52:55]\n"
R_"v189,a29\n"
R_"v190,a30\n"
M_"a[52:55],a[176:179],a[200:203],a[52:55]\n"
R_"v191,a31\n"
C_"v185,v186,v187\n"
M_"a[56:59],a[176:179],a[208:211],a[56:59]\n"
C_"v178,v172,v173\n"
C_"v179,v174,v175\n"
M_"a[56:59],a[180:183],a[212:215],a[56:59]\n"
O_"v[176:179],v255,s[48:51],s92 offen offset:2048 sc1\n"
M_"a[60:63],a[180:183],a[220:223],a[60:63]\n"
T_"a[180:181],v249 offset:45056\n"
U_"v160,v160,v188\n"
M_"a[60:63],a[176:179],a[216:219],a[60:63]\n"
W_"6)\n"
T_"a[176:177],v249 offset:36864\n"
U_"v161,v161,v189\n"
M_"v[240:243],a[184:187],a[224:227],v[240:243]\n"
W_"6)\n"
T_"a[178:179],v249 offset:40960\n"
U_"v162,v162,v190\n"
M_"v[240:243],a[188:191],a[228:231],v[240:243]\n"
T_"a[182:183],v249 offset:49152\n"
U_"v163,v163,v191\n"
M_"v[244:247],a[188:191],a[236:239],v[244:247]\n"
C_"v186,v160,v161\n"
C_"v187,v162,v163\n"
M_"v[244:247],a[184:187],a[232:235],v[244:247]\n"
O_"v[184:187],v255,s[48:51],s93 offen offset:2048 sc1\n"
M_"a[128:131],a[184:187],a[240:243],a[128:131]\n"
R_"v180,a32\n"
R_"v181,a33\n"
M_"a[128:131],a[188:191],a[244:247],a[128:131]\n"
R_"v182,a34\n"
R_"v183,a35\n"
M_"a[132:135],a[188:191],a[252:255],a[132:135]\n"
T_"a[188:189],v254 offset:27872\n"
R_"v168,a48\n"
M_"a[132:135],a[184:187],a[248:251],a[132:135]\n"
W_"6)\n"
T_"a[184:185],v254 offset:18656\n"
R_"v169,a49\n"
M_"a[64:67],a[168:171],a[192:195],a[64:67]\n"
W_"6)\n"
T_"a[186:187],v254 offset:23264\n"
R_"v170,a50\n"
M_"a[64:67],a[172:175],a[196:199],a[64:67]\n"
T_"a[190:191],v254 offset:32480\n"
R_"v171,a51\n"
M_"a[68:71],a[172:175],a[204:207],a[68:71]\n"
R_"v164,a36\n"
R_"v165,a37\n"
M_"a[68:71],a[168:171],a[200:203],a[68:71]\n"
R_"v166,a38\n"
R_"v167,a39\n"
M_"a[72:75],a[168:171],a[208:211],a[72:75]\n"
R_"v172,a52\n"
R_"v173,a53\n"
M_"a[72:75],a[172:175],a[212:215],a[72:75]\n"
R_"v174,a54\n"
R_"v175,a55\n"
M_"a[76:79],a[172:175],a[220:223],a[76:79]\n"
T_"a[172:173],v249 offset:45064\n"
U_"v180,v180,v168\n"
M_"a[76:79],a[168:171],a[216:219],a[76:79]\n"
W_"6)\n"
T_"a[168:169],v249 offset:36872\n"
U_"v181,v181,v169\n"
M_"v[0:3],a[176:179],a[224:227],v[0:3]\n"
W_"6)\n"
T_"a[170:171],v249 offset:40968\n"
U_"v182,v182,v170\n"
M_"v[0:3],a[180:183],a[228:231],v[0:3]\n"
T_"a[174:175],v249 offset:49160\n"
U_"v183,v183,v171\n"
M_"v[4:7],a[180:183],a[236:239],v[4:7]\n"
U_"v164,v164,v172\n"
U_"v165,v165,v173\n"
M_"v[4:7],a[176:179],a[232:235],v[4:7]\n"
U_"v166,v166,v174\n"
U_"v167,v167,v175\n"
M_"v[8:11],a[176:179],a[240:243],v[8:11]\n"
C_"v180,v180,v181\n"
R_"v168,a64\n"
M_"v[8:11],a[180:183],a[244:247],v[8:11]\n"
R_"v169,a65\n"
R_"v170,a66\n"
M_"v[12:15],a[180:183],a[252:255],v[12:15]\n"
T_"a[180:181],v254 offset:9472\n"
R_"v171,a67\n"
M_"v[12:15],a[176:179],a[248:251],v[12:15]\n"
W_"6)\n"
T_"a[176:177],v254 offset:256\n"
C_"v181,v182,v183\n"
M_"a[80:83],a[184:187],a[192:195],a[80:83]\n"
W_"6)\n"
T_"a[178:179],v254 offset:4864\n"
R_"v176,a40\n"
M_"a[80:83],a[188:191],a[196:199],a[80:83]\n"
T_"a[182:183],v254 offset:14080\n"
R_"v177,a41\n"
M_"a[84:87],a[188:191],a[204:207],a[84:87]\n"
R_"v178,a42\n"
R_"v179,a43\n"
M_"a[84:87],a[184:187],a[200:203],a[84:87]\n"
R_"v188,a56\n"
R_"v189,a57\n"
M_"a[88:91],a[184:187],a[208:211],a[88:91]\n"
R_"v172,a80\n"
R_"v173,a81\n"
M_"a[88:91],a[188:191],a[212:215],a[88:91]\n"
R_"v174,a82\n"
R_"v175,a83\n"
M_"a[92:95],a[188:191],a[220:223],a[92:95]\n"
T_"a[188:189],v250 offset:45056\n"
R_"v190,a58\n"
M_"a[92:95],a[184:187],a[216:219],a[92:95]\n"
W_"6)\n"
T_"a[184:185],v250 offset:36864\n"
R_"v191,a59\n"
M_"v[64:67],a[168:171],a[224:227],v[64:67]\n"
W_"6)\n"
T_"a[186:187],v250 offset:40960\n"
R_"v160,a44\n"
M_"v[64:67],a[172:175],a[228:231],v[64:67]\n"
T_"a[190:191],v250 offset:49152\n"
R_"v161,a45\n"
M_"v[68:71],a[172:175],a[236:239],v[68:71]\n"
R_"v162,a46\n"
R_"v163,a47\n"
M_"v[68:71],a[168:171],a[232:235],v[68:71]\n"
R_"v184,a60\n"
R_"v185,a61\n"
M_"v[72:75],a[168:171],a[240:243],v[72:75]\n"
R_"v186,a62\n"
R_"v187,a63\n"
M_"v[72:75],a[172:175],a[244:247],v[72:75]\n"
U_"v168,v168,v172\n"
U_"v169,v169,v173\n"
M_"v[76:79],a[172:175],a[252:255],v[76:79]\n"
T_"a[172:173],v254 offset:27904\n"
U_"v170,v170,v174\n"
M_"v[76:79],a[168:171],a[248:251],v[76:79]\n"
W_"6)\n"
T_"a[168:169],v254 offset:18688\n"
U_"v171,v171,v175\n"
M_"a[96:99],a[176:179],a[192:195],a[96:99]\n"
W_"6)\n"
T_"a[170:171],v254 offset:23296\n"
U_"v176,v176,v188\n"
M_"a[96:99],a[180:183],a[196:199],a[96:99]\n"
T_"a[174:175],v254 offset:32512\n"
U_"v177,v177,v189\n"
M_"a[100:103],a[180:183],a[204:207],a[100:103]\n"
U_"v178,v178,v190\n"
U_"v179,v179,v191\n"
M_"a[100:103],a[176:179],a[200:203],a[100:103]\n"
U_"v160,v160,v184\n"
U_"v161,v161,v185\n"
M_"a[104:107],a[176:179],a[208:211],a[104:107]\n"
U_"v162,v162,v186\n"
U_"v163,v163,v187\n"
M_"a[104:107],a[180:183],a[212:215],a[104:107]\n"
C_"v182,v168,v169\n"
C_"v183,v170,v171\n"
M_"a[108:111],a[180:183],a[220:223],a[108:111]\n"
T_"a[180:181],v250 offset:45064\n"
R_"v188,a68\n"
M_"a[108:111],a[176:179],a[216:219],a[108:111]\n"
W_"6)\n"
T_"a[176:177],v250 offset:36872\n"
R_"v189,a69\n"
M_"v[16:19],a[184:187],a[224:227],v[16:19]\n"
W_"6)\n"
T_"a[178:179],v250 offset:40968\n"
R_"v190,a70\n"
M_"v[16:19],a[188:191],a[228:231],v[16:19]\n"
T_"a[182:183],v250 offset:49160\n"
R_"v191,a71\n"
M_"v[20:23],a[188:191],a[236:239],v[20:23]\n"
O_"v[180:183],v255,s[48:51],s90 offen offset:3072 sc1\n"
M_"v[20:23],a[184:187],a[232:235],v[20:23]\n"
"v_xor_b32 v250,128,v249\n"
R_"v184,a84\n"
M_"v[24:27],a[184:187],a[240:243],v[24:27]\n"
R_"v185,a85\n"
R_"v186,a86\n"
M_"v[24:27],a[188:191],a[244:247],v[24:27]\n"
R_"v187,a87\n"
U_"v188,v188,v184\n"
M_"v[28:31],a[188:191],a[252:255],v[28:31]\n"
T_"a[188:189],v254 offset:9280\n"
U_"v189,v189,v185\n"
M_"v[28:31],a[184:187],a[248:251],v[28:31]\n"
W_"6)\n"
T_"a[184:185],v254 offset:64\n"
U_"v190,v190,v186\n"
M_"a[112:115],a[168:171],a[192:195],a[112:115]\n"
W_"6)\n"
T_"a[186:187],v254 offset:4672\n"
U_"v191,v191,v187\n"
M_"a[112:115],a[172:175],a[196:199],a[112:115]\n"
T_"a[190:191],v254 offset:13888\n"
C_"v160,v160,v161\n"
M_"a[116:119],a[172:175],a[204:207],a[116:119]\n"
R_"v180,a76\n"
R_"v181,a77\n"
M_"a[116:119],a[168:171],a[200:203],a[116:119]\n"
R_"v182,a78\n"
R_"v183,a79\n"
M_"a[120:123],a[168:171],a[208:211],a[120:123]\n"
R_"v184,a92\n"
R_"v185,a93\n"
M_"a[120:123],a[172:175],a[212:215],a[120:123]\n"
R_"v186,a94\n"
R_"v187,a95\n"
M_"a[124:127],a[172:175],a[220:223],a[124:127]\n"
T_"a[172:173],v250 offset:45056\n"
C_"v161,v162,v163\n"
M_"a[124:127],a[168:171],a[216:219],a[124:127]\n"
W_"6)\n"
T_"a[168:169],v250 offset:36864\n"
U_"v180,v180,v184\n"
M_"v[80:83],a[176:179],a[224:227],v[80:83]\n"
W_"6)\n"
T_"a[170:171],v250 offset:40960\n"
U_"v181,v181,v185\n"
M_"v[80:83],a[180:183],a[228:231],v[80:83]\n"
T_"a[174:175],v250 offset:49152\n"
U_"v182,v182,v186\n"
M_"v[84:87],a[180:183],a[236:239],v[84:87]\n"
U_"v183,v183,v187\n"
C_"v164,v164,v165\n"
M_"v[84:87],a[176:179],a[232:235],v[84:87]\n"
C_"v162,v180,v181\n"
C_"v163,v182,v183\n"
M_"v[88:91],a[176:179],a[240:243],v[88:91]\n"
O_"v[160:163],v255,s[48:51],s93 offen offset:3072 sc1\n"
M_"v[88:91],a[180:183],a[244:247],v[88:91]\n"
C_"v165,v166,v167\n"
"v_lshl_add_u32 v156,v157,6,v156\n"
M_"v[92:95],a[180:183],a[252:255],v[92:95]\n"
T_"a[180:181],v254 offset:27712\n"
C_"v166,v188,v189\n"
M_"v[92:95],a[176:179],a[248:251],v[92:95]\n"
W_"6)\n"
T_"a[176:177],v254 offset:18496\n"
C_"v167,v190,v191\n"
M_"v[192:195],a[184:187],a[192:195],v[192:195]\n"
W_"6)\n"
T_"a[178:179],v254 offset:23104\n"
"v_bfe_u32 v157,v255,8,1\n"
M_"v[192:195],a[188:191],a[196:199],v[192:195]\n"
T_"a[182:183],v254 offset:32320\n"
R_"v180,a108\n"
M_"v[196:199],a[188:191],a[204:207],v[196:199]\n"
O_"v[164:167],v255,s[48:51],s91 offen offset:3072 sc1\n"
M_"v[196:199],a[184:187],a[200:203],v[196:199]\n"
R_"v160,a124\n"
"v_lshl_add_u32 v156,v157,5,v156\n"
M_"v[200:203],a[184:187],a[208:211],v[200:203]\n"
C_"v176,v176,v177\n"
R_"v172,a72\n"
M_"v[200:203],a[188:191],a[212:215],v[200:203]\n"
R_"v173,a73\n"
R_"v168,a88\n"
M_"v[204:207],a[188:191],a[220:223],v[204:207]\n"
T_"a[188:189],v250 offset:45064\n"
R_"v169,a89\n"
M_"v[204:207],a[184:187],a[216:219],v[204:207]\n"
W_"6)\n"
T_"a[184:185],v250 offset:36872\n"
U_"v180,v180,v160\n"
M_"v[32:35],a[168:171],a[224:227],v[32:35]\n"
W_"6)\n"
T_"a[186:187],v250 offset:40968\n"
"v_bfe_u32 v157,v255,4,2\n"
M_"v[32:35],a[172:175],a[228:231],v[32:35]\n"
T_"a[190:191],v250 offset:49160\n"
C_"v177,v178,v179\n"
M_"v[36:39],a[172:175],a[236:239],v[36:39]\n"
"v_xor_b32 v250,192,v249\n"
R_"v174,a74\n"
M_"v[36:39],a[168:171],a[232:235],v[36:39]\n"
R_"v175,a75\n"
R_"v170,a90\n"
M_"v[40:43],a[168:171],a[240:243],v[40:43]\n"
R_"v171,a91\n"
U_"v172,v172,v168\n"
M_"v[40:43],a[172:175],a[244:247],v[40:43]\n"
U_"v173,v173,v169\n"
R_"v188,a96\n"
M_"v[44:47],a[172:175],a[252:255],v[44:47]\n"
T_"a[172:173],v254 offset:9312\n"
R_"v164,a112\n"
M_"v[44:47],a[168:171],a[248:251],v[44:47]\n"
W_"6)\n"
T_"a[168:169],v254 offset:96\n"
"v_lshl_add_u32 v152,v157,3,v156\n"
M_"v[208:211],a[176:179],a[192:195],v[208:211]\n"
W_"6)\n"
T_"a[170:171],v254 offset:4704\n"
U_"v174,v174,v170\n"
M_"v[208:211],a[180:183],a[196:199],v[208:211]\n"
T_"a[174:175],v254 offset:13920\n"
U_"v175,v175,v171\n"
M_"v[212:215],a[180:183],a[204:207],v[212:215]\n"
C_"v178,v172,v173\n"
U_"v188,v188,v164\n"
M_"v[212:215],a[176:179],a[200:203],v[212:215]\n"
C_"v179,v174,v175\n"
R_"v168,a100\n"
M_"v[216:219],a[176:179],a[208:211],v[216:219]\n"
O_"v[176:179],v255,s[48:51],s92 offen offset:3072 sc1\n"
M_"v[216:219],a[180:183],a[212:215],v[216:219]\n"
R_"v169,a101\n"
R_"v172,a116\n"
M_"v[220:223],a[180:183],a[220:223],v[220:223]\n"
T_"a[180:181],v250 offset:45056\n"
R_"v173,a117\n"
M_"v[220:223],a[176:179],a[216:219],v[220:223]\n"
W_"6)\n"
T_"a[176:177],v250 offset:36864\n"
R_"v170,a102\n"
M_"v[96:99],a[184:187],a[224:227],v[96:99]\n"
W_"6)\n"
T_"a[178:179],v250 offset:40960\n"
R_"v171,a103\n"
M_"v[96:99],a[188:191],a[228:231],v[96:99]\n"
T_"a[182:183],v250 offset:49152\n"
R_"v174,a118\n"
M_"v[100:103],a[188:191],a[236:239],v[100:103]\n"
R_"v175,a119\n"
U_"v168,v168,v172\n"
M_"v[100:103],a[184:187],a[232:235],v[100:103]\n"
U_"v169,v169,v173\n"
U_"v170,v170,v174\n"
M_"v[104:107],a[184:187],a[240:243],v[104:107]\n"
U_"v171,v171,v175\n"
C_"v168,v168,v169\n"
M_"v[104:107],a[188:191],a[244:247],v[104:107]\n"
R_"v189,a97\n"
R_"v165,a113\n"
M_"v[108:111],a[188:191],a[252:255],v[108:111]\n"
T_"a[188:189],v254 offset:27744\n"
C_"v169,v170,v171\n"
M_"v[108:111],a[184:187],a[248:251],v[108:111]\n"
W_"6)\n"
T_"a[184:185],v254 offset:18528\n"
R_"v176,a104\n"
M_"v[224:227],a[168:171],a[192:195],v[224:227]\n"
W_"6)\n"
T_"a[186:187],v254 offset:23136\n"
R_"v177,a105\n"
M_"v[224:227],a[172:175],a[196:199],v[224:227]\n"
T_"a[190:191],v254 offset:32352\n"
R_"v184,a120\n"
M_"v[228:231],a[172:175],a[204:207],v[228:231]\n"
"buffer_store_dwordx2 v[168:169],v158,s[48:51],s95 offen sc1\n"
M_"v[228:231],a[168:171],a[200:203],v[228:231]\n"
"s_sub_u32 s95,s88,144\n"
R_"v185,a121\n"
R_"v181,a109\n"
M_"v[232:235],a[168:171],a[208:211],v[232:235]\n"
"s_lshr_b32 s95,s95,5\n"
R_"v161,a125\n"
R_"v190,a98\n"
M_"v[232:235],a[172:175],a[212:215],v[232:235]\n"
R_"v191,a99\n"
R_"v166,a114\n"
M_"v[236:239],a[172:175],a[220:223],v[236:239]\n"
T_"a[172:173],v250 offset:45064\n"
R_"v167,a115\n"
M_"v[236:239],a[168:171],a[216:219],v[236:239]\n"
W_"6)\n"
T_"a[168:169],v250 offset:36872\n"
U_"v189,v189,v165\n"
M_"v[48:51],a[176:179],a[224:227],v[48:51]\n"
W_"6)\n"
T_"a[170:171],v250 offset:40968\n"
R_"v178,a106\n"
M_"v[48:51],a[180:183],a[228:231],v[48:51]\n"
T_"a[174:175],v250 offset:49160\n"
R_"v179,a107\n"
M_"v[52:55],a[180:183],a[236:239],v[52:55]\n"
"v_xor_b32 v250,64,v253\n"
R_"v186,a122\n"
M_"v[52:55],a[176:179],a[232:235],v[52:55]\n"
R_"v187,a123\n"
U_"v176,v176,v184\n"
M_"v[56:59],a[176:179],a[240:243],v[56:59]\n"
U_"v177,v177,v185\n"
R_"v182,a110\n"
M_"v[56:59],a[180:183],a[244:247],v[56:59]\n"
R_"v183,a111\n"
R_"v162,a126\n"
M_"v[60:63],a[180:183],a[252:255],v[60:63]\n"
R_"v163,a127\n"
U_"v181,v181,v161\n"
M_"v[60:63],a[176:179],a[248:251],v[60:63]\n"
W_"5)\n"
U_"v190,v190,v166\n"
U_"v191,v191,v167\n"
M_"v[240:243],a[184:187],a[192:195],v[240:243]\n"
W_"4)\n"
C_"v188,v188,v189\n"
U_"v178,v178,v186\n"
M_"v[240:243],a[188:191],a[196:199],v[240:243]\n"
U_"v179,v179,v187\n"
C_"v176,v176,v177\n"
M_"v[244:247],a[188:191],a[204:207],v[244:247]\n"
"s_waitcnt vmcnt(21) lgkmcnt(0)\n"
"s_barrier\n"
T_"a[176:177],v253 offset:36864\n"
M_"v[244:247],a[184:187],a[200:203],v[244:247]\n"
L_"v251,s[40:43],s84 offen lds\n"
M_"a[128:131],a[184:187],a[208:211],a[128:131]\n"
A_"m0,s61,69632\n"
T_"a[178:179],v253 offset:40960\n"
U_"v182,v182,v162\n"
M_"a[128:131],a[188:191],a[212:215],a[128:131]\n"
T_"a[180:181],v253 offset:45056\n"
U_"v183,v183,v163\n"
M_"a[132:135],a[188:191],a[220:223],a[132:135]\n"
T_"a[182:183],v253 offset:49152\n"
C_"v180,v180,v181\n"
M_"a[132:135],a[184:187],a[216:219],a[132:135]\n"
T_"a[184:185],v253 offset:36872\n"
C_"v189,v190,v191\n"
M_"v[112:115],a[168:171],a[224:227],v[112:115]\n"
T_"a[186:187],v253 offset:40968\n"
R_"v164,a128\n"
M_"v[112:115],a[172:175],a[228:231],v[112:115]\n"
T_"a[188:189],v253 offset:45064\n"
U_"v232,v232,v164\n"
M_"v[116:119],a[172:175],a[236:239],v[116:119]\n"
T_"a[190:191],v253 offset:49160\n"
C_"v177,v178,v179\n"
M_"v[116:119],a[168:171],a[232:235],v[116:119]\n"
B_"a[224:227],v144 offset:0\n"
C_"v181,v182,v183\n"
M_"v[120:123],a[168:171],a[240:243],v[120:123]\n"
L_"v251,s[40:43],s62 offen lds\n"
M_"v[120:123],a[172:175],a[244:247],v[120:123]\n"
A_"m0,s61,73728\n"
B_"a[228:231],v145 offset:0\n"
R_"v165,a129\n"
M_"v[124:127],a[172:175],a[252:255],v[124:127]\n"
A_"s62,s84,8192\n"
T_"a[172:173],v250 offset:45056\n"
R_"v166,a130\n"
M_"v[124:127],a[168:171],a[248:251],v[124:127]\n"
W_"9)\n"
T_"a[168:169],v250 offset:36864\n"
R_"v167,a131\n"
M_"v[0:3],a[176:179],a[192:195],v[0:3]\n"
W_"8)\n"
T_"a[170:171],v250 offset:40960\n"
U_"v233,v233,v165\n"
M_"v[0:3],a[180:183],a[196:199],v[0:3]\n"
T_"a[174:175],v250 offset:49152\n"
U_"v234,v234,v166\n"
M_"v[4:7],a[180:183],a[204:207],v[4:7]\n"
B_"a[232:235],v146 offset:0\n"
U_"v235,v235,v167\n"
M_"v[4:7],a[176:179],a[200:203],v[4:7]\n"
B_"a[236:239],v147 offset:0\n"
U_"v192,v192,v208\n"
M_"v[8:11],a[176:179],a[208:211],v[8:11]\n"
B_"a[240:243],v144 offset:4096\n"
U_"v193,v193,v209\n"
M_"v[8:11],a[180:183],a[212:215],v[8:11]\n"
L_"v251,s[40:43],s62 offen lds\n"
M_"v[12:15],a[180:183],a[220:223],v[12:15]\n"
A_"m0,s61,77824\n"
T_"a[180:181],v250 offset:45064\n"
C_"v0,v0,v1\n"
M_"v[12:15],a[176:179],a[216:219],v[12:15]\n"
W_"12)\n"
T_"a[176:177],v250 offset:36872\n"
C_"v1,v2,v3\n"
M_"v[64:67],a[184:187],a[192:195],v[64:67]\n"
W_"11)\n"
T_"a[178:179],v250 offset:40968\n"
C_"v4,v4,v5\n"
M_"v[64:67],a[188:191],a[196:199],v[64:67]\n"
A_"s62,s84,12288\n"
T_"a[182:183],v250 offset:49160\n"
C_"v5,v6,v7\n"
M_"v[68:71],a[188:191],a[204:207],v[68:71]\n"
A_"s84,s84,0x4000\n"
B_"a[244:247],v145 offset:4096\n"
U_"v194,v194,v210\n"
M_"v[68:71],a[184:187],a[200:203],v[68:71]\n"
B_"a[248:251],v146 offset:4096\n"
U_"v195,v195,v211\n"
M_"v[72:75],a[184:187],a[208:211],v[72:75]\n"
B_"a[252:255],v147 offset:4096\n"
C_"v192,v192,v193\n"
M_"v[72:75],a[188:191],a[212:215],v[72:75]\n"
"buffer_store_dwordx2 v[188:189],v158,s[48:51],s94 offen sc1\n"
M_"v[76:79],a[188:191],a[220:223],v[76:79]\n"
"s_mul_i32 s94,s88,1821\n"
L_"v251,s[40:43],s62 offen lds\n"
M_"v[76:79],a[184:187],a[216:219],v[76:79]\n"
W_"11)\n"
"v_xor_b32 v250,128,v253\n"
C_"v2,v64,v65\n"
M_"v[16:19],a[168:171],a[192:195],v[16:19]\n"
W_"10)\n"
T_"a[184:185],v250 offset:36864\n"
C_"v3,v66,v67\n"
M_"v[16:19],a[172:175],a[196:199],v[16:19]\n"
A_"m0,s60,49152\n"
T_"a[186:187],v250 offset:40960\n"
C_"v6,v68,v69\n"
M_"v[20:23],a[172:175],a[204:207],v[20:23]\n"
A_"s62,s84,4096\n"
T_"a[188:189],v250 offset:45056\n"
C_"v7,v70,v71\n"
M_"v[20:23],a[168:171],a[200:203],v[20:23]\n"
"s_lshr_b32 s94,s94,16\n"
T_"a[190:191],v250 offset:49152\n"
C_"v16,v16,v17\n"
M_"v[24:27],a[168:171],a[208:211],v[24:27]\n"
"buffer_store_dwordx2 v[176:177],v158,s[48:51],s96 offen sc1\n"
M_"v[24:27],a[172:175],a[212:215],v[24:27]\n"
S_"s96,0\n"
"buffer_store_dwordx2 v[180:181],v158,s[48:51],s97 offen sc1\n"
M_"v[28:31],a[172:175],a[220:223],v[28:31]\n"
T_"a[172:173],v250 offset:45064\n"
C_"v17,v18,v19\n"
M_"v[28:31],a[168:171],a[216:219],v[28:31]\n"
W_"9)\n"
L_"v252,s[44:47],s85 offen lds\n"
M_"v[80:83],a[176:179],a[192:195],v[80:83]\n"
W_"8)\n"
T_"a[168:169],v250 offset:36872\n"
C_"v20,v20,v21\n"
M_"v[80:83],a[180:183],a[196:199],v[80:83]\n"
T_"a[170:171],v250 offset:40968\n"
C_"v21,v22,v23\n"
M_"v[84:87],a[180:183],a[204:207],v[84:87]\n"
T_"a[174:175],v250 offset:49160\n"
C_"v8,v8,v9\n"
M_"v[84:87],a[176:179],a[200:203],v[84:87]\n"
"v_xor_b32 v250,192,v253\n"
C_"v9,v10,v11\n"
M_"v[88:91],a[176:179],a[208:211],v[88:91]\n"
C_"v18,v80,v81\n"
C_"v19,v82,v83\n"
M_"v[88:91],a[180:183],a[212:215],v[88:91]\n"
C_"v24,v24,v25\n"
C_"v10,v72,v73\n"
M_"v[92:95],a[180:183],a[220:223],v[92:95]\n"
T_"a[180:181],v250 offset:45056\n"
C_"v22,v84,v85\n"
M_"v[92:95],a[176:179],a[216:219],v[92:95]\n"
W_"7)\n"
T_"a[176:177],v250 offset:36864\n"
C_"v23,v86,v87\n"
M_"v[32:35],a[184:187],a[192:195],v[32:35]\n"
W_"6)\n"
L_"v252,s[44:47],s85 offen offset:2048 lds\n"
M_"v[32:35],a[188:191],a[196:199],v[32:35]\n"
A_"m0,s60,50176\n"
T_"a[178:179],v250 offset:40960\n"
C_"v11,v74,v75\n"
M_"v[36:39],a[188:191],a[204:207],v[36:39]\n"
A_"s85,s85,0x4000\n"
T_"a[182:183],v250 offset:49152\n"
C_"v25,v26,v27\n"
M_"v[36:39],a[184:187],a[200:203],v[36:39]\n"
C_"v32,v32,v33\n"
C_"v26,v88,v89\n"
M_"v[40:43],a[184:187],a[208:211],v[40:43]\n"
C_"v33,v34,v35\n"
C_"v27,v90,v91\n"
M_"v[40:43],a[188:191],a[212:215],v[40:43]\n"
C_"v12,v12,v13\n"
C_"v28,v28,v29\n"
M_"v[44:47],a[188:191],a[220:223],v[44:47]\n"
T_"a[188:189],v250 offset:45064\n"
C_"v36,v36,v37\n"
M_"v[44:47],a[184:187],a[216:219],v[44:47]\n"
W_"6)\n"
T_"a[184:185],v250 offset:36872\n"
C_"v37,v38,v39\n"
M_"v[96:99],a[168:171],a[192:195],v[96:99]\n"
W_"6)\n"
T_"a[186:187],v250 offset:40968\n"
C_"v40,v40,v41\n"
M_"v[96:99],a[172:175],a[196:199],v[96:99]\n"
L_"v252,s[44:47],s63 offen lds\n"
M_"v[100:103],a[172:175],a[204:207],v[100:103]\n"
T_"a[190:191],v250 offset:49160\n"
C_"v41,v42,v43\n"
M_"v[100:103],a[168:171],a[200:203],v[100:103]\n"
C_"v44,v44,v45\n"
C_"v13,v14,v15\n"
M_"v[104:107],a[168:171],a[208:211],v[104:107]\n"
C_"v34,v96,v97\n"
C_"v35,v98,v99\n"
M_"v[104:107],a[172:175],a[212:215],v[104:107]\n"
C_"v45,v46,v47\n"
C_"v14,v76,v77\n"
M_"v[108:111],a[172:175],a[220:223],v[108:111]\n"
C_"v38,v100,v101\n"
C_"v39,v102,v103\n"
M_"v[108:111],a[168:171],a[216:219],v[108:111]\n"
W_"5)\n"
C_"v15,v78,v79\n"
C_"v29,v30,v31\n"
M_"v[48:51],a[176:179],a[192:195],v[48:51]\n"
W_"4)\n"
C_"v42,v104,v105\n"
C_"v43,v106,v107\n"
M_"v[48:51],a[180:183],a[196:199],v[48:51]\n"
C_"v46,v108,v109\n"
C_"v47,v110,v111\n"
M_"v[52:55],a[180:183],a[204:207],v[52:55]\n"
W_"0)\n"
"s_barrier\n"
C_"v30,v92,v93\n"
M_"v[52:55],a[176:179],a[200:203],v[52:55]\n"
L_"v252,s[44:47],s63 offen offset:2048 lds\n"
M_"v[56:59],a[176:179],a[208:211],v[56:59]\n"
A_"m0,s60,81920\n"
C_"v48,v48,v49\n"
C_"v31,v94,v95\n"
M_"v[56:59],a[180:183],a[212:215],v[56:59]\n"
A_"s63,s85,128\n"
C_"v49,v50,v51\n"
C_"v193,v194,v195\n"
M_"v[60:63],a[180:183],a[220:223],v[60:63]\n"
C_"v52,v52,v53\n"
U_"v226,v226,v242\n"
M_"v[60:63],a[176:179],a[216:219],v[60:63]\n"
C_"v53,v54,v55\n"
U_"v227,v227,v243\n"
M_"v[112:115],a[184:187],a[192:195],v[112:115]\n"
B_"a[192:195],v144 offset:8192\n"
C_"v195,v226,v227\n"
M_"v[112:115],a[188:191],a[196:199],v[112:115]\n"
B_"a[196:199],v145 offset:8192\n"
U_"v224,v224,v240\n"
M_"v[116:119],a[188:191],a[204:207],v[116:119]\n"
B_"a[204:207],v147 offset:8192\n"
U_"v225,v225,v241\n"
M_"v[116:119],a[184:187],a[200:203],v[116:119]\n"
L_"v252,s[44:47],s85 offen lds\n"
M_"v[120:123],a[184:187],a[208:211],v[120:123]\n"
C_"v50,v112,v113\n"
C_"v51,v114,v115\n"
M_"v[120:123],a[188:191],a[212:215],v[120:123]\n"
C_"v56,v56,v57\n"
C_"v60,v60,v61\n"
M_"v[124:127],a[188:191],a[220:223],v[124:127]\n"
C_"v54,v116,v117\n"
C_"v55,v118,v119\n"
M_"v[124:127],a[184:187],a[216:219],v[124:127]\n"
C_"v57,v58,v59\n"
C_"v61,v62,v63\n"
M_"v[64:67],a[224:227],v[0:3],0\n"
B_"a[200:203],v146 offset:8192\n"
C_"v58,v120,v121\n"
M_"v[64:67],a[228:231],v[16:19],v[64:67]\n"
B_"a[208:211],v144 offset:12288\n"
C_"v59,v122,v123\n"
M_"v[64:67],a[232:235],v[32:35],v[64:67]\n"
B_"a[212:215],v145 offset:12288\n"
C_"v62,v124,v125\n"
M_"v[64:67],a[236:239],v[48:51],v[64:67]\n"
B_"a[216:219],v146 offset:12288\n"
C_"v63,v126,v127\n"
M_"v[68:71],a[236:239],v[52:55],0\n"
B_"a[220:223],v147 offset:12288\n"
C_"v194,v224,v225\n"
M_"v[68:71],a[232:235],v[36:39],v[68:71]\n"
L_"v252,s[44:47],s85 offen offset:2048 lds\n"
M_"v[68:71],a[228:231],v[20:23],v[68:71]\n"
A_"m0,s60,82944\n"
O_"v[192:195],v255,s[48:51],s90 offen offset:1024 sc1\n"
M_"v[68:71],a[224:227],v[4:7],v[68:71]\n"
A_"s85,s85,0x4000\n"
"v_max3_f32 v160,v64,v65,v66\n"
U_"v196,v196,v212\n"
M_"v[72:75],a[224:227],v[8:11],0\n"
"v_max_f32 v160,v160,v67\n"
U_"v197,v197,v213\n"
M_"v[72:75],a[228:231],v[24:27],v[72:75]\n"
"v_mov_b32 v156,v160\n"
U_"v198,v198,v214\n"
M_"v[72:75],a[232:235],v[40:43],v[72:75]\n"
"v_permlane16_swap_b32 v160,v156\n"
"v_max3_f32 v164,v68,v69,v70\n"
M_"v[72:75],a[236:239],v[56:59],v[72:75]\n"
"v_max_f32 v160,v160,v156\n"
"v_max_f32 v164,v164,v71\n"
M_"v[76:79],a[236:239],v[60:63],0\n"
"v_mov_b32 v156,v160\n"
"v_mov_b32 v157,v164\n"
M_"v[76:79],a[232:235],v[44:47],v[76:79]\n"
L_"v252,s[44:47],s63 offen lds\n"
M_"v[76:79],a[228:231],v[28:31],v[76:79]\n"
"v_permlane32_swap_b32 v160,v156\n"
"v_permlane16_swap_b32 v164,v157\n"
M_"v[76:79],a[224:227],v[12:15],v[76:79]\n"
"v_max3_f32 v168,v72,v73,v74\n"
"v_max_f32 v160,v160,v156\n"
M_"v[80:83],a[240:243],v[0:3],0\n"
"s_waitcnt vmcnt(27) lgkmcnt(0)\n"
"s_barrier\n"
B_"a[224:227],v144 offset:32768\n"
M_"v[80:83],a[244:247],v[16:19],v[80:83]\n"
B_"a[228:231],v145 offset:32768\n"
"v_max_f32 v164,v164,v157\n"
M_"v[80:83],a[248:251],v[32:35],v[80:83]\n"
B_"a[232:235],v146 offset:32768\n"
"v_max_f32 v168,v168,v75\n"
M_"v[80:83],a[252:255],v[48:51],v[80:83]\n"
B_"a[236:239],v147 offset:32768\n"
"v_sub_f32 v160,0,v160\n"
M_"v[84:87],a[252:255],v[52:55],0\n"
"v_mov_b32 v157,v164\n"
"v_mov_b32 v158,v168\n"
M_"v[84:87],a[248:251],v[36:39],v[84:87]\n"
"v_mov_b32 v161,v160\n"
"v_mov_b32 v162,v160\n"
M_"v[84:87],a[244:247],v[20:23],v[84:87]\n"
L_"v252,s[44:47],s63 offen offset:2048 lds\n"
M_"v[84:87],a[240:243],v[4:7],v[84:87]\n"
A_"m0,s61,98304\n"
"v_mov_b32 v163,v160\n"
"v_permlane32_swap_b32 v164,v157\n"
M_"v[88:91],a[240:243],v[8:11],0\n"
"v_permlane16_swap_b32 v168,v158\n"
"v_max3_f32 v172,v76,v77,v78\n"
M_"v[88:91],a[244:247],v[24:27],v[88:91]\n"
"v_max_f32 v164,v164,v157\n"
"v_max_f32 v168,v168,v158\n"
M_"v[88:91],a[248:251],v[40:43],v[88:91]\n"
"v_max_f32 v172,v172,v79\n"
"v_sub_f32 v164,0,v164\n"
M_"v[88:91],a[252:255],v[56:59],v[88:91]\n"
"v_mov_b32 v158,v168\n"
"v_mov_b32 v159,v172\n"
M_"v[92:95],a[252:255],v[60:63],0\n"
B_"a[252:255],v147 offset:36864\n"
"v_mov_b32 v165,v164\n"
M_"v[92:95],a[248:251],v[44:47],v[92:95]\n"
B_"a[248:251],v146 offset:36864\n"
"v_mov_b32 v166,v164\n"
M_"v[92:95],a[244:247],v[28:31],v[92:95]\n"
B_"a[244:247],v145 offset:36864\n"
"v_mov_b32 v167,v164\n"
M_"v[92:95],a[240:243],v[12:15],v[92:95]\n"
B_"a[240:243],v144 offset:36864\n"
"v_permlane32_swap_b32 v168,v158\n"
M_"v[96:99],a[192:195],v[0:3],v[160:163]\n"
L_"v251,s[40:43],s84 offen lds\n"
M_"v[96:99],a[196:199],v[16:19],v[96:99]\n"
A_"m0,s61,102400\n"
"v_permlane16_swap_b32 v172,v159\n"
"v_max_f32 v168,v168,v158\n"
M_"v[96:99],a[200:203],v[32:35],v[96:99]\n"
"v_max_f32 v172,v172,v159\n"
"v_sub_f32 v168,0,v168\n"
M_"v[96:99],a[204:207],v[48:51],v[96:99]\n"
"v_mov_b32 v159,v172\n"
"v_mov_b32 v169,v168\n"
M_"v[100:103],a[204:207],v[52:55],v[164:167]\n"
"v_mov_b32 v170,v168\n"
"v_mov_b32 v171,v168\n"
M_"v[100:103],a[200:203],v[36:39],v[100:103]\n"
"v_permlane32_swap_b32 v172,v159\n"
"v_add_f32 v64,v64,v160\n"
M_"v[100:103],a[196:199],v[20:23],v[100:103]\n"
"v_max_f32 v172,v172,v159\n"
"v_add_f32 v65,v65,v160\n"
M_"v[100:103],a[192:195],v[4:7],v[100:103]\n"
"v_sub_f32 v172,0,v172\n"
"v_add_f32 v66,v66,v160\n"
M_"v[104:107],a[192:195],v[8:11],v[168:171]\n"
L_"v251,s[40:43],s62 offen lds\n"
M_"v[104:107],a[196:199],v[24:27],v[104:107]\n"
A_"m0,s61,106496\n"
A_"s62,s84,8192\n"
"v_mov_b32 v173,v172\n"
M_"v[104:107],a[200:203],v[40:43],v[104:107]\n"
"v_mov_b32 v174,v172\n"
"v_mov_b32 v175,v172\n"
M_"v[104:107],a[204:207],v[56:59],v[104:107]\n"
E_"v64,v64\n"
M_"v[108:111],a[204:207],v[60:63],v[172:175]\n"
B_"a[204:207],v147 offset:40960\n"
"v_add_f32 v67,v67,v160\n"
M_"v[108:111],a[200:203],v[44:47],v[108:111]\n"
B_"a[200:203],v146 offset:40960\n"
"v_add_f32 v68,v68,v164\n"
M_"v[108:111],a[196:199],v[28:31],v[108:111]\n"
B_"a[196:199],v145 offset:40960\n"
"v_add_f32 v69,v69,v164\n"
M_"v[108:111],a[192:195],v[12:15],v[108:111]\n"
B_"a[192:195],v144 offset:40960\n"
"v_add_f32 v70,v70,v164\n"
M_"v[112:115],a[208:211],v[0:3],v[160:163]\n"
E_"v65,v65\n"
M_"v[112:115],a[212:215],v[16:19],v[112:115]\n"
L_"v251,s[40:43],s62 offen lds\n"
M_"v[112:115],a[216:219],v[32:35],v[112:115]\n"
A_"m0,s61,110592\n"
A_"s62,s84,12288\n"
C_"v128,v64,v65\n"
M_"v[112:115],a[220:223],v[48:51],v[112:115]\n"
A_"s84,s84,0x4000\n"
E_"v66,v66\n"
M_"v[116:119],a[220:223],v[52:55],v[164:167]\n"
E_"v67,v67\n"
M_"v[116:119],a[216:219],v[36:39],v[116:119]\n"
C_"v129,v66,v67\n"
"v_add_f32 v71,v71,v164\n"
M_"v[116:119],a[212:215],v[20:23],v[116:119]\n"
"s_cmp_lt_u32 s88,144\n"
"s_cselect_b32 s94,s94,s95\n"
"v_add_f32 v72,v72,v168\n"
M_"v[116:119],a[208:211],v[4:7],v[116:119]\n"
"s_sub_u32 s88,s88,s18\n"
E_"v68,v68\n"
M_"v[120:123],a[208:211],v[8:11],v[168:171]\n"
"s_mul_i32 s88,s88,36864\n"
E_"v69,v69\n"
M_"v[120:123],a[212:215],v[24:27],v[120:123]\n"
A_"s88,s88,s89\n"
E_"v70,v70\n"
M_"v[120:123],a[216:219],v[40:43],v[120:123]\n"
"s_min_u32 s95,s94,1\n"
L_"v251,s[40:43],s62 offen lds\n"
M_"v[120:123],a[220:223],v[56:59],v[120:123]\n"
A_"m0,s61,0\n"
A_"s62,s84,4096\n"
C_"v132,v68,v69\n"
M_"v[124:127],a[220:223],v[60:63],v[172:175]\n"
A_"s16,s16,s88\n"
"s_addc_u32 s17,s17,0\n"
B_"a[220:223],v147 offset:45056\n"
M_"v[124:127],a[216:219],v[44:47],v[124:127]\n"
"s_mul_i32 s88,s76,1821\n"
"s_lshl_b32 s94,s94,1\n"
B_"a[216:219],v146 offset:45056\n"
M_"v[124:127],a[212:215],v[28:31],v[124:127]\n"
"s_lshr_b32 s88,s88,16\n"
A_"s94,s94,s95\n"
B_"a[212:215],v145 offset:45056\n"
M_"v[124:127],a[208:211],v[12:15],v[124:127]\n"
W_"14)\n"
"s_mul_i32 s89,s88,36\n"
B_"a[208:211],v144 offset:45056\n"
M_"v[64:67],a[224:227],v[0:3],v[160:163]\n"
W_"14)\n"
L_"v251,s[40:43],s84 offen lds\n"
M_"v[64:67],a[228:231],v[16:19],v[64:67]\n"
W_"13)\n"
A_"m0,s61,4096\n"
"s_sub_u32 s89,s76,s89\n"
M_"v[64:67],a[232:235],v[32:35],v[64:67]\n"
W_"12)\n"
L_"v251,s[40:43],s62 offen lds\n"
M_"v[64:67],a[236:239],v[48:51],v[64:67]\n"
A_"m0,s61,8192\n"
A_"s62,s84,8192\n"
"s_mul_i32 s95,s89,7282\n"
E_"v71,v71\n"
"s_nop 0\n"
C_"v133,v70,v71\n"
M_"v[68:71],a[236:239],v[52:55],v[164:167]\n"
"s_lshr_b32 s95,s95,16\n"
L_"v251,s[40:43],s62 offen lds\n"
M_"v[68:71],a[232:235],v[36:39],v[68:71]\n"
A_"m0,s61,12288\n"
A_"s62,s84,12288\n"
"s_mul_i32 s95,s95,9\n"
M_"v[68:71],a[228:231],v[20:23],v[68:71]\n"
"s_sub_u32 s95,s89,s95\n"
L_"v251,s[40:43],s62 offen lds\n"
M_"v[68:71],a[224:227],v[4:7],v[68:71]\n"
"s_min_u32 s88,s88,15\n"
"v_add_f32 v73,v73,v168\n"
"v_add_f32 v74,v74,v168\n"
"v_add_f32 v75,v75,v168\n"
E_"v72,v72\n"
E_"v73,v73\n"
E_"v74,v74\n"
E_"v75,v75\n"
C_"v136,v72,v73\n"
C_"v137,v74,v75\n"
M_"v[72:75],a[224:227],v[8:11],v[168:171]\n"
"s_cmp_ge_u32 s95,2\n"
"s_cselect_b32 s95,-2,7\n"
"s_mul_i32 s88,s88,36864\n"
M_"v[72:75],a[228:231],v[24:27],v[72:75]\n"
A_"s89,s89,s95\n"
"v_add_f32 v76,v76,v172\n"
"v_add_f32 v77,v77,v172\n"
M_"v[72:75],a[232:235],v[40:43],v[72:75]\n"
"s_lshl_b32 s89,s89,5\n"
"v_add_f32 v78,v78,v172\n"
"v_add_f32 v79,v79,v172\n"
M_"v[72:75],a[236:239],v[56:59],v[72:75]\n"
A_"s88,s88,s89\n"
E_"v76,v76\n"
E_"v77,v77\n"
E_"v78,v78\n"
E_"v79,v79\n"
C_"v140,v76,v77\n"
C_"v141,v78,v79\n"
M_"v[76:79],a[236:239],v[60:63],v[172:175]\n"
A_"s88,s88,4128768\n"
"v_lshrrev_b32 v227,2,v255\n"
"v_add_f32 v80,v80,v160\n"
M_"v[76:79],a[232:235],v[44:47],v[76:79]\n"
A_"s20,s72,s88\n"
"s_addc_u32 s21,s73,0\n"
"v_add_f32 v81,v81,v160\n"
M_"v[76:79],a[228:231],v[28:31],v[76:79]\n"
"s_mul_i32 s88,s33,256\n"
"v_add_f32 v82,v82,v160\n"
"v_add_f32 v83,v83,v160\n"
M_"v[76:79],a[224:227],v[12:15],v[76:79]\n"
W_"8)\n"
A_"s88,s88,146432\n"
"v_mov_b32 v226,0\n"
E_"v80,v80\n"
E_"v81,v81\n"
E_"v82,v82\n"
E_"v83,v83\n"
C_"v130,v80,v81\n"
C_"v131,v82,v83\n"
M_"v[80:83],a[240:243],v[0:3],v[160:163]\n"
"v_add_u32 v227,s88,v227\n"
"ds_write_b32 v227,v226\n"
"s_waitcnt vmcnt(19) lgkmcnt(0)\n"
"s_barrier\n"
M_"v[80:83],a[244:247],v[16:19],v[80:83]\n"
A_"s84,s84,0x4000\n"
T_"a[128:129],v152 offset:16384\n"
"v_add_f32 v84,v84,v164\n"
M_"v[80:83],a[248:251],v[32:35],v[80:83]\n"
A_"m0,s61,32768\n"
T_"a[130:131],v152 offset:20480\n"
"v_add_f32 v85,v85,v164\n"
M_"v[80:83],a[252:255],v[48:51],v[80:83]\n"
A_"s62,s84,4096\n"
T_"a[144:145],v152 offset:16896\n"
"v_add_f32 v86,v86,v164\n"
"v_add_f32 v87,v87,v164\n"
E_"v84,v84\n"
E_"v85,v85\n"
E_"v86,v86\n"
E_"v87,v87\n"
C_"v134,v84,v85\n"
C_"v135,v86,v87\n"
M_"v[84:87],a[252:255],v[52:55],v[164:167]\n"
A_"s63,s85,128\n"
T_"a[146:147],v152 offset:20992\n"
"v_add_f32 v88,v88,v168\n"
M_"v[84:87],a[248:251],v[36:39],v[84:87]\n"
"s_mul_i32 s76,s76,9216\n"
T_"a[148:149],v152 offset:25088\n"
"v_add_f32 v89,v89,v168\n"
M_"v[84:87],a[244:247],v[20:23],v[84:87]\n"
"s_lshl_b32 s94,s94,5\n"
T_"a[150:151],v152 offset:29184\n"
"v_add_f32 v90,v90,v168\n"
M_"v[84:87],a[240:243],v[4:7],v[84:87]\n"
"s_and_b32 s17,s17,0xffff\n"
L_"v251,s[40:43],s84 offen lds\n"
"v_add_f32 v91,v91,v168\n"
E_"v88,v88\n"
E_"v89,v89\n"
E_"v90,v90\n"
E_"v91,v91\n"
C_"v138,v88,v89\n"
C_"v139,v90,v91\n"
M_"v[88:91],a[240:243],v[8:11],v[168:171]\n"
A_"m0,s61,36864\n"
"v_add_f32 v92,v92,v172\n"
"v_add_f32 v93,v93,v172\n"
M_"v[88:91],a[244:247],v[24:27],v[88:91]\n"
S_"s18,36000\n"
"v_add_f32 v94,v94,v172\n"
"v_add_f32 v95,v95,v172\n"
M_"v[88:91],a[248:251],v[40:43],v[88:91]\n"
"s_and_b32 s21,s21,0xffff\n"
E_"v92,v92\n"
M_"v[88:91],a[252:255],v[56:59],v[88:91]\n"
E_"v93,v93\n"
E_"v94,v94\n"
E_"v95,v95\n"
C_"v142,v92,v93\n"
C_"v143,v94,v95\n"
M_"v[92:95],a[252:255],v[60:63],v[172:175]\n"
U_"v199,v199,v215\n"
C_"v196,v196,v197\n"
M_"v[92:95],a[248:251],v[44:47],v[92:95]\n"
E_"v96,v96\n"
M_"v[92:95],a[244:247],v[28:31],v[92:95]\n"
E_"v97,v97\n"
M_"v[92:95],a[240:243],v[12:15],v[92:95]\n"
E_"v98,v98\n"
E_"v99,v99\n"
C_"v192,v96,v97\n"
C_"v193,v98,v99\n"
M_"v[96:99],a[192:195],v[0:3],v[160:163]\n"
C_"v197,v198,v199\n"
U_"v228,v228,v244\n"
M_"v[96:99],a[196:199],v[16:19],v[96:99]\n"
U_"v229,v229,v245\n"
U_"v230,v230,v246\n"
M_"v[96:99],a[200:203],v[32:35],v[96:99]\n"
U_"v231,v231,v247\n"
C_"v198,v228,v229\n"
M_"v[96:99],a[204:207],v[48:51],v[96:99]\n"
C_"v199,v230,v231\n"
U_"v200,v200,v216\n"
O_"v[196:199],v255,s[48:51],s91 offen offset:1024 sc1\n"
E_"v100,v100\n"
E_"v101,v101\n"
E_"v102,v102\n"
E_"v103,v103\n"
C_"v196,v100,v101\n"
C_"v197,v102,v103\n"
M_"v[100:103],a[204:207],v[52:55],v[164:167]\n"
U_"v201,v201,v217\n"
U_"v202,v202,v218\n"
M_"v[100:103],a[200:203],v[36:39],v[100:103]\n"
U_"v203,v203,v219\n"
C_"v200,v200,v201\n"
M_"v[100:103],a[196:199],v[20:23],v[100:103]\n"
C_"v201,v202,v203\n"
U_"v204,v204,v220\n"
M_"v[100:103],a[192:195],v[4:7],v[100:103]\n"
C_"v202,v232,v233\n"
C_"v203,v234,v235\n"
O_"v[200:203],v255,s[48:51],s92 offen offset:1024 sc1\n"
E_"v104,v104\n"
E_"v105,v105\n"
E_"v106,v106\n"
E_"v107,v107\n"
C_"v200,v104,v105\n"
C_"v201,v106,v107\n"
M_"v[104:107],a[192:195],v[8:11],v[168:171]\n"
L_"v251,s[40:43],s62 offen lds\n"
M_"v[104:107],a[196:199],v[24:27],v[104:107]\n"
A_"m0,s61,40960\n"
U_"v205,v205,v221\n"
U_"v206,v206,v222\n"
M_"v[104:107],a[200:203],v[40:43],v[104:107]\n"
A_"s62,s84,8192\n"
U_"v207,v207,v223\n"
C_"v204,v204,v205\n"
M_"v[104:107],a[204:207],v[56:59],v[104:107]\n"
R_"v188,a132\n"
R_"v189,a133\n"
R_"v190,a134\n"
R_"v191,a135\n"
C_"v205,v206,v207\n"
U_"v236,v236,v188\n"
U_"v237,v237,v189\n"
U_"v238,v238,v190\n"
U_"v239,v239,v191\n"
C_"v206,v236,v237\n"
C_"v207,v238,v239\n"
O_"v[204:207],v255,s[48:51],s93 offen offset:1024 sc1\n"
E_"v108,v108\n"
E_"v109,v109\n"
E_"v110,v110\n"
E_"v111,v111\n"
C_"v204,v108,v109\n"
C_"v205,v110,v111\n"
M_"v[108:111],a[204:207],v[60:63],v[172:175]\n"
T_"a[132:133],v152 offset:24576\n"
"v_bfe_u32 v250,v255,4,2\n"
M_"v[108:111],a[200:203],v[44:47],v[108:111]\n"
T_"a[134:135],v152 offset:28672\n"
"v_min_u32 v250,1,v250\n"
M_"v[108:111],a[196:199],v[28:31],v[108:111]\n"
E_"v112,v112\n"
M_"v[108:111],a[192:195],v[12:15],v[108:111]\n"
E_"v113,v113\n"
E_"v114,v114\n"
E_"v115,v115\n"
C_"v194,v112,v113\n"
C_"v195,v114,v115\n"
M_"v[112:115],a[208:211],v[0:3],v[160:163]\n"
E_"v116,v116\n"
M_"v[112:115],a[212:215],v[16:19],v[112:115]\n"
E_"v117,v117\n"
M_"v[112:115],a[216:219],v[32:35],v[112:115]\n"
E_"v118,v118\n"
M_"v[112:115],a[220:223],v[48:51],v[112:115]\n"
E_"v119,v119\n"
C_"v198,v116,v117\n"
C_"v199,v118,v119\n"
M_"v[116:119],a[220:223],v[52:55],v[164:167]\n"
L_"v251,s[40:43],s62 offen lds\n"
M_"v[116:119],a[216:219],v[36:39],v[116:119]\n"
A_"m0,s61,45056\n"
E_"v120,v120\n"
M_"v[116:119],a[212:215],v[20:23],v[116:119]\n"
A_"s62,s84,12288\n"
E_"v121,v121\n"
M_"v[116:119],a[208:211],v[4:7],v[116:119]\n"
A_"s84,s84,0x4000\n"
E_"v122,v122\n"
E_"v123,v123\n"
C_"v202,v120,v121\n"
C_"v203,v122,v123\n"
M_"v[120:123],a[208:211],v[8:11],v[168:171]\n"
E_"v124,v124\n"
M_"v[120:123],a[212:215],v[24:27],v[120:123]\n"
E_"v125,v125\n"
M_"v[120:123],a[216:219],v[40:43],v[120:123]\n"
E_"v126,v126\n"
M_"v[120:123],a[220:223],v[56:59],v[120:123]\n"
E_"v127,v127\n"
C_"v206,v124,v125\n"
C_"v207,v126,v127\n"
M_"v[124:127],a[220:223],v[60:63],v[172:175]\n"
"v_add_u32 v250,-1,v250\n"
"v_xor_b32 v153,32,v152\n"
M_"v[124:127],a[216:219],v[44:47],v[124:127]\n"
T_"a[136:137],v153 offset:16384\n"
"v_and_b32 v250,0x3f803f80,v250\n"
M_"v[124:127],a[212:215],v[28:31],v[124:127]\n"
T_"a[138:139],v153 offset:20480\n"
"v_accvgpr_write_b32 a176,v250\n"
M_"v[124:127],a[208:211],v[12:15],v[124:127]\n"
T_"a[140:141],v153 offset:24576\n"
"v_accvgpr_write_b32 a177,v250\n"
"v_accvgpr_write_b32 a178,v250\n"
"v_accvgpr_write_b32 a179,v250\n"
"s_nop 1\n"
M_"a[160:163],a[176:179],v[128:131],0\n"
T_"a[142:143],v153 offset:28672\n"
"v_add_u32 v148,0x10000,v144\n"
M_"a[160:163],a[176:179],v[192:195],a[160:163]\n"
T_"a[152:153],v153 offset:16896\n"
"v_add_u32 v149,0x10000,v145\n"
M_"a[164:167],a[176:179],v[132:135],0\n"
T_"a[154:155],v153 offset:20992\n"
"v_add_u32 v150,0x10000,v146\n"
M_"a[164:167],a[176:179],v[196:199],a[164:167]\n"
T_"a[156:157],v153 offset:25088\n"
"v_add_u32 v151,0x10000,v147\n"
M_"a[168:171],a[176:179],v[136:139],0\n"
T_"a[158:159],v153 offset:29184\n"
"v_bfe_u32 v240,v255,4,2\n"
M_"a[168:171],a[176:179],v[200:203],a[168:171]\n"
B_"a[224:227],v148 offset:0\n"
"v_mul_u32_u24 v240,0x104,v240\n"
M_"a[172:175],a[176:179],v[140:143],0\n"
B_"a[228:231],v149 offset:0\n"
"v_add_u32 v240,146432,v240\n"
M_"a[172:175],a[176:179],v[204:207],a[172:175]\n"
W_"15)\n"
B_"a[232:235],v150 offset:0\n"
"v_add_u32 v154,0x10000,v152\n"
M_"a[0:3],a[128:131],v[128:131],0\n"
W_"11)\n"
B_"a[236:239],v151 offset:0\n"
"v_add_u32 v155,0x10000,v153\n"
M_"a[0:3],a[132:135],v[192:195],a[0:3]\n"
B_"a[240:243],v148 offset:4096\n"
M_"a[4:7],a[132:135],v[196:199],0\n"
B_"a[244:247],v149 offset:4096\n"
M_"a[4:7],a[128:131],v[132:135],a[4:7]\n"
B_"a[248:251],v150 offset:4096\n"
M_"a[8:11],a[128:131],v[136:139],0\n"
B_"a[252:255],v151 offset:4096\n"
M_"a[8:11],a[132:135],v[200:203],a[8:11]\n"
B_"a[192:195],v148 offset:8192\n"
M_"a[12:15],a[132:135],v[204:207],0\n"
T_"a[132:133],v152 offset:25600\n"
M_"a[12:15],a[128:131],v[140:143],a[12:15]\n"
W_"15)\n"
T_"a[128:129],v152 offset:17408\n"
M_"a[16:19],a[136:139],v[128:131],0\n"
W_"15)\n"
T_"a[130:131],v152 offset:21504\n"
M_"a[16:19],a[140:143],v[192:195],a[16:19]\n"
T_"a[134:135],v152 offset:29696\n"
M_"a[20:23],a[140:143],v[196:199],0\n"
B_"a[196:199],v149 offset:8192\n"
M_"a[20:23],a[136:139],v[132:135],a[20:23]\n"
B_"a[200:203],v150 offset:8192\n"
M_"a[24:27],a[136:139],v[136:139],0\n"
B_"a[204:207],v151 offset:8192\n"
M_"a[24:27],a[140:143],v[200:203],a[24:27]\n"
B_"a[208:211],v148 offset:12288\n"
M_"a[28:31],a[140:143],v[204:207],0\n"
T_"a[140:141],v153 offset:25600\n"
M_"a[28:31],a[136:139],v[140:143],a[28:31]\n"
T_"a[136:137],v153 offset:17408\n"
M_"a[32:35],a[144:147],v[128:131],0\n"
T_"a[138:139],v153 offset:21504\n"
M_"a[32:35],a[148:151],v[192:195],a[32:35]\n"
T_"a[142:143],v153 offset:29696\n"
M_"a[36:39],a[148:151],v[196:199],0\n"
B_"a[212:215],v149 offset:12288\n"
M_"a[36:39],a[144:147],v[132:135],a[36:39]\n"
B_"a[216:219],v150 offset:12288\n"
M_"a[40:43],a[144:147],v[136:139],0\n"
B_"a[220:223],v151 offset:12288\n"
M_"a[40:43],a[148:151],v[200:203],a[40:43]\n"
L_"v251,s[40:43],s62 offen lds\n"
M_"a[44:47],a[148:151],v[204:207],0\n"
A_"m0,s60,147456\n"
T_"a[148:149],v152 offset:26112\n"
M_"a[44:47],a[144:147],v[140:143],a[44:47]\n"
W_"15)\n"
T_"a[144:145],v152 offset:17920\n"
M_"a[48:51],a[152:155],v[128:131],0\n"
T_"a[146:147],v152 offset:22016\n"
M_"a[48:51],a[156:159],v[192:195],a[48:51]\n"
T_"a[150:151],v152 offset:30208\n"
M_"a[52:55],a[156:159],v[196:199],0\n"
L_"v252,s[44:47],s85 offen lds\n"
M_"a[52:55],a[152:155],v[132:135],a[52:55]\n"
L_"v252,s[44:47],s85 offen offset:2048 lds\n"
M_"a[56:59],a[152:155],v[136:139],0\n"
A_"m0,s60,148480\n"
E_"v64,v64\n"
M_"a[56:59],a[156:159],v[200:203],a[56:59]\n"
A_"s85,s85,0x4000\n"
E_"v65,v65\n"
M_"a[60:63],a[156:159],v[204:207],0\n"
T_"a[156:157],v153 offset:26112\n"
C_"v176,v64,v65\n"
M_"a[60:63],a[152:155],v[140:143],a[60:63]\n"
W_"15)\n"
T_"a[152:153],v153 offset:17920\n"
M_"a[64:67],a[128:131],v[128:131],0\n"
T_"a[154:155],v153 offset:22016\n"
M_"a[64:67],a[132:135],v[192:195],a[64:67]\n"
T_"a[158:159],v153 offset:30208\n"
M_"a[68:71],a[132:135],v[196:199],0\n"
E_"v66,v66\n"
M_"a[68:71],a[128:131],v[132:135],a[68:71]\n"
E_"v67,v67\n"
M_"a[72:75],a[128:131],v[136:139],0\n"
E_"v68,v68\n"
M_"a[72:75],a[132:135],v[200:203],a[72:75]\n"
E_"v69,v69\n"
M_"a[76:79],a[132:135],v[204:207],0\n"
E_"v70,v70\n"
M_"a[76:79],a[128:131],v[140:143],a[76:79]\n"
W_"12)\n"
E_"v71,v71\n"
M_"a[80:83],a[136:139],v[128:131],0\n"
W_"11)\n"
E_"v72,v72\n"
M_"a[80:83],a[140:143],v[192:195],a[80:83]\n"
E_"v73,v73\n"
M_"a[84:87],a[140:143],v[196:199],0\n"
E_"v74,v74\n"
M_"a[84:87],a[136:139],v[132:135],a[84:87]\n"
L_"v252,s[44:47],s63 offen lds\n"
M_"a[88:91],a[136:139],v[136:139],0\n"
E_"v75,v75\n"
M_"a[88:91],a[140:143],v[200:203],a[88:91]\n"
E_"v76,v76\n"
M_"a[92:95],a[140:143],v[204:207],0\n"
E_"v77,v77\n"
M_"a[92:95],a[136:139],v[140:143],a[92:95]\n"
W_"5)\n"
E_"v78,v78\n"
M_"a[96:99],a[144:147],v[128:131],0\n"
W_"4)\n"
E_"v79,v79\n"
M_"a[96:99],a[148:151],v[192:195],a[96:99]\n"
E_"v80,v80\n"
M_"a[100:103],a[148:151],v[196:199],0\n"
E_"v81,v81\n"
M_"a[100:103],a[144:147],v[132:135],a[100:103]\n"
E_"v82,v82\n"
M_"a[104:107],a[144:147],v[136:139],0\n"
E_"v83,v83\n"
M_"a[104:107],a[148:151],v[200:203],a[104:107]\n"
C_"v177,v66,v67\n"
C_"v180,v68,v69\n"
M_"a[108:111],a[148:151],v[204:207],0\n"
E_"v84,v84\n"
M_"a[108:111],a[144:147],v[140:143],a[108:111]\n"
W_"1)\n"
E_"v85,v85\n"
M_"a[112:115],a[152:155],v[128:131],0\n"
W_"0)\n"
E_"v86,v86\n"
M_"a[112:115],a[156:159],v[192:195],a[112:115]\n"
C_"v181,v70,v71\n"
C_"v184,v72,v73\n"
M_"a[116:119],a[156:159],v[196:199],0\n"
E_"v87,v87\n"
M_"a[116:119],a[152:155],v[132:135],a[116:119]\n"
L_"v252,s[44:47],s63 offen offset:2048 lds\n"
M_"a[120:123],a[152:155],v[136:139],0\n"
E_"v88,v88\n"
M_"a[120:123],a[156:159],v[200:203],a[120:123]\n"
C_"v185,v74,v75\n"
C_"v188,v76,v77\n"
M_"a[124:127],a[156:159],v[204:207],0\n"
E_"v89,v89\n"
M_"a[124:127],a[152:155],v[140:143],a[124:127]\n"
"v_add_u32 v226,1,v226\n"
C_"v189,v78,v79\n"
M_"v[64:67],a[224:227],v[0:3],v[160:163]\n"
E_"v90,v90\n"
M_"v[64:67],a[228:231],v[16:19],v[64:67]\n"
E_"v91,v91\n"
M_"v[64:67],a[232:235],v[32:35],v[64:67]\n"
E_"v92,v92\n"
M_"v[64:67],a[236:239],v[48:51],v[64:67]\n"
E_"v93,v93\n"
M_"v[68:71],a[236:239],v[52:55],v[164:167]\n"
E_"v94,v94\n"
M_"v[68:71],a[232:235],v[36:39],v[68:71]\n"
C_"v178,v80,v81\n"
C_"v179,v82,v83\n"
M_"v[68:71],a[228:231],v[20:23],v[68:71]\n"
"s_waitcnt vmcnt(15)\n"
"ds_write_b32 v227,v226\n"
C_"v182,v84,v85\n"
M_"v[68:71],a[224:227],v[4:7],v[68:71]\n"
E_"v95,v95\n"
M_"v[72:75],a[224:227],v[8:11],v[168:171]\n"
E_"v96,v96\n"
M_"v[72:75],a[228:231],v[24:27],v[72:75]\n"
C_"v183,v86,v87\n"
C_"v186,v88,v89\n"
M_"v[72:75],a[232:235],v[40:43],v[72:75]\n"
E_"v97,v97\n"
M_"v[72:75],a[236:239],v[56:59],v[72:75]\n"
E_"v98,v98\n"
M_"v[76:79],a[236:239],v[60:63],v[172:175]\n"
E_"v99,v99\n"
M_"v[76:79],a[232:235],v[44:47],v[76:79]\n"
C_"v187,v90,v91\n"
C_"v190,v92,v93\n"
M_"v[76:79],a[228:231],v[28:31],v[76:79]\n"
E_"v100,v100\n"
M_"v[76:79],a[224:227],v[12:15],v[76:79]\n"
E_"v101,v101\n"
M_"v[80:83],a[240:243],v[0:3],v[160:163]\n"
E_"v102,v102\n"
M_"v[80:83],a[244:247],v[16:19],v[80:83]\n"
C_"v191,v94,v95\n"
C_"v208,v96,v97\n"
M_"v[80:83],a[248:251],v[32:35],v[80:83]\n"
E_"v103,v103\n"
M_"v[80:83],a[252:255],v[48:51],v[80:83]\n"
"ds_read_b32 v241,v240 offset:0\n"
C_"v209,v98,v99\n"
M_"v[84:87],a[252:255],v[52:55],v[164:167]\n"
E_"v104,v104\n"
M_"v[84:87],a[248:251],v[36:39],v[84:87]\n"
E_"v105,v105\n"
M_"v[84:87],a[244:247],v[20:23],v[84:87]\n"
E_"v106,v106\n"
M_"v[84:87],a[240:243],v[4:7],v[84:87]\n"
E_"v107,v107\n"
M_"v[88:91],a[240:243],v[8:11],v[168:171]\n"
E_"v108,v108\n"
M_"v[88:91],a[244:247],v[24:27],v[88:91]\n"
C_"v212,v100,v101\n"
C_"v213,v102,v103\n"
M_"v[88:91],a[248:251],v[40:43],v[88:91]\n"
W_"0)\n"
"v_cmp_lt_u32 vcc,v241,v226\n"
C_"v216,v104,v105\n"
M_"v[88:91],a[252:255],v[56:59],v[88:91]\n"
E_"v109,v109\n"
M_"v[92:95],a[252:255],v[60:63],v[172:175]\n"
"s_cbranch_vccnz SPINBB2_%=\n"
"BACKBB2_%=:\n"
A_"m0,s61,65536\n"
M_"v[92:95],a[248:251],v[44:47],v[92:95]\n"
A_"s62,s84,4096\n"
T_"a[128:129],v152 offset:49152\n"
C_"v217,v106,v107\n"
M_"v[92:95],a[244:247],v[28:31],v[92:95]\n"
A_"s63,s85,128\n"
E_"v110,v110\n"
M_"v[92:95],a[240:243],v[12:15],v[92:95]\n"
T_"a[130:131],v152 offset:53248\n"
C_"v220,v108,v109\n"
M_"v[96:99],a[192:195],v[0:3],v[160:163]\n"
L_"v251,s[40:43],s84 offen lds\n"
M_"v[96:99],a[196:199],v[16:19],v[96:99]\n"
A_"m0,s61,69632\n"
T_"a[132:133],v152 offset:57344\n"
"v_lshrrev_b32 v224,5,v255\n"
M_"v[96:99],a[200:203],v[32:35],v[96:99]\n"
E_"v111,v111\n"
M_"v[96:99],a[204:207],v[48:51],v[96:99]\n"
T_"a[134:135],v152 offset:61440\n"
C_"v221,v110,v111\n"
M_"v[100:103],a[204:207],v[52:55],v[164:167]\n"
E_"v112,v112\n"
M_"v[100:103],a[200:203],v[36:39],v[100:103]\n"
T_"a[136:137],v153 offset:49152\n"
"v_mul_u32_u24 v224,0x480,v224\n"
M_"v[100:103],a[196:199],v[20:23],v[100:103]\n"
E_"v113,v113\n"
M_"v[100:103],a[192:195],v[4:7],v[100:103]\n"
T_"a[138:139],v153 offset:53248\n"
C_"v210,v112,v113\n"
M_"v[104:107],a[192:195],v[8:11],v[168:171]\n"
E_"v114,v114\n"
M_"v[104:107],a[196:199],v[24:27],v[104:107]\n"
T_"a[140:141],v153 offset:57344\n"
"v_and_b32 v225,16,v255\n"
M_"v[104:107],a[200:203],v[40:43],v[104:107]\n"
E_"v115,v115\n"
M_"v[104:107],a[204:207],v[56:59],v[104:107]\n"
T_"a[142:143],v153 offset:61440\n"
C_"v211,v114,v115\n"
M_"v[108:111],a[204:207],v[60:63],v[172:175]\n"
E_"v116,v116\n"
M_"v[108:111],a[200:203],v[44:47],v[108:111]\n"
T_"a[144:145],v152 offset:49664\n"
"v_add_u32 v224,v224,v225\n"
M_"v[108:111],a[196:199],v[28:31],v[108:111]\n"
E_"v117,v117\n"
M_"v[108:111],a[192:195],v[12:15],v[108:111]\n"
T_"a[146:147],v152 offset:53760\n"
C_"v214,v116,v117\n"
M_"v[112:115],a[208:211],v[0:3],v[160:163]\n"
L_"v251,s[40:43],s62 offen lds\n"
M_"v[112:115],a[212:215],v[16:19],v[112:115]\n"
A_"m0,s61,73728\n"
T_"a[148:149],v152 offset:57856\n"
"v_bfe_u32 v225,v255,6,4\n"
M_"v[112:115],a[216:219],v[32:35],v[112:115]\n"
A_"s62,s84,8192\n"
E_"v118,v118\n"
M_"v[112:115],a[220:223],v[48:51],v[112:115]\n"
T_"a[150:151],v152 offset:61952\n"
"v_lshlrev_b32 v225,5,v225\n"
E_"v119,v119\n"
"s_nop 0\n"
C_"v215,v118,v119\n"
M_"v[116:119],a[220:223],v[52:55],v[164:167]\n"
E_"v120,v120\n"
M_"v[116:119],a[216:219],v[36:39],v[116:119]\n"
T_"a[152:153],v153 offset:49664\n"
"v_bfe_u32 v243,v255,4,2\n"
M_"v[116:119],a[212:215],v[20:23],v[116:119]\n"
E_"v121,v121\n"
M_"v[116:119],a[208:211],v[4:7],v[116:119]\n"
T_"a[154:155],v153 offset:53760\n"
C_"v218,v120,v121\n"
E_"v122,v122\n"
E_"v123,v123\n"
"s_nop 0\n"
C_"v219,v122,v123\n"
M_"v[120:123],a[208:211],v[8:11],v[168:171]\n"
E_"v124,v124\n"
M_"v[120:123],a[212:215],v[24:27],v[120:123]\n"
T_"a[156:157],v153 offset:57856\n"
"v_lshl_add_u32 v225,v243,3,v225\n"
M_"v[120:123],a[216:219],v[40:43],v[120:123]\n"
E_"v125,v125\n"
M_"v[120:123],a[220:223],v[56:59],v[120:123]\n"
T_"a[158:159],v153 offset:61952\n"
C_"v222,v124,v125\n"
E_"v126,v126\n"
E_"v127,v127\n"
"s_nop 0\n"
C_"v223,v126,v127\n"
M_"v[124:127],a[220:223],v[60:63],v[172:175]\n"
E_"v64,v64\n"
M_"v[124:127],a[216:219],v[44:47],v[124:127]\n"
B_"a[224:227],v148 offset:32768\n"
"v_lshlrev_b32 v243,16,v255\n"
M_"v[124:127],a[212:215],v[28:31],v[124:127]\n"
E_"v65,v65\n"
M_"v[124:127],a[208:211],v[12:15],v[124:127]\n"
B_"a[228:231],v149 offset:32768\n"
C_"v128,v64,v65\n"
M_"a[160:163],a[176:179],v[176:179],a[160:163]\n"
L_"v251,s[40:43],s62 offen lds\n"
M_"a[160:163],a[176:179],v[208:211],a[160:163]\n"
A_"m0,s61,77824\n"
B_"a[232:235],v150 offset:32768\n"
"v_mov_b32 v244,s66\n"
M_"a[164:167],a[176:179],v[180:183],a[164:167]\n"
A_"s62,s84,12288\n"
E_"v66,v66\n"
M_"a[164:167],a[176:179],v[212:215],a[164:167]\n"
A_"s84,s84,0x4000\n"
B_"a[236:239],v151 offset:32768\n"
"v_mov_b32 v245,s67\n"
M_"a[168:171],a[176:179],v[184:187],a[168:171]\n"
E_"v67,v67\n"
M_"a[168:171],a[176:179],v[216:219],a[168:171]\n"
B_"a[240:243],v148 offset:36864\n"
C_"v129,v66,v67\n"
M_"a[172:175],a[176:179],v[188:191],a[172:175]\n"
E_"v68,v68\n"
M_"a[172:175],a[176:179],v[220:223],a[172:175]\n"
W_"15)\n"
B_"a[244:247],v149 offset:36864\n"
"v_add_u32 v236,147456,v152\n"
M_"a[0:3],a[128:131],v[176:179],a[0:3]\n"
E_"v69,v69\n"
M_"a[0:3],a[132:135],v[208:211],a[0:3]\n"
B_"a[248:251],v150 offset:36864\n"
C_"v132,v68,v69\n"
M_"a[4:7],a[132:135],v[212:215],a[4:7]\n"
E_"v70,v70\n"
M_"a[4:7],a[128:131],v[180:183],a[4:7]\n"
B_"a[252:255],v151 offset:36864\n"
"v_add_u32 v237,147456,v153\n"
M_"a[8:11],a[128:131],v[184:187],a[8:11]\n"
E_"v71,v71\n"
M_"a[8:11],a[132:135],v[216:219],a[8:11]\n"
B_"a[192:195],v148 offset:40960\n"
C_"v133,v70,v71\n"
M_"a[12:15],a[132:135],v[220:223],a[12:15]\n"
E_"v72,v72\n"
M_"a[12:15],a[128:131],v[188:191],a[12:15]\n"
T_"a[128:129],v152 offset:50176\n"
"v_add_u32 v225,s35,v225\n"
M_"a[16:19],a[136:139],v[176:179],a[16:19]\n"
W_"15)\n"
L_"v251,s[40:43],s62 offen lds\n"
M_"a[16:19],a[140:143],v[208:211],a[16:19]\n"
A_"m0,s60,16384\n"
T_"a[130:131],v152 offset:54272\n"
M_"a[20:23],a[140:143],v[212:215],a[20:23]\n"
E_"v73,v73\n"
M_"a[20:23],a[136:139],v[180:183],a[20:23]\n"
T_"a[132:133],v152 offset:58368\n"
C_"v136,v72,v73\n"
M_"a[24:27],a[136:139],v[184:187],a[24:27]\n"
E_"v74,v74\n"
M_"a[24:27],a[140:143],v[216:219],a[24:27]\n"
T_"a[134:135],v152 offset:62464\n"
M_"a[28:31],a[140:143],v[220:223],a[28:31]\n"
E_"v75,v75\n"
M_"a[28:31],a[136:139],v[188:191],a[28:31]\n"
T_"a[136:137],v153 offset:50176\n"
C_"v137,v74,v75\n"
M_"a[32:35],a[144:147],v[176:179],a[32:35]\n"
W_"15)\n"
E_"v76,v76\n"
M_"a[32:35],a[148:151],v[208:211],a[32:35]\n"
T_"a[138:139],v153 offset:54272\n"
M_"a[36:39],a[148:151],v[212:215],a[36:39]\n"
E_"v77,v77\n"
M_"a[36:39],a[144:147],v[180:183],a[36:39]\n"
T_"a[140:141],v153 offset:58368\n"
C_"v140,v76,v77\n"
M_"a[40:43],a[144:147],v[184:187],a[40:43]\n"
E_"v78,v78\n"
M_"a[40:43],a[148:151],v[216:219],a[40:43]\n"
T_"a[142:143],v153 offset:62464\n"
M_"a[44:47],a[148:151],v[220:223],a[44:47]\n"
E_"v79,v79\n"
M_"a[44:47],a[144:147],v[188:191],a[44:47]\n"
T_"a[144:145],v152 offset:50688\n"
C_"v141,v78,v79\n"
M_"a[48:51],a[152:155],v[176:179],a[48:51]\n"
W_"15)\n"
L_"v252,s[44:47],s85 offen lds\n"
M_"a[48:51],a[156:159],v[208:211],a[48:51]\n"
T_"a[146:147],v152 offset:54784\n"
M_"a[52:55],a[156:159],v[212:215],a[52:55]\n"
E_"v80,v80\n"
M_"a[52:55],a[152:155],v[180:183],a[52:55]\n"
T_"a[148:149],v152 offset:58880\n"
M_"a[56:59],a[152:155],v[184:187],a[56:59]\n"
E_"v81,v81\n"
M_"a[56:59],a[156:159],v[216:219],a[56:59]\n"
T_"a[150:151],v152 offset:62976\n"
C_"v130,v80,v81\n"
M_"a[60:63],a[156:159],v[220:223],a[60:63]\n"
E_"v82,v82\n"
M_"a[60:63],a[152:155],v[188:191],a[60:63]\n"
W_"10)\n"
T_"a[152:153],v153 offset:50688\n"
M_"a[64:67],a[128:131],v[176:179],a[64:67]\n"
W_"9)\n"
E_"v83,v83\n"
M_"a[64:67],a[132:135],v[208:211],a[64:67]\n"
T_"a[154:155],v153 offset:54784\n"
C_"v131,v82,v83\n"
M_"a[68:71],a[132:135],v[212:215],a[68:71]\n"
E_"v84,v84\n"
M_"a[68:71],a[128:131],v[180:183],a[68:71]\n"
T_"a[156:157],v153 offset:58880\n"
M_"a[72:75],a[128:131],v[184:187],a[72:75]\n"
E_"v85,v85\n"
M_"a[72:75],a[132:135],v[216:219],a[72:75]\n"
T_"a[158:159],v153 offset:62976\n"
C_"v134,v84,v85\n"
M_"a[76:79],a[132:135],v[220:223],a[76:79]\n"
E_"v86,v86\n"
M_"a[76:79],a[128:131],v[188:191],a[76:79]\n"
W_"10)\n"
B_"a[196:199],v149 offset:40960\n"
M_"a[80:83],a[136:139],v[176:179],a[80:83]\n"
W_"9)\n"
L_"v252,s[44:47],s85 offen offset:2048 lds\n"
M_"a[80:83],a[140:143],v[208:211],a[80:83]\n"
A_"m0,s60,17408\n"
B_"a[200:203],v150 offset:40960\n"
M_"a[84:87],a[140:143],v[212:215],a[84:87]\n"
A_"s85,s85,0x4000\n"
E_"v87,v87\n"
M_"a[84:87],a[136:139],v[180:183],a[84:87]\n"
B_"a[204:207],v151 offset:40960\n"
C_"v135,v86,v87\n"
M_"a[88:91],a[136:139],v[184:187],a[88:91]\n"
E_"v88,v88\n"
M_"a[88:91],a[140:143],v[216:219],a[88:91]\n"
B_"a[208:211],v148 offset:45056\n"
M_"a[92:95],a[140:143],v[220:223],a[92:95]\n"
E_"v89,v89\n"
M_"a[92:95],a[136:139],v[188:191],a[92:95]\n"
W_"10)\n"
B_"a[212:215],v149 offset:45056\n"
C_"v138,v88,v89\n"
M_"a[96:99],a[144:147],v[176:179],a[96:99]\n"
W_"9)\n"
E_"v90,v90\n"
M_"a[96:99],a[148:151],v[208:211],a[96:99]\n"
B_"a[216:219],v150 offset:45056\n"
M_"a[100:103],a[148:151],v[212:215],a[100:103]\n"
E_"v91,v91\n"
M_"a[100:103],a[144:147],v[180:183],a[100:103]\n"
B_"a[220:223],v151 offset:45056\n"
C_"v139,v90,v91\n"
M_"a[104:107],a[144:147],v[184:187],a[104:107]\n"
E_"v92,v92\n"
M_"a[104:107],a[148:151],v[216:219],a[104:107]\n"
E_"v93,v93\n"
M_"a[108:111],a[148:151],v[220:223],a[108:111]\n"
E_"v94,v94\n"
M_"a[108:111],a[144:147],v[188:191],a[108:111]\n"
W_"9)\n"
L_"v252,s[44:47],s63 offen lds\n"
M_"a[112:115],a[152:155],v[176:179],a[112:115]\n"
W_"7)\n"
E_"v95,v95\n"
M_"a[112:115],a[156:159],v[208:211],a[112:115]\n"
E_"v96,v96\n"
M_"a[116:119],a[156:159],v[212:215],a[116:119]\n"
E_"v97,v97\n"
M_"a[116:119],a[152:155],v[180:183],a[116:119]\n"
E_"v98,v98\n"
M_"a[120:123],a[152:155],v[184:187],a[120:123]\n"
E_"v99,v99\n"
M_"a[120:123],a[156:159],v[216:219],a[120:123]\n"
E_"v100,v100\n"
M_"a[124:127],a[156:159],v[220:223],a[124:127]\n"
E_"v101,v101\n"
M_"a[124:127],a[152:155],v[188:191],a[124:127]\n"
"v_add_u32 v226,1,v226\n"
C_"v142,v92,v93\n"
M_"v[64:67],a[224:227],v[0:3],v[160:163]\n"
E_"v102,v102\n"
M_"v[64:67],a[228:231],v[16:19],v[64:67]\n"
E_"v103,v103\n"
M_"v[64:67],a[232:235],v[32:35],v[64:67]\n"
E_"v104,v104\n"
M_"v[64:67],a[236:239],v[48:51],v[64:67]\n"
E_"v105,v105\n"
M_"v[68:71],a[236:239],v[52:55],v[164:167]\n"
E_"v106,v106\n"
M_"v[68:71],a[232:235],v[36:39],v[68:71]\n"
E_"v107,v107\n"
M_"v[68:71],a[228:231],v[20:23],v[68:71]\n"
"s_waitcnt vmcnt(18) lgkmcnt(0)\n"
"ds_write_b32 v227,v226\n"
C_"v143,v94,v95\n"
M_"v[68:71],a[224:227],v[4:7],v[68:71]\n"
L_"v252,s[44:47],s63 offen offset:2048 lds\n"
M_"v[72:75],a[224:227],v[8:11],v[168:171]\n"
E_"v108,v108\n"
M_"v[72:75],a[228:231],v[24:27],v[72:75]\n"
E_"v109,v109\n"
M_"v[72:75],a[232:235],v[40:43],v[72:75]\n"
E_"v110,v110\n"
M_"v[72:75],a[236:239],v[56:59],v[72:75]\n"
E_"v111,v111\n"
M_"v[76:79],a[236:239],v[60:63],v[172:175]\n"
E_"v112,v112\n"
M_"v[76:79],a[232:235],v[44:47],v[76:79]\n"
E_"v113,v113\n"
M_"v[76:79],a[228:231],v[28:31],v[76:79]\n"
E_"v114,v114\n"
M_"v[76:79],a[224:227],v[12:15],v[76:79]\n"
E_"v115,v115\n"
M_"v[80:83],a[240:243],v[0:3],v[160:163]\n"
E_"v116,v116\n"
M_"v[80:83],a[244:247],v[16:19],v[80:83]\n"
E_"v117,v117\n"
M_"v[80:83],a[248:251],v[32:35],v[80:83]\n"
E_"v118,v118\n"
M_"v[80:83],a[252:255],v[48:51],v[80:83]\n"
"ds_read_b32 v241,v240 offset:0\n"
C_"v192,v96,v97\n"
M_"v[84:87],a[252:255],v[52:55],v[164:167]\n"
E_"v119,v119\n"
M_"v[84:87],a[248:251],v[36:39],v[84:87]\n"
C_"v193,v98,v99\n"
C_"v196,v100,v101\n"
M_"v[84:87],a[244:247],v[20:23],v[84:87]\n"
C_"v197,v102,v103\n"
C_"v200,v104,v105\n"
M_"v[84:87],a[240:243],v[4:7],v[84:87]\n"
E_"v120,v120\n"
M_"v[88:91],a[240:243],v[8:11],v[168:171]\n"
C_"v201,v106,v107\n"
C_"v204,v108,v109\n"
M_"v[88:91],a[244:247],v[24:27],v[88:91]\n"
E_"v121,v121\n"
M_"v[88:91],a[248:251],v[40:43],v[88:91]\n"
W_"0)\n"
"v_cmp_lt_u32 vcc,v241,v226\n"
C_"v205,v110,v111\n"
M_"v[88:91],a[252:255],v[56:59],v[88:91]\n"
E_"v122,v122\n"
M_"v[92:95],a[252:255],v[60:63],v[172:175]\n"
"s_cbranch_vccnz SPINBB3_%=\n"
"BACKBB3_%=:\n"
A_"m0,s61,98304\n"
M_"v[92:95],a[248:251],v[44:47],v[92:95]\n"
A_"s62,s84,4096\n"
T_"a[128:129],v154 offset:16384\n"
C_"v194,v112,v113\n"
M_"v[92:95],a[244:247],v[28:31],v[92:95]\n"
A_"s63,s85,128\n"
E_"v123,v123\n"
M_"v[92:95],a[240:243],v[12:15],v[92:95]\n"
T_"a[130:131],v154 offset:20480\n"
C_"v195,v114,v115\n"
M_"v[96:99],a[192:195],v[0:3],v[160:163]\n"
L_"v251,s[40:43],s84 offen lds\n"
M_"v[96:99],a[196:199],v[16:19],v[96:99]\n"
A_"m0,s61,102400\n"
T_"a[132:133],v154 offset:24576\n"
C_"v198,v116,v117\n"
M_"v[96:99],a[200:203],v[32:35],v[96:99]\n"
E_"v124,v124\n"
M_"v[96:99],a[204:207],v[48:51],v[96:99]\n"
T_"a[134:135],v154 offset:28672\n"
C_"v199,v118,v119\n"
M_"v[100:103],a[204:207],v[52:55],v[164:167]\n"
E_"v125,v125\n"
M_"v[100:103],a[200:203],v[36:39],v[100:103]\n"
T_"a[136:137],v155 offset:16384\n"
C_"v202,v120,v121\n"
M_"v[100:103],a[196:199],v[20:23],v[100:103]\n"
E_"v126,v126\n"
M_"v[100:103],a[192:195],v[4:7],v[100:103]\n"
T_"a[138:139],v155 offset:20480\n"
C_"v203,v122,v123\n"
M_"v[104:107],a[192:195],v[8:11],v[168:171]\n"
E_"v127,v127\n"
M_"v[104:107],a[196:199],v[24:27],v[104:107]\n"
T_"a[140:141],v155 offset:24576\n"
C_"v206,v124,v125\n"
M_"v[104:107],a[200:203],v[40:43],v[104:107]\n"
E_"v64,v64\n"
M_"v[104:107],a[204:207],v[56:59],v[104:107]\n"
T_"a[142:143],v155 offset:28672\n"
C_"v207,v126,v127\n"
M_"v[108:111],a[204:207],v[60:63],v[172:175]\n"
E_"v65,v65\n"
M_"v[108:111],a[200:203],v[44:47],v[108:111]\n"
T_"a[144:145],v154 offset:16896\n"
C_"v176,v64,v65\n"
M_"v[108:111],a[196:199],v[28:31],v[108:111]\n"
E_"v66,v66\n"
M_"v[108:111],a[192:195],v[12:15],v[108:111]\n"
T_"a[146:147],v154 offset:20992\n"
M_"v[112:115],a[208:211],v[0:3],v[160:163]\n"
L_"v251,s[40:43],s62 offen lds\n"
M_"v[112:115],a[212:215],v[16:19],v[112:115]\n"
A_"m0,s61,106496\n"
T_"a[148:149],v154 offset:25088\n"
M_"v[112:115],a[216:219],v[32:35],v[112:115]\n"
A_"s62,s84,8192\n"
E_"v67,v67\n"
M_"v[112:115],a[220:223],v[48:51],v[112:115]\n"
T_"a[150:151],v154 offset:29184\n"
C_"v177,v66,v67\n"
M_"v[116:119],a[220:223],v[52:55],v[164:167]\n"
E_"v68,v68\n"
M_"v[116:119],a[216:219],v[36:39],v[116:119]\n"
T_"a[152:153],v155 offset:16896\n"
M_"v[116:119],a[212:215],v[20:23],v[116:119]\n"
E_"v69,v69\n"
M_"v[116:119],a[208:211],v[4:7],v[116:119]\n"
T_"a[154:155],v155 offset:20992\n"
C_"v180,v68,v69\n"
M_"v[120:123],a[208:211],v[8:11],v[168:171]\n"
E_"v70,v70\n"
M_"v[120:123],a[212:215],v[24:27],v[120:123]\n"
T_"a[156:157],v155 offset:25088\n"
M_"v[120:123],a[216:219],v[40:43],v[120:123]\n"
E_"v71,v71\n"
M_"v[120:123],a[220:223],v[56:59],v[120:123]\n"
T_"a[158:159],v155 offset:29184\n"
C_"v181,v70,v71\n"
M_"v[124:127],a[220:223],v[60:63],v[172:175]\n"
E_"v72,v72\n"
M_"v[124:127],a[216:219],v[44:47],v[124:127]\n"
B_"a[224:227],v144 offset:0\n"
M_"v[124:127],a[212:215],v[28:31],v[124:127]\n"
E_"v73,v73\n"
M_"v[124:127],a[208:211],v[12:15],v[124:127]\n"
B_"a[228:231],v145 offset:0\n"
C_"v184,v72,v73\n"
M_"a[160:163],a[176:179],v[128:131],a[160:163]\n"
L_"v251,s[40:43],s62 offen lds\n"
M_"a[160:163],a[176:179],v[192:195],a[160:163]\n"
A_"m0,s61,110592\n"
B_"a[232:235],v146 offset:0\n"
M_"a[164:167],a[176:179],v[132:135],a[164:167]\n"
A_"s62,s84,12288\n"
E_"v74,v74\n"
M_"a[164:167],a[176:179],v[196:199],a[164:167]\n"
A_"s84,s84,0x4000\n"
B_"a[236:239],v147 offset:0\n"
M_"a[168:171],a[176:179],v[136:139],a[168:171]\n"
E_"v75,v75\n"
M_"a[168:171],a[176:179],v[200:203],a[168:171]\n"
B_"a[240:243],v144 offset:4096\n"
C_"v185,v74,v75\n"
M_"a[172:175],a[176:179],v[140:143],a[172:175]\n"
E_"v76,v76\n"
M_"a[172:175],a[176:179],v[204:207],a[172:175]\n"
W_"15)\n"
B_"a[244:247],v145 offset:4096\n"
M_"a[0:3],a[128:131],v[128:131],a[0:3]\n"
E_"v77,v77\n"
M_"a[0:3],a[132:135],v[192:195],a[0:3]\n"
B_"a[248:251],v146 offset:4096\n"
C_"v188,v76,v77\n"
M_"a[4:7],a[132:135],v[196:199],a[4:7]\n"
E_"v78,v78\n"
M_"a[4:7],a[128:131],v[132:135],a[4:7]\n"
B_"a[252:255],v147 offset:4096\n"
M_"a[8:11],a[128:131],v[136:139],a[8:11]\n"
E_"v79,v79\n"
M_"a[8:11],a[132:135],v[200:203],a[8:11]\n"
B_"a[192:195],v144 offset:8192\n"
C_"v189,v78,v79\n"
M_"a[12:15],a[132:135],v[204:207],a[12:15]\n"
E_"v80,v80\n"
M_"a[12:15],a[128:131],v[140:143],a[12:15]\n"
T_"a[128:129],v154 offset:17408\n"
M_"a[16:19],a[136:139],v[128:131],a[16:19]\n"
W_"15)\n"
L_"v251,s[40:43],s62 offen lds\n"
M_"a[16:19],a[140:143],v[192:195],a[16:19]\n"
A_"m0,s60,49152\n"
T_"a[130:131],v154 offset:21504\n"
M_"a[20:23],a[140:143],v[196:199],a[20:23]\n"
E_"v81,v81\n"
M_"a[20:23],a[136:139],v[132:135],a[20:23]\n"
T_"a[132:133],v154 offset:25600\n"
C_"v178,v80,v81\n"
M_"a[24:27],a[136:139],v[136:139],a[24:27]\n"
E_"v82,v82\n"
M_"a[24:27],a[140:143],v[200:203],a[24:27]\n"
T_"a[134:135],v154 offset:29696\n"
M_"a[28:31],a[140:143],v[204:207],a[28:31]\n"
E_"v83,v83\n"
M_"a[28:31],a[136:139],v[140:143],a[28:31]\n"
T_"a[136:137],v155 offset:17408\n"
C_"v179,v82,v83\n"
M_"a[32:35],a[144:147],v[128:131],a[32:35]\n"
W_"15)\n"
E_"v84,v84\n"
M_"a[32:35],a[148:151],v[192:195],a[32:35]\n"
T_"a[138:139],v155 offset:21504\n"
M_"a[36:39],a[148:151],v[196:199],a[36:39]\n"
E_"v85,v85\n"
M_"a[36:39],a[144:147],v[132:135],a[36:39]\n"
T_"a[140:141],v155 offset:25600\n"
C_"v182,v84,v85\n"
M_"a[40:43],a[144:147],v[136:139],a[40:43]\n"
E_"v86,v86\n"
M_"a[40:43],a[148:151],v[200:203],a[40:43]\n"
T_"a[142:143],v155 offset:29696\n"
M_"a[44:47],a[148:151],v[204:207],a[44:47]\n"
E_"v87,v87\n"
M_"a[44:47],a[144:147],v[140:143],a[44:47]\n"
T_"a[144:145],v154 offset:17920\n"
C_"v183,v86,v87\n"
M_"a[48:51],a[152:155],v[128:131],a[48:51]\n"
W_"15)\n"
L_"v252,s[44:47],s85 offen lds\n"
M_"a[48:51],a[156:159],v[192:195],a[48:51]\n"
T_"a[146:147],v154 offset:22016\n"
M_"a[52:55],a[156:159],v[196:199],a[52:55]\n"
E_"v88,v88\n"
M_"a[52:55],a[152:155],v[132:135],a[52:55]\n"
T_"a[148:149],v154 offset:26112\n"
M_"a[56:59],a[152:155],v[136:139],a[56:59]\n"
E_"v89,v89\n"
M_"a[56:59],a[156:159],v[200:203],a[56:59]\n"
T_"a[150:151],v154 offset:30208\n"
C_"v186,v88,v89\n"
M_"a[60:63],a[156:159],v[204:207],a[60:63]\n"
E_"v90,v90\n"
M_"a[60:63],a[152:155],v[140:143],a[60:63]\n"
W_"10)\n"
T_"a[152:153],v155 offset:17920\n"
M_"a[64:67],a[128:131],v[128:131],a[64:67]\n"
W_"9)\n"
E_"v91,v91\n"
M_"a[64:67],a[132:135],v[192:195],a[64:67]\n"
T_"a[154:155],v155 offset:22016\n"
C_"v187,v90,v91\n"
M_"a[68:71],a[132:135],v[196:199],a[68:71]\n"
E_"v92,v92\n"
M_"a[68:71],a[128:131],v[132:135],a[68:71]\n"
T_"a[156:157],v155 offset:26112\n"
M_"a[72:75],a[128:131],v[136:139],a[72:75]\n"
E_"v93,v93\n"
M_"a[72:75],a[132:135],v[200:203],a[72:75]\n"
T_"a[158:159],v155 offset:30208\n"
C_"v190,v92,v93\n"
M_"a[76:79],a[132:135],v[204:207],a[76:79]\n"
E_"v94,v94\n"
M_"a[76:79],a[128:131],v[140:143],a[76:79]\n"
W_"10)\n"
B_"a[196:199],v145 offset:8192\n"
M_"a[80:83],a[136:139],v[128:131],a[80:83]\n"
W_"9)\n"
L_"v252,s[44:47],s85 offen offset:2048 lds\n"
M_"a[80:83],a[140:143],v[192:195],a[80:83]\n"
A_"m0,s60,50176\n"
B_"a[200:203],v146 offset:8192\n"
M_"a[84:87],a[140:143],v[196:199],a[84:87]\n"
A_"s85,s85,0x4000\n"
E_"v95,v95\n"
M_"a[84:87],a[136:139],v[132:135],a[84:87]\n"
B_"a[204:207],v147 offset:8192\n"
C_"v191,v94,v95\n"
M_"a[88:91],a[136:139],v[136:139],a[88:91]\n"
E_"v96,v96\n"
M_"a[88:91],a[140:143],v[200:203],a[88:91]\n"
B_"a[208:211],v144 offset:12288\n"
M_"a[92:95],a[140:143],v[204:207],a[92:95]\n"
E_"v97,v97\n"
M_"a[92:95],a[136:139],v[140:143],a[92:95]\n"
W_"10)\n"
B_"a[212:215],v145 offset:12288\n"
C_"v208,v96,v97\n"
M_"a[96:99],a[144:147],v[128:131],a[96:99]\n"
W_"9)\n"
E_"v98,v98\n"
M_"a[96:99],a[148:151],v[192:195],a[96:99]\n"
B_"a[216:219],v146 offset:12288\n"
M_"a[100:103],a[148:151],v[196:199],a[100:103]\n"
E_"v99,v99\n"
M_"a[100:103],a[144:147],v[132:135],a[100:103]\n"
B_"a[220:223],v147 offset:12288\n"
C_"v209,v98,v99\n"
M_"a[104:107],a[144:147],v[136:139],a[104:107]\n"
E_"v100,v100\n"
M_"a[104:107],a[148:151],v[200:203],a[104:107]\n"
E_"v101,v101\n"
M_"a[108:111],a[148:151],v[204:207],a[108:111]\n"
E_"v102,v102\n"
M_"a[108:111],a[144:147],v[140:143],a[108:111]\n"
W_"9)\n"
L_"v252,s[44:47],s63 offen lds\n"
M_"a[112:115],a[152:155],v[128:131],a[112:115]\n"
W_"7)\n"
E_"v103,v103\n"
M_"a[112:115],a[156:159],v[192:195],a[112:115]\n"
E_"v104,v104\n"
M_"a[116:119],a[156:159],v[196:199],a[116:119]\n"
E_"v105,v105\n"
M_"a[116:119],a[152:155],v[132:135],a[116:119]\n"
E_"v106,v106\n"
M_"a[120:123],a[152:155],v[136:139],a[120:123]\n"
E_"v107,v107\n"
M_"a[120:123],a[156:159],v[200:203],a[120:123]\n"
E_"v108,v108\n"
M_"a[124:127],a[156:159],v[204:207],a[124:127]\n"
C_"v212,v100,v101\n"
C_"v213,v102,v103\n"
M_"a[124:127],a[152:155],v[140:143],a[124:127]\n"
"s_sub_u32 s86,s86,1\n"
"s_nop 0\n"
M_"v[64:67],a[224:227],v[0:3],v[160:163]\n"
"v_add_u32 v226,1,v226\n"
C_"v216,v104,v105\n"
M_"v[64:67],a[228:231],v[16:19],v[64:67]\n"
E_"v109,v109\n"
M_"v[64:67],a[232:235],v[32:35],v[64:67]\n"
E_"v110,v110\n"
M_"v[64:67],a[236:239],v[48:51],v[64:67]\n"
E_"v111,v111\n"
M_"v[68:71],a[236:239],v[52:55],v[164:167]\n"
C_"v217,v106,v107\n"
C_"v220,v108,v109\n"
M_"v[68:71],a[232:235],v[36:39],v[68:71]\n"
C_"v221,v110,v111\n"
M_"v[68:71],a[228:231],v[20:23],v[68:71]\n"
"s_waitcnt vmcnt(15) lgkmcnt(0)\n"
"ds_write_b32 v227,v226\n"
M_"v[68:71],a[224:227],v[4:7],v[68:71]\n"
L_"v252,s[44:47],s63 offen offset:2048 lds\n"
M_"v[72:75],a[224:227],v[8:11],v[168:171]\n"
E_"v112,v112\n"
M_"v[72:75],a[228:231],v[24:27],v[72:75]\n"
E_"v113,v113\n"
M_"v[72:75],a[232:235],v[40:43],v[72:75]\n"
E_"v114,v114\n"
M_"v[72:75],a[236:239],v[56:59],v[72:75]\n"
C_"v210,v112,v113\n"
M_"v[76:79],a[236:239],v[60:63],v[172:175]\n"
E_"v115,v115\n"
M_"v[76:79],a[232:235],v[44:47],v[76:79]\n"
C_"v211,v114,v115\n"
M_"v[76:79],a[228:231],v[28:31],v[76:79]\n"
E_"v116,v116\n"
M_"v[76:79],a[224:227],v[12:15],v[76:79]\n"
M_"v[80:83],a[240:243],v[0:3],v[160:163]\n"
E_"v117,v117\n"
M_"v[80:83],a[244:247],v[16:19],v[80:83]\n"
C_"v214,v116,v117\n"
M_"v[80:83],a[248:251],v[32:35],v[80:83]\n"
E_"v118,v118\n"
M_"v[80:83],a[252:255],v[48:51],v[80:83]\n"
"ds_read_b32 v241,v240 offset:0\n"
M_"v[84:87],a[252:255],v[52:55],v[164:167]\n"
E_"v119,v119\n"
M_"v[84:87],a[248:251],v[36:39],v[84:87]\n"
C_"v215,v118,v119\n"
M_"v[84:87],a[244:247],v[20:23],v[84:87]\n"
M_"v[84:87],a[240:243],v[4:7],v[84:87]\n"
E_"v120,v120\n"
M_"v[88:91],a[240:243],v[8:11],v[168:171]\n"
M_"v[88:91],a[244:247],v[24:27],v[88:91]\n"
E_"v121,v121\n"
M_"v[88:91],a[248:251],v[40:43],v[88:91]\n"
W_"0)\n"
"v_cmp_lt_u32 vcc,v241,v226\n"
C_"v218,v120,v121\n"
M_"v[88:91],a[252:255],v[56:59],v[88:91]\n"
E_"v122,v122\n"
M_"v[92:95],a[252:255],v[60:63],v[172:175]\n"
"s_cbranch_vccnz SPINBC0_%=\n"
"BACKBC0_%=:\n"
A_"m0,s61,0\n"
M_"v[92:95],a[248:251],v[44:47],v[92:95]\n"
A_"s62,s84,4096\n"
T_"a[128:129],v236 offset:0\n"
M_"v[92:95],a[244:247],v[28:31],v[92:95]\n"
A_"s88,s74,8192\n"
E_"v123,v123\n"
M_"v[92:95],a[240:243],v[12:15],v[92:95]\n"
"s_sub_u32 s95,s96,4\n"
T_"a[130:131],v236 offset:4096\n"
C_"v219,v122,v123\n"
M_"v[96:99],a[192:195],v[0:3],v[160:163]\n"
"s_lshl_b32 s97,s95,10\n"
L_"v251,s[40:43],s84 offen lds\n"
M_"v[96:99],a[196:199],v[16:19],v[96:99]\n"
A_"m0,s61,4096\n"
T_"a[132:133],v236 offset:8192\n"
M_"v[96:99],a[200:203],v[32:35],v[96:99]\n"
A_"s97,s97,s76\n"
E_"v124,v124\n"
M_"v[96:99],a[204:207],v[48:51],v[96:99]\n"
A_"s63,s85,128\n"
T_"a[134:135],v236 offset:12288\n"
M_"v[100:103],a[204:207],v[52:55],v[164:167]\n"
E_"v125,v125\n"
M_"v[100:103],a[200:203],v[36:39],v[100:103]\n"
T_"a[136:137],v237 offset:0\n"
C_"v222,v124,v125\n"
M_"v[100:103],a[196:199],v[20:23],v[100:103]\n"
E_"v126,v126\n"
M_"v[100:103],a[192:195],v[4:7],v[100:103]\n"
T_"a[138:139],v237 offset:4096\n"
M_"v[104:107],a[192:195],v[8:11],v[168:171]\n"
E_"v127,v127\n"
M_"v[104:107],a[196:199],v[24:27],v[104:107]\n"
T_"a[140:141],v237 offset:8192\n"
C_"v223,v126,v127\n"
M_"v[104:107],a[200:203],v[40:43],v[104:107]\n"
E_"v64,v64\n"
M_"v[104:107],a[204:207],v[56:59],v[104:107]\n"
T_"a[142:143],v237 offset:12288\n"
M_"v[108:111],a[204:207],v[60:63],v[172:175]\n"
E_"v65,v65\n"
M_"v[108:111],a[200:203],v[44:47],v[108:111]\n"
T_"a[144:145],v236 offset:512\n"
C_"v128,v64,v65\n"
M_"v[108:111],a[196:199],v[28:31],v[108:111]\n"
E_"v66,v66\n"
M_"v[108:111],a[192:195],v[12:15],v[108:111]\n"
T_"a[146:147],v236 offset:4608\n"
M_"v[112:115],a[208:211],v[0:3],v[160:163]\n"
L_"v251,s[40:43],s62 offen lds\n"
M_"v[112:115],a[212:215],v[16:19],v[112:115]\n"
A_"m0,s61,8192\n"
T_"a[148:149],v236 offset:8704\n"
M_"v[112:115],a[216:219],v[32:35],v[112:115]\n"
A_"s62,s84,8192\n"
E_"v67,v67\n"
M_"v[112:115],a[220:223],v[48:51],v[112:115]\n"
T_"a[150:151],v236 offset:12800\n"
C_"v129,v66,v67\n"
M_"v[116:119],a[220:223],v[52:55],v[164:167]\n"
E_"v68,v68\n"
M_"v[116:119],a[216:219],v[36:39],v[116:119]\n"
"s_cmp_lt_u32 s96,16\n"
"s_cselect_b32 s88,1073741824,s88\n"
T_"a[152:153],v237 offset:512\n"
M_"v[116:119],a[212:215],v[20:23],v[116:119]\n"
A_"s96,s96,1\n"
E_"v69,v69\n"
M_"v[116:119],a[208:211],v[4:7],v[116:119]\n"
T_"a[154:155],v237 offset:4608\n"
C_"v132,v68,v69\n"
M_"v[120:123],a[208:211],v[8:11],v[168:171]\n"
"buffer_store_dwordx2 v[244:245],v243,s[56:59],s88 offen sc1\n"
M_"v[120:123],a[212:215],v[24:27],v[120:123]\n"
T_"a[156:157],v237 offset:8704\n"
M_"v[120:123],a[216:219],v[40:43],v[120:123]\n"
E_"v70,v70\n"
M_"v[120:123],a[220:223],v[56:59],v[120:123]\n"
T_"a[158:159],v237 offset:12800\n"
M_"v[124:127],a[220:223],v[60:63],v[172:175]\n"
E_"v71,v71\n"
M_"v[124:127],a[216:219],v[44:47],v[124:127]\n"
B_"a[224:227],v144 offset:32768\n"
C_"v133,v70,v71\n"
M_"v[124:127],a[212:215],v[28:31],v[124:127]\n"
E_"v72,v72\n"
M_"v[124:127],a[208:211],v[12:15],v[124:127]\n"
B_"a[228:231],v145 offset:32768\n"
M_"a[160:163],a[176:179],v[176:179],a[160:163]\n"
L_"v251,s[40:43],s62 offen lds\n"
M_"a[160:163],a[176:179],v[208:211],a[160:163]\n"
A_"m0,s61,12288\n"
B_"a[232:235],v146 offset:32768\n"
M_"a[164:167],a[176:179],v[180:183],a[164:167]\n"
A_"s62,s84,12288\n"
E_"v73,v73\n"
M_"a[164:167],a[176:179],v[212:215],a[164:167]\n"
A_"s84,s84,0x4000\n"
B_"a[236:239],v147 offset:32768\n"
C_"v136,v72,v73\n"
M_"a[168:171],a[176:179],v[184:187],a[168:171]\n"
E_"v74,v74\n"
M_"a[168:171],a[176:179],v[216:219],a[168:171]\n"
T_"v[228:229],v225 offset:0\n"
M_"a[172:175],a[176:179],v[188:191],a[172:175]\n"
E_"v75,v75\n"
M_"a[172:175],a[176:179],v[220:223],a[172:175]\n"
W_"15)\n"
T_"v[230:231],v225 offset:512\n"
C_"v137,v74,v75\n"
M_"a[0:3],a[128:131],v[176:179],a[0:3]\n"
E_"v76,v76\n"
M_"a[0:3],a[132:135],v[208:211],a[0:3]\n"
B_"a[240:243],v144 offset:36864\n"
M_"a[4:7],a[132:135],v[212:215],a[4:7]\n"
E_"v77,v77\n"
M_"a[4:7],a[128:131],v[180:183],a[4:7]\n"
B_"a[244:247],v145 offset:36864\n"
C_"v140,v76,v77\n"
M_"a[8:11],a[128:131],v[184:187],a[8:11]\n"
E_"v78,v78\n"
M_"a[8:11],a[132:135],v[216:219],a[8:11]\n"
B_"a[248:251],v146 offset:36864\n"
M_"a[12:15],a[132:135],v[220:223],a[12:15]\n"
E_"v79,v79\n"
M_"a[12:15],a[128:131],v[188:191],a[12:15]\n"
T_"a[128:129],v236 offset:1024\n"
C_"v141,v78,v79\n"
M_"a[16:19],a[136:139],v[176:179],a[16:19]\n"
W_"15)\n"
L_"v251,s[40:43],s62 offen lds\n"
M_"a[16:19],a[140:143],v[208:211],a[16:19]\n"
A_"m0,s60,81920\n"
T_"a[130:131],v236 offset:5120\n"
M_"a[20:23],a[140:143],v[212:215],a[20:23]\n"
E_"v80,v80\n"
M_"a[20:23],a[136:139],v[180:183],a[20:23]\n"
T_"a[132:133],v236 offset:9216\n"
M_"a[24:27],a[136:139],v[184:187],a[24:27]\n"
"s_cmp_eq_u32 s95,9\n"
"s_cselect_b32 s97,s77,s97\n"
M_"a[24:27],a[140:143],v[216:219],a[24:27]\n"
"s_cmp_gt_u32 s95,9\n"
"s_cselect_b32 s97,1073741824,s97\n"
T_"a[134:135],v236 offset:13312\n"
M_"a[28:31],a[140:143],v[220:223],a[28:31]\n"
E_"v81,v81\n"
M_"a[28:31],a[136:139],v[188:191],a[28:31]\n"
T_"a[136:137],v237 offset:1024\n"
C_"v130,v80,v81\n"
M_"a[32:35],a[144:147],v[176:179],a[32:35]\n"
W_"15)\n"
E_"v82,v82\n"
M_"a[32:35],a[148:151],v[208:211],a[32:35]\n"
T_"a[138:139],v237 offset:5120\n"
M_"a[36:39],a[148:151],v[212:215],a[36:39]\n"
W_"9)\n"
O_"v[228:231],v255,s[24:27],s97 offen sc1\n"
M_"a[36:39],a[144:147],v[180:183],a[36:39]\n"
T_"a[140:141],v237 offset:9216\n"
M_"a[40:43],a[144:147],v[184:187],a[40:43]\n"
E_"v83,v83\n"
M_"a[40:43],a[148:151],v[216:219],a[40:43]\n"
T_"a[142:143],v237 offset:13312\n"
C_"v131,v82,v83\n"
M_"a[44:47],a[148:151],v[220:223],a[44:47]\n"
E_"v84,v84\n"
M_"a[44:47],a[144:147],v[188:191],a[44:47]\n"
T_"a[144:145],v236 offset:1536\n"
M_"a[48:51],a[152:155],v[176:179],a[48:51]\n"
L_"v252,s[44:47],s85 offen lds\n"
M_"a[48:51],a[156:159],v[208:211],a[48:51]\n"
T_"a[146:147],v236 offset:5632\n"
M_"a[52:55],a[156:159],v[212:215],a[52:55]\n"
E_"v85,v85\n"
M_"a[52:55],a[152:155],v[180:183],a[52:55]\n"
T_"a[148:149],v236 offset:9728\n"
C_"v134,v84,v85\n"
M_"a[56:59],a[152:155],v[184:187],a[56:59]\n"
E_"v86,v86\n"
M_"a[56:59],a[156:159],v[216:219],a[56:59]\n"
T_"a[150:151],v236 offset:13824\n"
M_"a[60:63],a[156:159],v[220:223],a[60:63]\n"
E_"v87,v87\n"
M_"a[60:63],a[152:155],v[188:191],a[60:63]\n"
W_"10)\n"
T_"a[152:153],v237 offset:1536\n"
C_"v135,v86,v87\n"
M_"a[64:67],a[128:131],v[176:179],a[64:67]\n"
W_"9)\n"
E_"v88,v88\n"
M_"a[64:67],a[132:135],v[208:211],a[64:67]\n"
T_"a[154:155],v237 offset:5632\n"
M_"a[68:71],a[132:135],v[212:215],a[68:71]\n"
E_"v89,v89\n"
M_"a[68:71],a[128:131],v[180:183],a[68:71]\n"
T_"a[156:157],v237 offset:9728\n"
C_"v138,v88,v89\n"
M_"a[72:75],a[128:131],v[184:187],a[72:75]\n"
E_"v90,v90\n"
M_"a[72:75],a[132:135],v[216:219],a[72:75]\n"
T_"a[158:159],v237 offset:13824\n"
M_"a[76:79],a[132:135],v[220:223],a[76:79]\n"
E_"v91,v91\n"
M_"a[76:79],a[128:131],v[188:191],a[76:79]\n"
W_"10)\n"
B_"a[252:255],v147 offset:36864\n"
C_"v139,v90,v91\n"
M_"a[80:83],a[136:139],v[176:179],a[80:83]\n"
W_"9)\n"
L_"v252,s[44:47],s85 offen offset:2048 lds\n"
M_"a[80:83],a[140:143],v[208:211],a[80:83]\n"
A_"m0,s60,82944\n"
B_"a[192:195],v144 offset:40960\n"
M_"a[84:87],a[140:143],v[212:215],a[84:87]\n"
A_"s85,s85,0x4000\n"
E_"v92,v92\n"
M_"a[84:87],a[136:139],v[180:183],a[84:87]\n"
B_"a[196:199],v145 offset:40960\n"
M_"a[88:91],a[136:139],v[184:187],a[88:91]\n"
E_"v93,v93\n"
M_"a[88:91],a[140:143],v[216:219],a[88:91]\n"
B_"a[200:203],v146 offset:40960\n"
C_"v142,v92,v93\n"
M_"a[92:95],a[140:143],v[220:223],a[92:95]\n"
E_"v94,v94\n"
M_"a[92:95],a[136:139],v[188:191],a[92:95]\n"
W_"10)\n"
B_"a[204:207],v147 offset:40960\n"
M_"a[96:99],a[144:147],v[176:179],a[96:99]\n"
W_"9)\n"
E_"v95,v95\n"
M_"a[96:99],a[148:151],v[208:211],a[96:99]\n"
B_"a[208:211],v144 offset:45056\n"
C_"v143,v94,v95\n"
M_"a[100:103],a[148:151],v[212:215],a[100:103]\n"
E_"v96,v96\n"
M_"a[100:103],a[144:147],v[180:183],a[100:103]\n"
B_"a[212:215],v145 offset:45056\n"
M_"a[104:107],a[144:147],v[184:187],a[104:107]\n"
E_"v97,v97\n"
M_"a[104:107],a[148:151],v[216:219],a[104:107]\n"
B_"a[216:219],v146 offset:45056\n"
C_"v192,v96,v97\n"
M_"a[108:111],a[148:151],v[220:223],a[108:111]\n"
E_"v98,v98\n"
M_"a[108:111],a[144:147],v[188:191],a[108:111]\n"
W_"10)\n"
B_"a[220:223],v147 offset:45056\n"
M_"a[112:115],a[152:155],v[176:179],a[112:115]\n"
W_"9)\n"
L_"v252,s[44:47],s63 offen lds\n"
M_"a[112:115],a[156:159],v[208:211],a[112:115]\n"
E_"v99,v99\n"
M_"a[116:119],a[156:159],v[212:215],a[116:119]\n"
E_"v100,v100\n"
M_"a[116:119],a[152:155],v[180:183],a[116:119]\n"
E_"v101,v101\n"
M_"a[120:123],a[152:155],v[184:187],a[120:123]\n"
E_"v102,v102\n"
M_"a[120:123],a[156:159],v[216:219],a[120:123]\n"
E_"v103,v103\n"
M_"a[124:127],a[156:159],v[220:223],a[124:127]\n"
E_"v104,v104\n"
M_"a[124:127],a[152:155],v[188:191],a[124:127]\n"
"v_add_u32 v226,1,v226\n"
C_"v193,v98,v99\n"
M_"v[64:67],a[224:227],v[0:3],v[160:163]\n"
E_"v105,v105\n"
M_"v[64:67],a[228:231],v[16:19],v[64:67]\n"
E_"v106,v106\n"
M_"v[64:67],a[232:235],v[32:35],v[64:67]\n"
E_"v107,v107\n"
M_"v[64:67],a[236:239],v[48:51],v[64:67]\n"
E_"v108,v108\n"
M_"v[68:71],a[236:239],v[52:55],v[164:167]\n"
E_"v109,v109\n"
M_"v[68:71],a[232:235],v[36:39],v[68:71]\n"
E_"v110,v110\n"
M_"v[68:71],a[228:231],v[20:23],v[68:71]\n"
"s_waitcnt vmcnt(17) lgkmcnt(0)\n"
"ds_write_b32 v227,v226\n"
C_"v196,v100,v101\n"
M_"v[68:71],a[224:227],v[4:7],v[68:71]\n"
L_"v252,s[44:47],s63 offen offset:2048 lds\n"
M_"v[72:75],a[224:227],v[8:11],v[168:171]\n"
A_"m0,s35,0\n"
E_"v111,v111\n"
M_"v[72:75],a[228:231],v[24:27],v[72:75]\n"
L_"v224,s[16:19],s94 offen lds\n"
M_"v[72:75],a[232:235],v[40:43],v[72:75]\n"
A_"s94,s94,32\n"
E_"v112,v112\n"
M_"v[72:75],a[236:239],v[56:59],v[72:75]\n"
"s_sub_u32 s95,s94,288\n"
E_"v113,v113\n"
M_"v[76:79],a[236:239],v[60:63],v[172:175]\n"
"s_min_u32 s94,s94,s95\n"
E_"v114,v114\n"
M_"v[76:79],a[232:235],v[44:47],v[76:79]\n"
E_"v115,v115\n"
M_"v[76:79],a[228:231],v[28:31],v[76:79]\n"
E_"v116,v116\n"
M_"v[76:79],a[224:227],v[12:15],v[76:79]\n"
C_"v197,v102,v103\n"
C_"v200,v104,v105\n"
M_"v[80:83],a[240:243],v[0:3],v[160:163]\n"
E_"v117,v117\n"
M_"v[80:83],a[244:247],v[16:19],v[80:83]\n"
C_"v201,v106,v107\n"
C_"v204,v108,v109\n"
M_"v[80:83],a[248:251],v[32:35],v[80:83]\n"
E_"v118,v118\n"
M_"v[80:83],a[252:255],v[48:51],v[80:83]\n"
"ds_read_b32 v241,v240 offset:0\n"
C_"v205,v110,v111\n"
M_"v[84:87],a[252:255],v[52:55],v[164:167]\n"
E_"v119,v119\n"
M_"v[84:87],a[248:251],v[36:39],v[84:87]\n"
C_"v194,v112,v113\n"
C_"v195,v114,v115\n"
M_"v[84:87],a[244:247],v[20:23],v[84:87]\n"
C_"v198,v116,v117\n"
C_"v199,v118,v119\n"
M_"v[84:87],a[240:243],v[4:7],v[84:87]\n"
E_"v120,v120\n"
M_"v[88:91],a[240:243],v[8:11],v[168:171]\n"
"s_cmp_eq_u32 s96,9\n"
"s_cselect_b32 s18,s22,s18\n"
M_"v[88:91],a[244:247],v[24:27],v[88:91]\n"
E_"v121,v121\n"
M_"v[88:91],a[248:251],v[40:43],v[88:91]\n"
W_"0)\n"
"v_cmp_lt_u32 vcc,v241,v226\n"
C_"v202,v120,v121\n"
M_"v[88:91],a[252:255],v[56:59],v[88:91]\n"
E_"v122,v122\n"
M_"v[92:95],a[252:255],v[60:63],v[172:175]\n"
"s_cbranch_vccnz SPINBC1_%=\n"
"BACKBC1_%=:\n"
A_"m0,s61,32768\n"
M_"v[92:95],a[248:251],v[44:47],v[92:95]\n"
A_"s62,s84,4096\n"
T_"a[128:129],v152 offset:16384\n"
M_"v[92:95],a[244:247],v[28:31],v[92:95]\n"
"s_sub_u32 s95,s96,4\n"
E_"v123,v123\n"
M_"v[92:95],a[240:243],v[12:15],v[92:95]\n"
"s_lshl_b32 s97,s95,10\n"
T_"a[130:131],v152 offset:20480\n"
C_"v203,v122,v123\n"
M_"v[96:99],a[192:195],v[0:3],v[160:163]\n"
A_"s97,s97,s76\n"
L_"v251,s[40:43],s84 offen lds\n"
M_"v[96:99],a[196:199],v[16:19],v[96:99]\n"
A_"m0,s61,36864\n"
T_"a[132:133],v152 offset:24576\n"
M_"v[96:99],a[200:203],v[32:35],v[96:99]\n"
A_"s63,s85,128\n"
E_"v124,v124\n"
M_"v[96:99],a[204:207],v[48:51],v[96:99]\n"
T_"a[134:135],v152 offset:28672\n"
M_"v[100:103],a[204:207],v[52:55],v[164:167]\n"
E_"v125,v125\n"
M_"v[100:103],a[200:203],v[36:39],v[100:103]\n"
T_"a[136:137],v153 offset:16384\n"
C_"v206,v124,v125\n"
M_"v[100:103],a[196:199],v[20:23],v[100:103]\n"
E_"v126,v126\n"
M_"v[100:103],a[192:195],v[4:7],v[100:103]\n"
T_"a[138:139],v153 offset:20480\n"
M_"v[104:107],a[192:195],v[8:11],v[168:171]\n"
E_"v127,v127\n"
M_"v[104:107],a[196:199],v[24:27],v[104:107]\n"
T_"a[140:141],v153 offset:24576\n"
C_"v207,v126,v127\n"
M_"v[104:107],a[200:203],v[40:43],v[104:107]\n"
E_"v64,v64\n"
M_"v[104:107],a[204:207],v[56:59],v[104:107]\n"
T_"a[142:143],v153 offset:28672\n"
M_"v[108:111],a[204:207],v[60:63],v[172:175]\n"
E_"v65,v65\n"
M_"v[108:111],a[200:203],v[44:47],v[108:111]\n"
T_"a[144:145],v152 offset:16896\n"
C_"v176,v64,v65\n"
M_"v[108:111],a[196:199],v[28:31],v[108:111]\n"
E_"v66,v66\n"
M_"v[108:111],a[192:195],v[12:15],v[108:111]\n"
T_"a[146:147],v152 offset:20992\n"
M_"v[112:115],a[208:211],v[0:3],v[160:163]\n"
L_"v251,s[40:43],s62 offen lds\n"
M_"v[112:115],a[212:215],v[16:19],v[112:115]\n"
A_"m0,s61,40960\n"
T_"a[148:149],v152 offset:25088\n"
M_"v[112:115],a[216:219],v[32:35],v[112:115]\n"
A_"s62,s84,8192\n"
E_"v67,v67\n"
M_"v[112:115],a[220:223],v[48:51],v[112:115]\n"
T_"a[150:151],v152 offset:29184\n"
C_"v177,v66,v67\n"
M_"v[116:119],a[220:223],v[52:55],v[164:167]\n"
E_"v68,v68\n"
M_"v[116:119],a[216:219],v[36:39],v[116:119]\n"
T_"a[152:153],v153 offset:16896\n"
M_"v[116:119],a[212:215],v[20:23],v[116:119]\n"
E_"v69,v69\n"
M_"v[116:119],a[208:211],v[4:7],v[116:119]\n"
T_"a[154:155],v153 offset:20992\n"
C_"v180,v68,v69\n"
M_"v[120:123],a[208:211],v[8:11],v[168:171]\n"
E_"v70,v70\n"
M_"v[120:123],a[212:215],v[24:27],v[120:123]\n"
T_"a[156:157],v153 offset:25088\n"
M_"v[120:123],a[216:219],v[40:43],v[120:123]\n"
E_"v71,v71\n"
M_"v[120:123],a[220:223],v[56:59],v[120:123]\n"
T_"a[158:159],v153 offset:29184\n"
C_"v181,v70,v71\n"
M_"v[124:127],a[220:223],v[60:63],v[172:175]\n"
E_"v72,v72\n"
M_"v[124:127],a[216:219],v[44:47],v[124:127]\n"
B_"a[224:227],v148 offset:0\n"
M_"v[124:127],a[212:215],v[28:31],v[124:127]\n"
E_"v73,v73\n"
M_"v[124:127],a[208:211],v[12:15],v[124:127]\n"
B_"a[228:231],v149 offset:0\n"
C_"v184,v72,v73\n"
M_"a[160:163],a[176:179],v[128:131],a[160:163]\n"
L_"v251,s[40:43],s62 offen lds\n"
M_"a[160:163],a[176:179],v[192:195],a[160:163]\n"
A_"m0,s61,45056\n"
B_"a[232:235],v150 offset:0\n"
M_"a[164:167],a[176:179],v[132:135],a[164:167]\n"
A_"s62,s84,12288\n"
E_"v74,v74\n"
M_"a[164:167],a[176:179],v[196:199],a[164:167]\n"
A_"s84,s84,0x4000\n"
B_"a[236:239],v151 offset:0\n"
M_"a[168:171],a[176:179],v[136:139],a[168:171]\n"
E_"v75,v75\n"
M_"a[168:171],a[176:179],v[200:203],a[168:171]\n"
T_"v[232:233],v225 offset:1024\n"
C_"v185,v74,v75\n"
M_"a[172:175],a[176:179],v[140:143],a[172:175]\n"
E_"v76,v76\n"
M_"a[172:175],a[176:179],v[204:207],a[172:175]\n"
W_"15)\n"
T_"v[234:235],v225 offset:1536\n"
M_"a[0:3],a[128:131],v[128:131],a[0:3]\n"
E_"v77,v77\n"
M_"a[0:3],a[132:135],v[192:195],a[0:3]\n"
B_"a[240:243],v148 offset:4096\n"
C_"v188,v76,v77\n"
M_"a[4:7],a[132:135],v[196:199],a[4:7]\n"
E_"v78,v78\n"
M_"a[4:7],a[128:131],v[132:135],a[4:7]\n"
B_"a[244:247],v149 offset:4096\n"
M_"a[8:11],a[128:131],v[136:139],a[8:11]\n"
E_"v79,v79\n"
M_"a[8:11],a[132:135],v[200:203],a[8:11]\n"
B_"a[248:251],v150 offset:4096\n"
C_"v189,v78,v79\n"
M_"a[12:15],a[132:135],v[204:207],a[12:15]\n"
E_"v80,v80\n"
M_"a[12:15],a[128:131],v[140:143],a[12:15]\n"
T_"a[128:129],v152 offset:17408\n"
M_"a[16:19],a[136:139],v[128:131],a[16:19]\n"
W_"15)\n"
L_"v251,s[40:43],s62 offen lds\n"
M_"a[16:19],a[140:143],v[192:195],a[16:19]\n"
A_"m0,s60,147456\n"
T_"a[130:131],v152 offset:21504\n"
M_"a[20:23],a[140:143],v[196:199],a[20:23]\n"
E_"v81,v81\n"
M_"a[20:23],a[136:139],v[132:135],a[20:23]\n"
T_"a[132:133],v152 offset:25600\n"
C_"v178,v80,v81\n"
M_"a[24:27],a[136:139],v[136:139],a[24:27]\n"
"s_cmp_eq_u32 s95,9\n"
"s_cselect_b32 s97,s77,s97\n"
M_"a[24:27],a[140:143],v[200:203],a[24:27]\n"
"s_cmp_gt_u32 s95,9\n"
"s_cselect_b32 s97,1073741824,s97\n"
T_"a[134:135],v152 offset:29696\n"
M_"a[28:31],a[140:143],v[204:207],a[28:31]\n"
E_"v82,v82\n"
M_"a[28:31],a[136:139],v[140:143],a[28:31]\n"
T_"a[136:137],v153 offset:17408\n"
M_"a[32:35],a[144:147],v[128:131],a[32:35]\n"
W_"15)\n"
E_"v83,v83\n"
M_"a[32:35],a[148:151],v[192:195],a[32:35]\n"
T_"a[138:139],v153 offset:21504\n"
C_"v179,v82,v83\n"
M_"a[36:39],a[148:151],v[196:199],a[36:39]\n"
W_"9)\n"
O_"v[232:235],v255,s[24:27],s97 offen sc1\n"
M_"a[36:39],a[144:147],v[132:135],a[36:39]\n"
T_"a[140:141],v153 offset:25600\n"
M_"a[40:43],a[144:147],v[136:139],a[40:43]\n"
E_"v84,v84\n"
M_"a[40:43],a[148:151],v[200:203],a[40:43]\n"
T_"a[142:143],v153 offset:29696\n"
M_"a[44:47],a[148:151],v[204:207],a[44:47]\n"
E_"v85,v85\n"
M_"a[44:47],a[144:147],v[140:143],a[44:47]\n"
T_"a[144:145],v152 offset:17920\n"
C_"v182,v84,v85\n"
M_"a[48:51],a[152:155],v[128:131],a[48:51]\n"
L_"v252,s[44:47],s85 offen lds\n"
M_"a[48:51],a[156:159],v[192:195],a[48:51]\n"
T_"a[146:147],v152 offset:22016\n"
M_"a[52:55],a[156:159],v[196:199],a[52:55]\n"
E_"v86,v86\n"
M_"a[52:55],a[152:155],v[132:135],a[52:55]\n"
T_"a[148:149],v152 offset:26112\n"
M_"a[56:59],a[152:155],v[136:139],a[56:59]\n"
E_"v87,v87\n"
M_"a[56:59],a[156:159],v[200:203],a[56:59]\n"
"s_cmp_eq_u32 s96,9\n"
"s_cselect_b64 s[16:17],s[20:21],s[16:17]\n"
T_"a[150:151],v152 offset:30208\n"
M_"a[60:63],a[156:159],v[204:207],a[60:63]\n"
"s_cmp_eq_u32 s96,9\n"
"s_cselect_b32 s94,0,s94\n"
C_"v183,v86,v87\n"
M_"a[60:63],a[152:155],v[140:143],a[60:63]\n"
W_"10)\n"
T_"a[152:153],v153 offset:17920\n"
M_"a[64:67],a[128:131],v[128:131],a[64:67]\n"
W_"9)\n"
"s_cmp_gt_u32 s96,9\n"
"s_cselect_b32 s18,0,s18\n"
M_"a[64:67],a[132:135],v[192:195],a[64:67]\n"
A_"s96,s96,1\n"
T_"a[154:155],v153 offset:22016\n"
M_"a[68:71],a[132:135],v[196:199],a[68:71]\n"
E_"v88,v88\n"
M_"a[68:71],a[128:131],v[132:135],a[68:71]\n"
T_"a[156:157],v153 offset:26112\n"
M_"a[72:75],a[128:131],v[136:139],a[72:75]\n"
E_"v89,v89\n"
M_"a[72:75],a[132:135],v[200:203],a[72:75]\n"
T_"a[158:159],v153 offset:30208\n"
C_"v186,v88,v89\n"
M_"a[76:79],a[132:135],v[204:207],a[76:79]\n"
E_"v90,v90\n"
M_"a[76:79],a[128:131],v[140:143],a[76:79]\n"
W_"10)\n"
B_"a[252:255],v151 offset:4096\n"
M_"a[80:83],a[136:139],v[128:131],a[80:83]\n"
W_"9)\n"
L_"v252,s[44:47],s85 offen offset:2048 lds\n"
M_"a[80:83],a[140:143],v[192:195],a[80:83]\n"
A_"m0,s60,148480\n"
B_"a[192:195],v148 offset:8192\n"
M_"a[84:87],a[140:143],v[196:199],a[84:87]\n"
A_"s85,s85,0x4000\n"
E_"v91,v91\n"
M_"a[84:87],a[136:139],v[132:135],a[84:87]\n"
B_"a[196:199],v149 offset:8192\n"
C_"v187,v90,v91\n"
M_"a[88:91],a[136:139],v[136:139],a[88:91]\n"
E_"v92,v92\n"
M_"a[88:91],a[140:143],v[200:203],a[88:91]\n"
B_"a[200:203],v150 offset:8192\n"
M_"a[92:95],a[140:143],v[204:207],a[92:95]\n"
E_"v93,v93\n"
M_"a[92:95],a[136:139],v[140:143],a[92:95]\n"
W_"10)\n"
B_"a[204:207],v151 offset:8192\n"
C_"v190,v92,v93\n"
M_"a[96:99],a[144:147],v[128:131],a[96:99]\n"
W_"9)\n"
E_"v94,v94\n"
M_"a[96:99],a[148:151],v[192:195],a[96:99]\n"
B_"a[208:211],v148 offset:12288\n"
M_"a[100:103],a[148:151],v[196:199],a[100:103]\n"
E_"v95,v95\n"
M_"a[100:103],a[144:147],v[132:135],a[100:103]\n"
B_"a[212:215],v149 offset:12288\n"
C_"v191,v94,v95\n"
M_"a[104:107],a[144:147],v[136:139],a[104:107]\n"
E_"v96,v96\n"
M_"a[104:107],a[148:151],v[200:203],a[104:107]\n"
B_"a[216:219],v150 offset:12288\n"
M_"a[108:111],a[148:151],v[204:207],a[108:111]\n"
E_"v97,v97\n"
M_"a[108:111],a[144:147],v[140:143],a[108:111]\n"
W_"10)\n"
B_"a[220:223],v151 offset:12288\n"
C_"v208,v96,v97\n"
M_"a[112:115],a[152:155],v[128:131],a[112:115]\n"
W_"9)\n"
L_"v252,s[44:47],s63 offen lds\n"
M_"a[112:115],a[156:159],v[192:195],a[112:115]\n"
E_"v98,v98\n"
M_"a[116:119],a[156:159],v[196:199],a[116:119]\n"
E_"v99,v99\n"
M_"a[116:119],a[152:155],v[132:135],a[116:119]\n"
E_"v100,v100\n"
M_"a[120:123],a[152:155],v[136:139],a[120:123]\n"
E_"v101,v101\n"
M_"a[120:123],a[156:159],v[200:203],a[120:123]\n"
E_"v102,v102\n"
M_"a[124:127],a[156:159],v[204:207],a[124:127]\n"
E_"v103,v103\n"
M_"a[124:127],a[152:155],v[140:143],a[124:127]\n"
"v_add_u32 v226,1,v226\n"
C_"v209,v98,v99\n"
M_"v[64:67],a[224:227],v[0:3],v[160:163]\n"
E_"v104,v104\n"
M_"v[64:67],a[228:231],v[16:19],v[64:67]\n"
E_"v105,v105\n"
M_"v[64:67],a[232:235],v[32:35],v[64:67]\n"
E_"v106,v106\n"
M_"v[64:67],a[236:239],v[48:51],v[64:67]\n"
E_"v107,v107\n"
M_"v[68:71],a[236:239],v[52:55],v[164:167]\n"
E_"v108,v108\n"
M_"v[68:71],a[232:235],v[36:39],v[68:71]\n"
E_"v109,v109\n"
M_"v[68:71],a[228:231],v[20:23],v[68:71]\n"
"s_waitcnt vmcnt(19) lgkmcnt(0)\n"
"ds_write_b32 v227,v226\n"
C_"v212,v100,v101\n"
M_"v[68:71],a[224:227],v[4:7],v[68:71]\n"
L_"v252,s[44:47],s63 offen offset:2048 lds\n"
M_"v[72:75],a[224:227],v[8:11],v[168:171]\n"
A_"m0,s35,1024\n"
E_"v110,v110\n"
M_"v[72:75],a[228:231],v[24:27],v[72:75]\n"
L_"v224,s[16:19],s94 offen lds\n"
M_"v[72:75],a[232:235],v[40:43],v[72:75]\n"
A_"s94,s94,32\n"
E_"v111,v111\n"
M_"v[72:75],a[236:239],v[56:59],v[72:75]\n"
"s_sub_u32 s95,s94,288\n"
E_"v112,v112\n"
M_"v[76:79],a[236:239],v[60:63],v[172:175]\n"
"s_min_u32 s94,s94,s95\n"
E_"v113,v113\n"
M_"v[76:79],a[232:235],v[44:47],v[76:79]\n"
E_"v114,v114\n"
M_"v[76:79],a[228:231],v[28:31],v[76:79]\n"
E_"v115,v115\n"
M_"v[76:79],a[224:227],v[12:15],v[76:79]\n"
E_"v116,v116\n"
M_"v[80:83],a[240:243],v[0:3],v[160:163]\n"
E_"v117,v117\n"
M_"v[80:83],a[244:247],v[16:19],v[80:83]\n"
C_"v213,v102,v103\n"
C_"v216,v104,v105\n"
M_"v[80:83],a[248:251],v[32:35],v[80:83]\n"
E_"v118,v118\n"
M_"v[80:83],a[252:255],v[48:51],v[80:83]\n"
"ds_read_b32 v241,v240 offset:0\n"
C_"v217,v106,v107\n"
M_"v[84:87],a[252:255],v[52:55],v[164:167]\n"
E_"v119,v119\n"
M_"v[84:87],a[248:251],v[36:39],v[84:87]\n"
C_"v220,v108,v109\n"
C_"v221,v110,v111\n"
M_"v[84:87],a[244:247],v[20:23],v[84:87]\n"
C_"v210,v112,v113\n"
C_"v211,v114,v115\n"
M_"v[84:87],a[240:243],v[4:7],v[84:87]\n"
E_"v120,v120\n"
M_"v[88:91],a[240:243],v[8:11],v[168:171]\n"
C_"v214,v116,v117\n"
C_"v215,v118,v119\n"
M_"v[88:91],a[244:247],v[24:27],v[88:91]\n"
E_"v121,v121\n"
M_"v[88:91],a[248:251],v[40:43],v[88:91]\n"
W_"0)\n"
"v_cmp_lt_u32 vcc,v241,v226\n"
C_"v218,v120,v121\n"
M_"v[88:91],a[252:255],v[56:59],v[88:91]\n"
E_"v122,v122\n"
M_"v[92:95],a[252:255],v[60:63],v[172:175]\n"
"s_cbranch_vccnz SPINBC2_%=\n"
"BACKBC2_%=:\n"
A_"m0,s61,65536\n"
M_"v[92:95],a[248:251],v[44:47],v[92:95]\n"
A_"s62,s84,4096\n"
T_"a[128:129],v152 offset:49152\n"
M_"v[92:95],a[244:247],v[28:31],v[92:95]\n"
"s_sub_u32 s95,s96,4\n"
E_"v123,v123\n"
M_"v[92:95],a[240:243],v[12:15],v[92:95]\n"
"s_lshl_b32 s97,s95,10\n"
T_"a[130:131],v152 offset:53248\n"
C_"v219,v122,v123\n"
M_"v[96:99],a[192:195],v[0:3],v[160:163]\n"
A_"s97,s97,s76\n"
L_"v251,s[40:43],s84 offen lds\n"
M_"v[96:99],a[196:199],v[16:19],v[96:99]\n"
A_"m0,s61,69632\n"
T_"a[132:133],v152 offset:57344\n"
M_"v[96:99],a[200:203],v[32:35],v[96:99]\n"
A_"s63,s85,128\n"
E_"v124,v124\n"
M_"v[96:99],a[204:207],v[48:51],v[96:99]\n"
T_"a[134:135],v152 offset:61440\n"
M_"v[100:103],a[204:207],v[52:55],v[164:167]\n"
E_"v125,v125\n"
M_"v[100:103],a[200:203],v[36:39],v[100:103]\n"
T_"a[136:137],v153 offset:49152\n"
C_"v222,v124,v125\n"
M_"v[100:103],a[196:199],v[20:23],v[100:103]\n"
E_"v126,v126\n"
M_"v[100:103],a[192:195],v[4:7],v[100:103]\n"
T_"a[138:139],v153 offset:53248\n"
M_"v[104:107],a[192:195],v[8:11],v[168:171]\n"
E_"v127,v127\n"
M_"v[104:107],a[196:199],v[24:27],v[104:107]\n"
T_"a[140:141],v153 offset:57344\n"
C_"v223,v126,v127\n"
M_"v[104:107],a[200:203],v[40:43],v[104:107]\n"
E_"v64,v64\n"
M_"v[104:107],a[204:207],v[56:59],v[104:107]\n"
T_"a[142:143],v153 offset:61440\n"
M_"v[108:111],a[204:207],v[60:63],v[172:175]\n"
E_"v65,v65\n"
M_"v[108:111],a[200:203],v[44:47],v[108:111]\n"
T_"a[144:145],v152 offset:49664\n"
C_"v128,v64,v65\n"
M_"v[108:111],a[196:199],v[28:31],v[108:111]\n"
E_"v66,v66\n"
M_"v[108:111],a[192:195],v[12:15],v[108:111]\n"
T_"a[146:147],v152 offset:53760\n"
M_"v[112:115],a[208:211],v[0:3],v[160:163]\n"
L_"v251,s[40:43],s62 offen lds\n"
M_"v[112:115],a[212:215],v[16:19],v[112:115]\n"
A_"m0,s61,73728\n"
T_"a[148:149],v152 offset:57856\n"
M_"v[112:115],a[216:219],v[32:35],v[112:115]\n"
A_"s62,s84,8192\n"
E_"v67,v67\n"
M_"v[112:115],a[220:223],v[48:51],v[112:115]\n"
T_"a[150:151],v152 offset:61952\n"
C_"v129,v66,v67\n"
M_"v[116:119],a[220:223],v[52:55],v[164:167]\n"
E_"v68,v68\n"
M_"v[116:119],a[216:219],v[36:39],v[116:119]\n"
T_"a[152:153],v153 offset:49664\n"
M_"v[116:119],a[212:215],v[20:23],v[116:119]\n"
E_"v69,v69\n"
M_"v[116:119],a[208:211],v[4:7],v[116:119]\n"
T_"a[154:155],v153 offset:53760\n"
C_"v132,v68,v69\n"
M_"v[120:123],a[208:211],v[8:11],v[168:171]\n"
E_"v70,v70\n"
M_"v[120:123],a[212:215],v[24:27],v[120:123]\n"
T_"a[156:157],v153 offset:57856\n"
M_"v[120:123],a[216:219],v[40:43],v[120:123]\n"
E_"v71,v71\n"
M_"v[120:123],a[220:223],v[56:59],v[120:123]\n"
T_"a[158:159],v153 offset:61952\n"
C_"v133,v70,v71\n"
M_"v[124:127],a[220:223],v[60:63],v[172:175]\n"
E_"v72,v72\n"
M_"v[124:127],a[216:219],v[44:47],v[124:127]\n"
B_"a[224:227],v148 offset:32768\n"
M_"v[124:127],a[212:215],v[28:31],v[124:127]\n"
E_"v73,v73\n"
M_"v[124:127],a[208:211],v[12:15],v[124:127]\n"
B_"a[228:231],v149 offset:32768\n"
C_"v136,v72,v73\n"
M_"a[160:163],a[176:179],v[176:179],a[160:163]\n"
L_"v251,s[40:43],s62 offen lds\n"
M_"a[160:163],a[176:179],v[208:211],a[160:163]\n"
A_"m0,s61,77824\n"
B_"a[232:235],v150 offset:32768\n"
M_"a[164:167],a[176:179],v[180:183],a[164:167]\n"
A_"s62,s84,12288\n"
E_"v74,v74\n"
M_"a[164:167],a[176:179],v[212:215],a[164:167]\n"
A_"s84,s84,0x4000\n"
B_"a[236:239],v151 offset:32768\n"
M_"a[168:171],a[176:179],v[184:187],a[168:171]\n"
E_"v75,v75\n"
M_"a[168:171],a[176:179],v[216:219],a[168:171]\n"
T_"v[228:229],v225 offset:2048\n"
C_"v137,v74,v75\n"
M_"a[172:175],a[176:179],v[188:191],a[172:175]\n"
E_"v76,v76\n"
M_"a[172:175],a[176:179],v[220:223],a[172:175]\n"
W_"15)\n"
T_"v[230:231],v225 offset:2560\n"
M_"a[0:3],a[128:131],v[176:179],a[0:3]\n"
E_"v77,v77\n"
M_"a[0:3],a[132:135],v[208:211],a[0:3]\n"
B_"a[240:243],v148 offset:36864\n"
C_"v140,v76,v77\n"
M_"a[4:7],a[132:135],v[212:215],a[4:7]\n"
E_"v78,v78\n"
M_"a[4:7],a[128:131],v[180:183],a[4:7]\n"
B_"a[244:247],v149 offset:36864\n"
M_"a[8:11],a[128:131],v[184:187],a[8:11]\n"
E_"v79,v79\n"
M_"a[8:11],a[132:135],v[216:219],a[8:11]\n"
B_"a[248:251],v150 offset:36864\n"
C_"v141,v78,v79\n"
M_"a[12:15],a[132:135],v[220:223],a[12:15]\n"
E_"v80,v80\n"
M_"a[12:15],a[128:131],v[188:191],a[12:15]\n"
T_"a[128:129],v152 offset:50176\n"
M_"a[16:19],a[136:139],v[176:179],a[16:19]\n"
W_"15)\n"
L_"v251,s[40:43],s62 offen lds\n"
M_"a[16:19],a[140:143],v[208:211],a[16:19]\n"
A_"m0,s60,16384\n"
T_"a[130:131],v152 offset:54272\n"
M_"a[20:23],a[140:143],v[212:215],a[20:23]\n"
E_"v81,v81\n"
M_"a[20:23],a[136:139],v[180:183],a[20:23]\n"
T_"a[132:133],v152 offset:58368\n"
C_"v130,v80,v81\n"
M_"a[24:27],a[136:139],v[184:187],a[24:27]\n"
"s_cmp_eq_u32 s95,9\n"
"s_cselect_b32 s97,s77,s97\n"
M_"a[24:27],a[140:143],v[216:219],a[24:27]\n"
"s_cmp_gt_u32 s95,9\n"
"s_cselect_b32 s97,1073741824,s97\n"
T_"a[134:135],v152 offset:62464\n"
M_"a[28:31],a[140:143],v[220:223],a[28:31]\n"
E_"v82,v82\n"
M_"a[28:31],a[136:139],v[188:191],a[28:31]\n"
T_"a[136:137],v153 offset:50176\n"
M_"a[32:35],a[144:147],v[176:179],a[32:35]\n"
W_"15)\n"
E_"v83,v83\n"
M_"a[32:35],a[148:151],v[208:211],a[32:35]\n"
T_"a[138:139],v153 offset:54272\n"
C_"v131,v82,v83\n"
M_"a[36:39],a[148:151],v[212:215],a[36:39]\n"
W_"9)\n"
O_"v[228:231],v255,s[24:27],s97 offen sc1\n"
M_"a[36:39],a[144:147],v[180:183],a[36:39]\n"
T_"a[140:141],v153 offset:58368\n"
M_"a[40:43],a[144:147],v[184:187],a[40:43]\n"
E_"v84,v84\n"
M_"a[40:43],a[148:151],v[216:219],a[40:43]\n"
T_"a[142:143],v153 offset:62464\n"
M_"a[44:47],a[148:151],v[220:223],a[44:47]\n"
E_"v85,v85\n"
M_"a[44:47],a[144:147],v[188:191],a[44:47]\n"
T_"a[144:145],v152 offset:50688\n"
C_"v134,v84,v85\n"
M_"a[48:51],a[152:155],v[176:179],a[48:51]\n"
L_"v252,s[44:47],s85 offen lds\n"
M_"a[48:51],a[156:159],v[208:211],a[48:51]\n"
T_"a[146:147],v152 offset:54784\n"
M_"a[52:55],a[156:159],v[212:215],a[52:55]\n"
E_"v86,v86\n"
M_"a[52:55],a[152:155],v[180:183],a[52:55]\n"
T_"a[148:149],v152 offset:58880\n"
M_"a[56:59],a[152:155],v[184:187],a[56:59]\n"
"s_cmp_eq_u32 s96,9\n"
"s_cselect_b32 s18,s22,s18\n"
M_"a[56:59],a[156:159],v[216:219],a[56:59]\n"
"s_cmp_eq_u32 s96,9\n"
"s_cselect_b64 s[16:17],s[20:21],s[16:17]\n"
T_"a[150:151],v152 offset:62976\n"
M_"a[60:63],a[156:159],v[220:223],a[60:63]\n"
"s_cmp_eq_u32 s96,9\n"
"s_cselect_b32 s94,0,s94\n"
M_"a[60:63],a[152:155],v[188:191],a[60:63]\n"
W_"10)\n"
T_"a[152:153],v153 offset:50688\n"
M_"a[64:67],a[128:131],v[176:179],a[64:67]\n"
W_"9)\n"
"s_cmp_gt_u32 s96,9\n"
"s_cselect_b32 s18,0,s18\n"
M_"a[64:67],a[132:135],v[208:211],a[64:67]\n"
A_"s96,s96,1\n"
T_"a[154:155],v153 offset:54784\n"
M_"a[68:71],a[132:135],v[212:215],a[68:71]\n"
E_"v87,v87\n"
M_"a[68:71],a[128:131],v[180:183],a[68:71]\n"
T_"a[156:157],v153 offset:58880\n"
C_"v135,v86,v87\n"
M_"a[72:75],a[128:131],v[184:187],a[72:75]\n"
E_"v88,v88\n"
M_"a[72:75],a[132:135],v[216:219],a[72:75]\n"
T_"a[158:159],v153 offset:62976\n"
M_"a[76:79],a[132:135],v[220:223],a[76:79]\n"
E_"v89,v89\n"
M_"a[76:79],a[128:131],v[188:191],a[76:79]\n"
W_"10)\n"
B_"a[252:255],v151 offset:36864\n"
C_"v138,v88,v89\n"
M_"a[80:83],a[136:139],v[176:179],a[80:83]\n"
W_"9)\n"
L_"v252,s[44:47],s85 offen offset:2048 lds\n"
M_"a[80:83],a[140:143],v[208:211],a[80:83]\n"
A_"m0,s60,17408\n"
B_"a[192:195],v148 offset:40960\n"
M_"a[84:87],a[140:143],v[212:215],a[84:87]\n"
A_"s85,s85,0x4000\n"
E_"v90,v90\n"
M_"a[84:87],a[136:139],v[180:183],a[84:87]\n"
B_"a[196:199],v149 offset:40960\n"
M_"a[88:91],a[136:139],v[184:187],a[88:91]\n"
E_"v91,v91\n"
M_"a[88:91],a[140:143],v[216:219],a[88:91]\n"
B_"a[200:203],v150 offset:40960\n"
C_"v139,v90,v91\n"
M_"a[92:95],a[140:143],v[220:223],a[92:95]\n"
E_"v92,v92\n"
M_"a[92:95],a[136:139],v[188:191],a[92:95]\n"
W_"10)\n"
B_"a[204:207],v151 offset:40960\n"
M_"a[96:99],a[144:147],v[176:179],a[96:99]\n"
W_"9)\n"
E_"v93,v93\n"
M_"a[96:99],a[148:151],v[208:211],a[96:99]\n"
B_"a[208:211],v148 offset:45056\n"
C_"v142,v92,v93\n"
M_"a[100:103],a[148:151],v[212:215],a[100:103]\n"
E_"v94,v94\n"
M_"a[100:103],a[144:147],v[180:183],a[100:103]\n"
B_"a[212:215],v149 offset:45056\n"
M_"a[104:107],a[144:147],v[184:187],a[104:107]\n"
E_"v95,v95\n"
M_"a[104:107],a[148:151],v[216:219],a[104:107]\n"
B_"a[216:219],v150 offset:45056\n"
C_"v143,v94,v95\n"
M_"a[108:111],a[148:151],v[220:223],a[108:111]\n"
E_"v96,v96\n"
M_"a[108:111],a[144:147],v[188:191],a[108:111]\n"
W_"10)\n"
B_"a[220:223],v151 offset:45056\n"
M_"a[112:115],a[152:155],v[176:179],a[112:115]\n"
W_"9)\n"
L_"v252,s[44:47],s63 offen lds\n"
M_"a[112:115],a[156:159],v[208:211],a[112:115]\n"
E_"v97,v97\n"
M_"a[116:119],a[156:159],v[212:215],a[116:119]\n"
E_"v98,v98\n"
M_"a[116:119],a[152:155],v[180:183],a[116:119]\n"
E_"v99,v99\n"
M_"a[120:123],a[152:155],v[184:187],a[120:123]\n"
E_"v100,v100\n"
M_"a[120:123],a[156:159],v[216:219],a[120:123]\n"
E_"v101,v101\n"
M_"a[124:127],a[156:159],v[220:223],a[124:127]\n"
E_"v102,v102\n"
M_"a[124:127],a[152:155],v[188:191],a[124:127]\n"
"v_add_u32 v226,1,v226\n"
C_"v192,v96,v97\n"
M_"v[64:67],a[224:227],v[0:3],v[160:163]\n"
E_"v103,v103\n"
M_"v[64:67],a[228:231],v[16:19],v[64:67]\n"
E_"v104,v104\n"
M_"v[64:67],a[232:235],v[32:35],v[64:67]\n"
E_"v105,v105\n"
M_"v[64:67],a[236:239],v[48:51],v[64:67]\n"
E_"v106,v106\n"
M_"v[68:71],a[236:239],v[52:55],v[164:167]\n"
E_"v107,v107\n"
M_"v[68:71],a[232:235],v[36:39],v[68:71]\n"
E_"v108,v108\n"
M_"v[68:71],a[228:231],v[20:23],v[68:71]\n"
"s_waitcnt vmcnt(19) lgkmcnt(0)\n"
"ds_write_b32 v227,v226\n"
C_"v193,v98,v99\n"
M_"v[68:71],a[224:227],v[4:7],v[68:71]\n"
L_"v252,s[44:47],s63 offen offset:2048 lds\n"
M_"v[72:75],a[224:227],v[8:11],v[168:171]\n"
A_"m0,s35,2048\n"
E_"v109,v109\n"
M_"v[72:75],a[228:231],v[24:27],v[72:75]\n"
L_"v224,s[16:19],s94 offen lds\n"
M_"v[72:75],a[232:235],v[40:43],v[72:75]\n"
A_"s94,s94,32\n"
E_"v110,v110\n"
M_"v[72:75],a[236:239],v[56:59],v[72:75]\n"
"s_sub_u32 s95,s94,288\n"
E_"v111,v111\n"
M_"v[76:79],a[236:239],v[60:63],v[172:175]\n"
"s_min_u32 s94,s94,s95\n"
E_"v112,v112\n"
M_"v[76:79],a[232:235],v[44:47],v[76:79]\n"
E_"v113,v113\n"
M_"v[76:79],a[228:231],v[28:31],v[76:79]\n"
E_"v114,v114\n"
M_"v[76:79],a[224:227],v[12:15],v[76:79]\n"
E_"v115,v115\n"
M_"v[80:83],a[240:243],v[0:3],v[160:163]\n"
E_"v116,v116\n"
M_"v[80:83],a[244:247],v[16:19],v[80:83]\n"
E_"v117,v117\n"
M_"v[80:83],a[248:251],v[32:35],v[80:83]\n"
E_"v118,v118\n"
M_"v[80:83],a[252:255],v[48:51],v[80:83]\n"
"ds_read_b32 v241,v240 offset:0\n"
C_"v196,v100,v101\n"
M_"v[84:87],a[252:255],v[52:55],v[164:167]\n"
E_"v119,v119\n"
M_"v[84:87],a[248:251],v[36:39],v[84:87]\n"
C_"v197,v102,v103\n"
C_"v200,v104,v105\n"
M_"v[84:87],a[244:247],v[20:23],v[84:87]\n"
C_"v201,v106,v107\n"
C_"v204,v108,v109\n"
M_"v[84:87],a[240:243],v[4:7],v[84:87]\n"
E_"v120,v120\n"
M_"v[88:91],a[240:243],v[8:11],v[168:171]\n"
C_"v205,v110,v111\n"
C_"v194,v112,v113\n"
M_"v[88:91],a[244:247],v[24:27],v[88:91]\n"
E_"v121,v121\n"
M_"v[88:91],a[248:251],v[40:43],v[88:91]\n"
W_"0)\n"
"v_cmp_lt_u32 vcc,v241,v226\n"
C_"v195,v114,v115\n"
M_"v[88:91],a[252:255],v[56:59],v[88:91]\n"
E_"v122,v122\n"
M_"v[92:95],a[252:255],v[60:63],v[172:175]\n"
"s_cbranch_vccnz SPINBC3_%=\n"
"BACKBC3_%=:\n"
A_"m0,s61,98304\n"
M_"v[92:95],a[248:251],v[44:47],v[92:95]\n"
A_"s62,s84,4096\n"
T_"a[128:129],v154 offset:16384\n"
C_"v198,v116,v117\n"
M_"v[92:95],a[244:247],v[28:31],v[92:95]\n"
"s_sub_u32 s95,s96,4\n"
E_"v123,v123\n"
M_"v[92:95],a[240:243],v[12:15],v[92:95]\n"
"s_lshl_b32 s97,s95,10\n"
T_"a[130:131],v154 offset:20480\n"
C_"v199,v118,v119\n"
M_"v[96:99],a[192:195],v[0:3],v[160:163]\n"
A_"s97,s97,s76\n"
L_"v251,s[40:43],s84 offen lds\n"
M_"v[96:99],a[196:199],v[16:19],v[96:99]\n"
A_"m0,s61,102400\n"
T_"a[132:133],v154 offset:24576\n"
C_"v202,v120,v121\n"
M_"v[96:99],a[200:203],v[32:35],v[96:99]\n"
A_"s63,s85,128\n"
E_"v124,v124\n"
M_"v[96:99],a[204:207],v[48:51],v[96:99]\n"
T_"a[134:135],v154 offset:28672\n"
C_"v203,v122,v123\n"
M_"v[100:103],a[204:207],v[52:55],v[164:167]\n"
E_"v125,v125\n"
M_"v[100:103],a[200:203],v[36:39],v[100:103]\n"
T_"a[136:137],v155 offset:16384\n"
C_"v206,v124,v125\n"
M_"v[100:103],a[196:199],v[20:23],v[100:103]\n"
E_"v126,v126\n"
M_"v[100:103],a[192:195],v[4:7],v[100:103]\n"
T_"a[138:139],v155 offset:20480\n"
M_"v[104:107],a[192:195],v[8:11],v[168:171]\n"
E_"v127,v127\n"
M_"v[104:107],a[196:199],v[24:27],v[104:107]\n"
T_"a[140:141],v155 offset:24576\n"
C_"v207,v126,v127\n"
M_"v[104:107],a[200:203],v[40:43],v[104:107]\n"
E_"v64,v64\n"
M_"v[104:107],a[204:207],v[56:59],v[104:107]\n"
T_"a[142:143],v155 offset:28672\n"
M_"v[108:111],a[204:207],v[60:63],v[172:175]\n"
E_"v65,v65\n"
M_"v[108:111],a[200:203],v[44:47],v[108:111]\n"
T_"a[144:145],v154 offset:16896\n"
C_"v176,v64,v65\n"
M_"v[108:111],a[196:199],v[28:31],v[108:111]\n"
E_"v66,v66\n"
M_"v[108:111],a[192:195],v[12:15],v[108:111]\n"
T_"a[146:147],v154 offset:20992\n"
M_"v[112:115],a[208:211],v[0:3],v[160:163]\n"
L_"v251,s[40:43],s62 offen lds\n"
M_"v[112:115],a[212:215],v[16:19],v[112:115]\n"
A_"m0,s61,106496\n"
T_"a[148:149],v154 offset:25088\n"
M_"v[112:115],a[216:219],v[32:35],v[112:115]\n"
A_"s62,s84,8192\n"
E_"v67,v67\n"
M_"v[112:115],a[220:223],v[48:51],v[112:115]\n"
T_"a[150:151],v154 offset:29184\n"
C_"v177,v66,v67\n"
M_"v[116:119],a[220:223],v[52:55],v[164:167]\n"
E_"v68,v68\n"
M_"v[116:119],a[216:219],v[36:39],v[116:119]\n"
T_"a[152:153],v155 offset:16896\n"
M_"v[116:119],a[212:215],v[20:23],v[116:119]\n"
E_"v69,v69\n"
M_"v[116:119],a[208:211],v[4:7],v[116:119]\n"
T_"a[154:155],v155 offset:20992\n"
C_"v180,v68,v69\n"
M_"v[120:123],a[208:211],v[8:11],v[168:171]\n"
E_"v70,v70\n"
M_"v[120:123],a[212:215],v[24:27],v[120:123]\n"
T_"a[156:157],v155 offset:25088\n"
M_"v[120:123],a[216:219],v[40:43],v[120:123]\n"
E_"v71,v71\n"
M_"v[120:123],a[220:223],v[56:59],v[120:123]\n"
T_"a[158:159],v155 offset:29184\n"
C_"v181,v70,v71\n"
M_"v[124:127],a[220:223],v[60:63],v[172:175]\n"
E_"v72,v72\n"
M_"v[124:127],a[216:219],v[44:47],v[124:127]\n"
B_"a[224:227],v144 offset:0\n"
M_"v[124:127],a[212:215],v[28:31],v[124:127]\n"
E_"v73,v73\n"
M_"v[124:127],a[208:211],v[12:15],v[124:127]\n"
B_"a[228:231],v145 offset:0\n"
C_"v184,v72,v73\n"
M_"a[160:163],a[176:179],v[128:131],a[160:163]\n"
L_"v251,s[40:43],s62 offen lds\n"
M_"a[160:163],a[176:179],v[192:195],a[160:163]\n"
A_"m0,s61,110592\n"
B_"a[232:235],v146 offset:0\n"
M_"a[164:167],a[176:179],v[132:135],a[164:167]\n"
A_"s62,s84,12288\n"
E_"v74,v74\n"
M_"a[164:167],a[176:179],v[196:199],a[164:167]\n"
A_"s84,s84,0x4000\n"
B_"a[236:239],v147 offset:0\n"
M_"a[168:171],a[176:179],v[136:139],a[168:171]\n"
E_"v75,v75\n"
M_"a[168:171],a[176:179],v[200:203],a[168:171]\n"
T_"v[232:233],v225 offset:3072\n"
C_"v185,v74,v75\n"
M_"a[172:175],a[176:179],v[140:143],a[172:175]\n"
E_"v76,v76\n"
M_"a[172:175],a[176:179],v[204:207],a[172:175]\n"
W_"15)\n"
T_"v[234:235],v225 offset:3584\n"
M_"a[0:3],a[128:131],v[128:131],a[0:3]\n"
E_"v77,v77\n"
M_"a[0:3],a[132:135],v[192:195],a[0:3]\n"
B_"a[240:243],v144 offset:4096\n"
C_"v188,v76,v77\n"
M_"a[4:7],a[132:135],v[196:199],a[4:7]\n"
E_"v78,v78\n"
M_"a[4:7],a[128:131],v[132:135],a[4:7]\n"
B_"a[244:247],v145 offset:4096\n"
M_"a[8:11],a[128:131],v[136:139],a[8:11]\n"
E_"v79,v79\n"
M_"a[8:11],a[132:135],v[200:203],a[8:11]\n"
B_"a[248:251],v146 offset:4096\n"
C_"v189,v78,v79\n"
M_"a[12:15],a[132:135],v[204:207],a[12:15]\n"
E_"v80,v80\n"
M_"a[12:15],a[128:131],v[140:143],a[12:15]\n"
T_"a[128:129],v154 offset:17408\n"
M_"a[16:19],a[136:139],v[128:131],a[16:19]\n"
W_"15)\n"
L_"v251,s[40:43],s62 offen lds\n"
M_"a[16:19],a[140:143],v[192:195],a[16:19]\n"
A_"m0,s60,49152\n"
T_"a[130:131],v154 offset:21504\n"
M_"a[20:23],a[140:143],v[196:199],a[20:23]\n"
E_"v81,v81\n"
M_"a[20:23],a[136:139],v[132:135],a[20:23]\n"
T_"a[132:133],v154 offset:25600\n"
C_"v178,v80,v81\n"
M_"a[24:27],a[136:139],v[136:139],a[24:27]\n"
"s_cmp_eq_u32 s95,9\n"
"s_cselect_b32 s97,s77,s97\n"
M_"a[24:27],a[140:143],v[200:203],a[24:27]\n"
"s_cmp_gt_u32 s95,9\n"
"s_cselect_b32 s97,1073741824,s97\n"
T_"a[134:135],v154 offset:29696\n"
M_"a[28:31],a[140:143],v[204:207],a[28:31]\n"
E_"v82,v82\n"
M_"a[28:31],a[136:139],v[140:143],a[28:31]\n"
T_"a[136:137],v155 offset:17408\n"
M_"a[32:35],a[144:147],v[128:131],a[32:35]\n"
W_"15)\n"
E_"v83,v83\n"
M_"a[32:35],a[148:151],v[192:195],a[32:35]\n"
T_"a[138:139],v155 offset:21504\n"
C_"v179,v82,v83\n"
M_"a[36:39],a[148:151],v[196:199],a[36:39]\n"
W_"9)\n"
O_"v[232:235],v255,s[24:27],s97 offen sc1\n"
M_"a[36:39],a[144:147],v[132:135],a[36:39]\n"
T_"a[140:141],v155 offset:25600\n"
M_"a[40:43],a[144:147],v[136:139],a[40:43]\n"
E_"v84,v84\n"
M_"a[40:43],a[148:151],v[200:203],a[40:43]\n"
T_"a[142:143],v155 offset:29696\n"
M_"a[44:47],a[148:151],v[204:207],a[44:47]\n"
E_"v85,v85\n"
M_"a[44:47],a[144:147],v[140:143],a[44:47]\n"
T_"a[144:145],v154 offset:17920\n"
C_"v182,v84,v85\n"
M_"a[48:51],a[152:155],v[128:131],a[48:51]\n"
L_"v252,s[44:47],s85 offen lds\n"
M_"a[48:51],a[156:159],v[192:195],a[48:51]\n"
T_"a[146:147],v154 offset:22016\n"
M_"a[52:55],a[156:159],v[196:199],a[52:55]\n"
E_"v86,v86\n"
M_"a[52:55],a[152:155],v[132:135],a[52:55]\n"
T_"a[148:149],v154 offset:26112\n"
M_"a[56:59],a[152:155],v[136:139],a[56:59]\n"
"s_cmp_eq_u32 s96,9\n"
"s_cselect_b32 s18,s22,s18\n"
M_"a[56:59],a[156:159],v[200:203],a[56:59]\n"
"s_cmp_eq_u32 s96,9\n"
"s_cselect_b64 s[16:17],s[20:21],s[16:17]\n"
T_"a[150:151],v154 offset:30208\n"
M_"a[60:63],a[156:159],v[204:207],a[60:63]\n"
"s_cmp_eq_u32 s96,9\n"
"s_cselect_b32 s94,0,s94\n"
M_"a[60:63],a[152:155],v[140:143],a[60:63]\n"
W_"10)\n"
T_"a[152:153],v155 offset:17920\n"
M_"a[64:67],a[128:131],v[128:131],a[64:67]\n"
W_"9)\n"
"s_cmp_gt_u32 s96,9\n"
"s_cselect_b32 s18,0,s18\n"
M_"a[64:67],a[132:135],v[192:195],a[64:67]\n"
A_"s96,s96,1\n"
T_"a[154:155],v155 offset:22016\n"
M_"a[68:71],a[132:135],v[196:199],a[68:71]\n"
E_"v87,v87\n"
M_"a[68:71],a[128:131],v[132:135],a[68:71]\n"
T_"a[156:157],v155 offset:26112\n"
C_"v183,v86,v87\n"
M_"a[72:75],a[128:131],v[136:139],a[72:75]\n"
E_"v88,v88\n"
M_"a[72:75],a[132:135],v[200:203],a[72:75]\n"
T_"a[158:159],v155 offset:30208\n"
M_"a[76:79],a[132:135],v[204:207],a[76:79]\n"
E_"v89,v89\n"
M_"a[76:79],a[128:131],v[140:143],a[76:79]\n"
W_"10)\n"
B_"a[252:255],v147 offset:4096\n"
C_"v186,v88,v89\n"
M_"a[80:83],a[136:139],v[128:131],a[80:83]\n"
W_"9)\n"
L_"v252,s[44:47],s85 offen offset:2048 lds\n"
M_"a[80:83],a[140:143],v[192:195],a[80:83]\n"
A_"m0,s60,50176\n"
B_"a[192:195],v144 offset:8192\n"
M_"a[84:87],a[140:143],v[196:199],a[84:87]\n"
A_"s85,s85,0x4000\n"
E_"v90,v90\n"
M_"a[84:87],a[136:139],v[132:135],a[84:87]\n"
B_"a[196:199],v145 offset:8192\n"
M_"a[88:91],a[136:139],v[136:139],a[88:91]\n"
E_"v91,v91\n"
M_"a[88:91],a[140:143],v[200:203],a[88:91]\n"
B_"a[200:203],v146 offset:8192\n"
C_"v187,v90,v91\n"
M_"a[92:95],a[140:143],v[204:207],a[92:95]\n"
E_"v92,v92\n"
M_"a[92:95],a[136:139],v[140:143],a[92:95]\n"
W_"10)\n"
B_"a[204:207],v147 offset:8192\n"
M_"a[96:99],a[144:147],v[128:131],a[96:99]\n"
W_"9)\n"
E_"v93,v93\n"
M_"a[96:99],a[148:151],v[192:195],a[96:99]\n"
B_"a[208:211],v144 offset:12288\n"
C_"v190,v92,v93\n"
M_"a[100:103],a[148:151],v[196:199],a[100:103]\n"
E_"v94,v94\n"
M_"a[100:103],a[144:147],v[132:135],a[100:103]\n"
B_"a[212:215],v145 offset:12288\n"
M_"a[104:107],a[144:147],v[136:139],a[104:107]\n"
E_"v95,v95\n"
M_"a[104:107],a[148:151],v[200:203],a[104:107]\n"
B_"a[216:219],v146 offset:12288\n"
C_"v191,v94,v95\n"
M_"a[108:111],a[148:151],v[204:207],a[108:111]\n"
E_"v96,v96\n"
M_"a[108:111],a[144:147],v[140:143],a[108:111]\n"
W_"10)\n"
B_"a[220:223],v147 offset:12288\n"
M_"a[112:115],a[152:155],v[128:131],a[112:115]\n"
W_"9)\n"
L_"v252,s[44:47],s63 offen lds\n"
M_"a[112:115],a[156:159],v[192:195],a[112:115]\n"
E_"v97,v97\n"
M_"a[116:119],a[156:159],v[196:199],a[116:119]\n"
E_"v98,v98\n"
M_"a[116:119],a[152:155],v[132:135],a[116:119]\n"
E_"v99,v99\n"
M_"a[120:123],a[152:155],v[136:139],a[120:123]\n"
E_"v100,v100\n"
M_"a[120:123],a[156:159],v[200:203],a[120:123]\n"
E_"v101,v101\n"
M_"a[124:127],a[156:159],v[204:207],a[124:127]\n"
E_"v102,v102\n"
M_"a[124:127],a[152:155],v[140:143],a[124:127]\n"
"s_sub_u32 s86,s86,1\n"
"s_nop 0\n"
S_"s86,4\n"
"LOOP_%=:\n"
M_"v[64:67],a[224:227],v[0:3],v[160:163]\n"
"v_add_u32 v226,1,v226\n"
C_"v208,v96,v97\n"
M_"v[64:67],a[228:231],v[16:19],v[64:67]\n"
E_"v103,v103\n"
M_"v[64:67],a[232:235],v[32:35],v[64:67]\n"
E_"v104,v104\n"
M_"v[64:67],a[236:239],v[48:51],v[64:67]\n"
E_"v105,v105\n"
M_"v[68:71],a[236:239],v[52:55],v[164:167]\n"
E_"v106,v106\n"
M_"v[68:71],a[232:235],v[36:39],v[68:71]\n"
E_"v107,v107\n"
M_"v[68:71],a[228:231],v[20:23],v[68:71]\n"
"s_waitcnt vmcnt(19) lgkmcnt(0)\n"
"ds_write_b32 v227,v226\n"
C_"v209,v98,v99\n"
M_"v[68:71],a[224:227],v[4:7],v[68:71]\n"
L_"v252,s[44:47],s63 offen offset:2048 lds\n"
M_"v[72:75],a[224:227],v[8:11],v[168:171]\n"
A_"m0,s35,3072\n"
E_"v108,v108\n"
M_"v[72:75],a[228:231],v[24:27],v[72:75]\n"
L_"v224,s[16:19],s94 offen lds\n"
M_"v[72:75],a[232:235],v[40:43],v[72:75]\n"
A_"s94,s94,32\n"
E_"v109,v109\n"
M_"v[72:75],a[236:239],v[56:59],v[72:75]\n"
"s_sub_u32 s95,s94,288\n"
E_"v110,v110\n"
M_"v[76:79],a[236:239],v[60:63],v[172:175]\n"
"s_min_u32 s94,s94,s95\n"
E_"v111,v111\n"
M_"v[76:79],a[232:235],v[44:47],v[76:79]\n"
E_"v112,v112\n"
M_"v[76:79],a[228:231],v[28:31],v[76:79]\n"
E_"v113,v113\n"
M_"v[76:79],a[224:227],v[12:15],v[76:79]\n"
E_"v114,v114\n"
M_"v[80:83],a[240:243],v[0:3],v[160:163]\n"
E_"v115,v115\n"
M_"v[80:83],a[244:247],v[16:19],v[80:83]\n"
E_"v116,v116\n"
M_"v[80:83],a[248:251],v[32:35],v[80:83]\n"
E_"v117,v117\n"
M_"v[80:83],a[252:255],v[48:51],v[80:83]\n"
"ds_read_b32 v241,v240 offset:0\n"
C_"v212,v100,v101\n"
M_"v[84:87],a[252:255],v[52:55],v[164:167]\n"
E_"v118,v118\n"
M_"v[84:87],a[248:251],v[36:39],v[84:87]\n"
E_"v119,v119\n"
M_"v[84:87],a[244:247],v[20:23],v[84:87]\n"
C_"v213,v102,v103\n"
C_"v216,v104,v105\n"
M_"v[84:87],a[240:243],v[4:7],v[84:87]\n"
E_"v120,v120\n"
M_"v[88:91],a[240:243],v[8:11],v[168:171]\n"
C_"v217,v106,v107\n"
C_"v220,v108,v109\n"
M_"v[88:91],a[244:247],v[24:27],v[88:91]\n"
E_"v121,v121\n"
M_"v[88:91],a[248:251],v[40:43],v[88:91]\n"
W_"0)\n"
"v_cmp_lt_u32 vcc,v241,v226\n"
C_"v221,v110,v111\n"
M_"v[88:91],a[252:255],v[56:59],v[88:91]\n"
E_"v122,v122\n"
M_"v[92:95],a[252:255],v[60:63],v[172:175]\n"
"s_cbranch_vccnz SPINBD0_%=\n"
"BACKBD0_%=:\n"
A_"m0,s61,0\n"
M_"v[92:95],a[248:251],v[44:47],v[92:95]\n"
A_"s62,s84,4096\n"
T_"a[128:129],v236 offset:0\n"
C_"v210,v112,v113\n"
M_"v[92:95],a[244:247],v[28:31],v[92:95]\n"
A_"s88,s74,8192\n"
E_"v123,v123\n"
M_"v[92:95],a[240:243],v[12:15],v[92:95]\n"
"s_sub_u32 s95,s96,4\n"
T_"a[130:131],v236 offset:4096\n"
C_"v211,v114,v115\n"
M_"v[96:99],a[192:195],v[0:3],v[160:163]\n"
"s_lshl_b32 s97,s95,10\n"
L_"v251,s[40:43],s84 offen lds\n"
M_"v[96:99],a[196:199],v[16:19],v[96:99]\n"
A_"m0,s61,4096\n"
T_"a[132:133],v236 offset:8192\n"
C_"v214,v116,v117\n"
M_"v[96:99],a[200:203],v[32:35],v[96:99]\n"
A_"s97,s97,s76\n"
E_"v124,v124\n"
M_"v[96:99],a[204:207],v[48:51],v[96:99]\n"
A_"s63,s85,128\n"
T_"a[134:135],v236 offset:12288\n"
C_"v215,v118,v119\n"
M_"v[100:103],a[204:207],v[52:55],v[164:167]\n"
E_"v125,v125\n"
M_"v[100:103],a[200:203],v[36:39],v[100:103]\n"
T_"a[136:137],v237 offset:0\n"
C_"v218,v120,v121\n"
M_"v[100:103],a[196:199],v[20:23],v[100:103]\n"
E_"v126,v126\n"
M_"v[100:103],a[192:195],v[4:7],v[100:103]\n"
T_"a[138:139],v237 offset:4096\n"
C_"v219,v122,v123\n"
M_"v[104:107],a[192:195],v[8:11],v[168:171]\n"
E_"v127,v127\n"
M_"v[104:107],a[196:199],v[24:27],v[104:107]\n"
T_"a[140:141],v237 offset:8192\n"
C_"v222,v124,v125\n"
M_"v[104:107],a[200:203],v[40:43],v[104:107]\n"
E_"v64,v64\n"
M_"v[104:107],a[204:207],v[56:59],v[104:107]\n"
T_"a[142:143],v237 offset:12288\n"
C_"v223,v126,v127\n"
M_"v[108:111],a[204:207],v[60:63],v[172:175]\n"
E_"v65,v65\n"
M_"v[108:111],a[200:203],v[44:47],v[108:111]\n"
T_"a[144:145],v236 offset:512\n"
C_"v128,v64,v65\n"
M_"v[108:111],a[196:199],v[28:31],v[108:111]\n"
E_"v66,v66\n"
M_"v[108:111],a[192:195],v[12:15],v[108:111]\n"
T_"a[146:147],v236 offset:4608\n"
M_"v[112:115],a[208:211],v[0:3],v[160:163]\n"
L_"v251,s[40:43],s62 offen lds\n"
M_"v[112:115],a[212:215],v[16:19],v[112:115]\n"
A_"m0,s61,8192\n"
T_"a[148:149],v236 offset:8704\n"
M_"v[112:115],a[216:219],v[32:35],v[112:115]\n"
A_"s62,s84,8192\n"
E_"v67,v67\n"
M_"v[112:115],a[220:223],v[48:51],v[112:115]\n"
T_"a[150:151],v236 offset:12800\n"
C_"v129,v66,v67\n"
M_"v[116:119],a[220:223],v[52:55],v[164:167]\n"
E_"v68,v68\n"
M_"v[116:119],a[216:219],v[36:39],v[116:119]\n"
"s_cmp_lt_u32 s96,16\n"
"s_cselect_b32 s88,1073741824,s88\n"
T_"a[152:153],v237 offset:512\n"
M_"v[116:119],a[212:215],v[20:23],v[116:119]\n"
E_"v69,v69\n"
M_"v[116:119],a[208:211],v[4:7],v[116:119]\n"
T_"a[154:155],v237 offset:4608\n"
C_"v132,v68,v69\n"
M_"v[120:123],a[208:211],v[8:11],v[168:171]\n"
"buffer_store_dwordx2 v[244:245],v243,s[56:59],s88 offen sc1\n"
M_"v[120:123],a[212:215],v[24:27],v[120:123]\n"
T_"a[156:157],v237 offset:8704\n"
M_"v[120:123],a[216:219],v[40:43],v[120:123]\n"
E_"v70,v70\n"
M_"v[120:123],a[220:223],v[56:59],v[120:123]\n"
T_"a[158:159],v237 offset:12800\n"
M_"v[124:127],a[220:223],v[60:63],v[172:175]\n"
E_"v71,v71\n"
M_"v[124:127],a[216:219],v[44:47],v[124:127]\n"
B_"a[224:227],v144 offset:32768\n"
C_"v133,v70,v71\n"
M_"v[124:127],a[212:215],v[28:31],v[124:127]\n"
E_"v72,v72\n"
M_"v[124:127],a[208:211],v[12:15],v[124:127]\n"
B_"a[228:231],v145 offset:32768\n"
M_"a[160:163],a[176:179],v[176:179],a[160:163]\n"
L_"v251,s[40:43],s62 offen lds\n"
M_"a[160:163],a[176:179],v[208:211],a[160:163]\n"
A_"m0,s61,12288\n"
B_"a[232:235],v146 offset:32768\n"
M_"a[164:167],a[176:179],v[180:183],a[164:167]\n"
A_"s62,s84,12288\n"
E_"v73,v73\n"
M_"a[164:167],a[176:179],v[212:215],a[164:167]\n"
A_"s84,s84,0x4000\n"
B_"a[236:239],v147 offset:32768\n"
C_"v136,v72,v73\n"
M_"a[168:171],a[176:179],v[184:187],a[168:171]\n"
E_"v74,v74\n"
M_"a[168:171],a[176:179],v[216:219],a[168:171]\n"
T_"v[228:229],v225 offset:0\n"
M_"a[172:175],a[176:179],v[188:191],a[172:175]\n"
E_"v75,v75\n"
M_"a[172:175],a[176:179],v[220:223],a[172:175]\n"
W_"15)\n"
T_"v[230:231],v225 offset:512\n"
C_"v137,v74,v75\n"
M_"a[0:3],a[128:131],v[176:179],a[0:3]\n"
E_"v76,v76\n"
M_"a[0:3],a[132:135],v[208:211],a[0:3]\n"
B_"a[240:243],v144 offset:36864\n"
M_"a[4:7],a[132:135],v[212:215],a[4:7]\n"
E_"v77,v77\n"
M_"a[4:7],a[128:131],v[180:183],a[4:7]\n"
B_"a[244:247],v145 offset:36864\n"
C_"v140,v76,v77\n"
M_"a[8:11],a[128:131],v[184:187],a[8:11]\n"
E_"v78,v78\n"
M_"a[8:11],a[132:135],v[216:219],a[8:11]\n"
B_"a[248:251],v146 offset:36864\n"
M_"a[12:15],a[132:135],v[220:223],a[12:15]\n"
E_"v79,v79\n"
M_"a[12:15],a[128:131],v[188:191],a[12:15]\n"
T_"a[128:129],v236 offset:1024\n"
C_"v141,v78,v79\n"
M_"a[16:19],a[136:139],v[176:179],a[16:19]\n"
W_"15)\n"
L_"v251,s[40:43],s62 offen lds\n"
M_"a[16:19],a[140:143],v[208:211],a[16:19]\n"
A_"m0,s60,81920\n"
T_"a[130:131],v236 offset:5120\n"
M_"a[20:23],a[140:143],v[212:215],a[20:23]\n"
E_"v80,v80\n"
M_"a[20:23],a[136:139],v[180:183],a[20:23]\n"
T_"a[132:133],v236 offset:9216\n"
M_"a[24:27],a[136:139],v[184:187],a[24:27]\n"
"s_cmp_eq_u32 s95,9\n"
"s_cselect_b32 s97,s77,s97\n"
M_"a[24:27],a[140:143],v[216:219],a[24:27]\n"
"s_cmp_gt_u32 s95,9\n"
"s_cselect_b32 s97,1073741824,s97\n"
T_"a[134:135],v236 offset:13312\n"
M_"a[28:31],a[140:143],v[220:223],a[28:31]\n"
E_"v81,v81\n"
M_"a[28:31],a[136:139],v[188:191],a[28:31]\n"
T_"a[136:137],v237 offset:1024\n"
C_"v130,v80,v81\n"
M_"a[32:35],a[144:147],v[176:179],a[32:35]\n"
W_"15)\n"
E_"v82,v82\n"
M_"a[32:35],a[148:151],v[208:211],a[32:35]\n"
T_"a[138:139],v237 offset:5120\n"
M_"a[36:39],a[148:151],v[212:215],a[36:39]\n"
W_"9)\n"
O_"v[228:231],v255,s[24:27],s97 offen sc1\n"
M_"a[36:39],a[144:147],v[180:183],a[36:39]\n"
T_"a[140:141],v237 offset:9216\n"
M_"a[40:43],a[144:147],v[184:187],a[40:43]\n"
E_"v83,v83\n"
M_"a[40:43],a[148:151],v[216:219],a[40:43]\n"
T_"a[142:143],v237 offset:13312\n"
C_"v131,v82,v83\n"
M_"a[44:47],a[148:151],v[220:223],a[44:47]\n"
E_"v84,v84\n"
M_"a[44:47],a[144:147],v[188:191],a[44:47]\n"
T_"a[144:145],v236 offset:1536\n"
M_"a[48:51],a[152:155],v[176:179],a[48:51]\n"
L_"v252,s[44:47],s85 offen lds\n"
M_"a[48:51],a[156:159],v[208:211],a[48:51]\n"
T_"a[146:147],v236 offset:5632\n"
M_"a[52:55],a[156:159],v[212:215],a[52:55]\n"
E_"v85,v85\n"
M_"a[52:55],a[152:155],v[180:183],a[52:55]\n"
T_"a[148:149],v236 offset:9728\n"
C_"v134,v84,v85\n"
M_"a[56:59],a[152:155],v[184:187],a[56:59]\n"
"s_cmp_eq_u32 s96,9\n"
"s_cselect_b32 s18,s22,s18\n"
M_"a[56:59],a[156:159],v[216:219],a[56:59]\n"
"s_cmp_eq_u32 s96,9\n"
"s_cselect_b64 s[16:17],s[20:21],s[16:17]\n"
T_"a[150:151],v236 offset:13824\n"
M_"a[60:63],a[156:159],v[220:223],a[60:63]\n"
"s_cmp_eq_u32 s96,9\n"
"s_cselect_b32 s94,0,s94\n"
M_"a[60:63],a[152:155],v[188:191],a[60:63]\n"
W_"10)\n"
T_"a[152:153],v237 offset:1536\n"
M_"a[64:67],a[128:131],v[176:179],a[64:67]\n"
W_"9)\n"
"s_cmp_gt_u32 s96,9\n"
"s_cselect_b32 s18,0,s18\n"
M_"a[64:67],a[132:135],v[208:211],a[64:67]\n"
A_"s96,s96,1\n"
T_"a[154:155],v237 offset:5632\n"
M_"a[68:71],a[132:135],v[212:215],a[68:71]\n"
E_"v86,v86\n"
M_"a[68:71],a[128:131],v[180:183],a[68:71]\n"
T_"a[156:157],v237 offset:9728\n"
M_"a[72:75],a[128:131],v[184:187],a[72:75]\n"
E_"v87,v87\n"
M_"a[72:75],a[132:135],v[216:219],a[72:75]\n"
T_"a[158:159],v237 offset:13824\n"
C_"v135,v86,v87\n"
M_"a[76:79],a[132:135],v[220:223],a[76:79]\n"
E_"v88,v88\n"
M_"a[76:79],a[128:131],v[188:191],a[76:79]\n"
W_"10)\n"
B_"a[252:255],v147 offset:36864\n"
M_"a[80:83],a[136:139],v[176:179],a[80:83]\n"
W_"9)\n"
L_"v252,s[44:47],s85 offen offset:2048 lds\n"
M_"a[80:83],a[140:143],v[208:211],a[80:83]\n"
A_"m0,s60,82944\n"
B_"a[192:195],v144 offset:40960\n"
M_"a[84:87],a[140:143],v[212:215],a[84:87]\n"
A_"s85,s85,0x4000\n"
E_"v89,v89\n"
M_"a[84:87],a[136:139],v[180:183],a[84:87]\n"
B_"a[196:199],v145 offset:40960\n"
C_"v138,v88,v89\n"
M_"a[88:91],a[136:139],v[184:187],a[88:91]\n"
E_"v90,v90\n"
M_"a[88:91],a[140:143],v[216:219],a[88:91]\n"
B_"a[200:203],v146 offset:40960\n"
M_"a[92:95],a[140:143],v[220:223],a[92:95]\n"
E_"v91,v91\n"
M_"a[92:95],a[136:139],v[188:191],a[92:95]\n"
W_"10)\n"
B_"a[204:207],v147 offset:40960\n"
C_"v139,v90,v91\n"
M_"a[96:99],a[144:147],v[176:179],a[96:99]\n"
W_"9)\n"
E_"v92,v92\n"
M_"a[96:99],a[148:151],v[208:211],a[96:99]\n"
B_"a[208:211],v144 offset:45056\n"
M_"a[100:103],a[148:151],v[212:215],a[100:103]\n"
E_"v93,v93\n"
M_"a[100:103],a[144:147],v[180:183],a[100:103]\n"
B_"a[212:215],v145 offset:45056\n"
C_"v142,v92,v93\n"
M_"a[104:107],a[144:147],v[184:187],a[104:107]\n"
E_"v94,v94\n"
M_"a[104:107],a[148:151],v[216:219],a[104:107]\n"
B_"a[216:219],v146 offset:45056\n"
M_"a[108:111],a[148:151],v[220:223],a[108:111]\n"
E_"v95,v95\n"
M_"a[108:111],a[144:147],v[188:191],a[108:111]\n"
W_"10)\n"
B_"a[220:223],v147 offset:45056\n"
C_"v143,v94,v95\n"
M_"a[112:115],a[152:155],v[176:179],a[112:115]\n"
W_"9)\n"
L_"v252,s[44:47],s63 offen lds\n"
M_"a[112:115],a[156:159],v[208:211],a[112:115]\n"
E_"v96,v96\n"
M_"a[116:119],a[156:159],v[212:215],a[116:119]\n"
E_"v97,v97\n"
M_"a[116:119],a[152:155],v[180:183],a[116:119]\n"
E_"v98,v98\n"
M_"a[120:123],a[152:155],v[184:187],a[120:123]\n"
E_"v99,v99\n"
M_"a[120:123],a[156:159],v[216:219],a[120:123]\n"
E_"v100,v100\n"
M_"a[124:127],a[156:159],v[220:223],a[124:127]\n"
E_"v101,v101\n"
M_"a[124:127],a[152:155],v[188:191],a[124:127]\n"
"v_add_u32 v226,1,v226\n"
C_"v192,v96,v97\n"
M_"v[64:67],a[224:227],v[0:3],v[160:163]\n"
E_"v102,v102\n"
M_"v[64:67],a[228:231],v[16:19],v[64:67]\n"
E_"v103,v103\n"
M_"v[64:67],a[232:235],v[32:35],v[64:67]\n"
E_"v104,v104\n"
M_"v[64:67],a[236:239],v[48:51],v[64:67]\n"
E_"v105,v105\n"
M_"v[68:71],a[236:239],v[52:55],v[164:167]\n"
E_"v106,v106\n"
M_"v[68:71],a[232:235],v[36:39],v[68:71]\n"
E_"v107,v107\n"
M_"v[68:71],a[228:231],v[20:23],v[68:71]\n"
"s_waitcnt vmcnt(20) lgkmcnt(0)\n"
"ds_write_b32 v227,v226\n"
C_"v193,v98,v99\n"
M_"v[68:71],a[224:227],v[4:7],v[68:71]\n"
L_"v252,s[44:47],s63 offen offset:2048 lds\n"
M_"v[72:75],a[224:227],v[8:11],v[168:171]\n"
A_"m0,s35,0\n"
E_"v108,v108\n"
M_"v[72:75],a[228:231],v[24:27],v[72:75]\n"
L_"v224,s[16:19],s94 offen lds\n"
M_"v[72:75],a[232:235],v[40:43],v[72:75]\n"
A_"s94,s94,32\n"
E_"v109,v109\n"
M_"v[72:75],a[236:239],v[56:59],v[72:75]\n"
"s_sub_u32 s95,s94,288\n"
E_"v110,v110\n"
M_"v[76:79],a[236:239],v[60:63],v[172:175]\n"
"s_min_u32 s94,s94,s95\n"
E_"v111,v111\n"
M_"v[76:79],a[232:235],v[44:47],v[76:79]\n"
E_"v112,v112\n"
M_"v[76:79],a[228:231],v[28:31],v[76:79]\n"
E_"v113,v113\n"
M_"v[76:79],a[224:227],v[12:15],v[76:79]\n"
E_"v114,v114\n"
M_"v[80:83],a[240:243],v[0:3],v[160:163]\n"
E_"v115,v115\n"
M_"v[80:83],a[244:247],v[16:19],v[80:83]\n"
E_"v116,v116\n"
M_"v[80:83],a[248:251],v[32:35],v[80:83]\n"
E_"v117,v117\n"
M_"v[80:83],a[252:255],v[48:51],v[80:83]\n"
"ds_read_b32 v241,v240 offset:0\n"
C_"v196,v100,v101\n"
M_"v[84:87],a[252:255],v[52:55],v[164:167]\n"
E_"v118,v118\n"
M_"v[84:87],a[248:251],v[36:39],v[84:87]\n"
E_"v119,v119\n"
M_"v[84:87],a[244:247],v[20:23],v[84:87]\n"
C_"v197,v102,v103\n"
C_"v200,v104,v105\n"
M_"v[84:87],a[240:243],v[4:7],v[84:87]\n"
E_"v120,v120\n"
M_"v[88:91],a[240:243],v[8:11],v[168:171]\n"
C_"v201,v106,v107\n"
C_"v204,v108,v109\n"
M_"v[88:91],a[244:247],v[24:27],v[88:91]\n"
E_"v121,v121\n"
M_"v[88:91],a[248:251],v[40:43],v[88:91]\n"
W_"0)\n"
"v_cmp_lt_u32 vcc,v241,v226\n"
C_"v205,v110,v111\n"
M_"v[88:91],a[252:255],v[56:59],v[88:91]\n"
E_"v122,v122\n"
M_"v[92:95],a[252:255],v[60:63],v[172:175]\n"
"s_cbranch_vccnz SPINBD1_%=\n"
"BACKBD1_%=:\n"
A_"m0,s61,32768\n"
M_"v[92:95],a[248:251],v[44:47],v[92:95]\n"
A_"s62,s84,4096\n"
T_"a[128:129],v152 offset:16384\n"
C_"v194,v112,v113\n"
M_"v[92:95],a[244:247],v[28:31],v[92:95]\n"
"s_sub_u32 s95,s96,4\n"
E_"v123,v123\n"
M_"v[92:95],a[240:243],v[12:15],v[92:95]\n"
"s_lshl_b32 s97,s95,10\n"
T_"a[130:131],v152 offset:20480\n"
C_"v195,v114,v115\n"
M_"v[96:99],a[192:195],v[0:3],v[160:163]\n"
A_"s97,s97,s76\n"
L_"v251,s[40:43],s84 offen lds\n"
M_"v[96:99],a[196:199],v[16:19],v[96:99]\n"
A_"m0,s61,36864\n"
T_"a[132:133],v152 offset:24576\n"
C_"v198,v116,v117\n"
M_"v[96:99],a[200:203],v[32:35],v[96:99]\n"
A_"s63,s85,128\n"
E_"v124,v124\n"
M_"v[96:99],a[204:207],v[48:51],v[96:99]\n"
T_"a[134:135],v152 offset:28672\n"
C_"v199,v118,v119\n"
M_"v[100:103],a[204:207],v[52:55],v[164:167]\n"
E_"v125,v125\n"
M_"v[100:103],a[200:203],v[36:39],v[100:103]\n"
T_"a[136:137],v153 offset:16384\n"
C_"v202,v120,v121\n"
M_"v[100:103],a[196:199],v[20:23],v[100:103]\n"
E_"v126,v126\n"
M_"v[100:103],a[192:195],v[4:7],v[100:103]\n"
T_"a[138:139],v153 offset:20480\n"
C_"v203,v122,v123\n"
M_"v[104:107],a[192:195],v[8:11],v[168:171]\n"
E_"v127,v127\n"
M_"v[104:107],a[196:199],v[24:27],v[104:107]\n"
T_"a[140:141],v153 offset:24576\n"
C_"v206,v124,v125\n"
M_"v[104:107],a[200:203],v[40:43],v[104:107]\n"
E_"v64,v64\n"
M_"v[104:107],a[204:207],v[56:59],v[104:107]\n"
T_"a[142:143],v153 offset:28672\n"
C_"v207,v126,v127\n"
M_"v[108:111],a[204:207],v[60:63],v[172:175]\n"
E_"v65,v65\n"
M_"v[108:111],a[200:203],v[44:47],v[108:111]\n"
T_"a[144:145],v152 offset:16896\n"
C_"v176,v64,v65\n"
M_"v[108:111],a[196:199],v[28:31],v[108:111]\n"
E_"v66,v66\n"
M_"v[108:111],a[192:195],v[12:15],v[108:111]\n"
T_"a[146:147],v152 offset:20992\n"
M_"v[112:115],a[208:211],v[0:3],v[160:163]\n"
L_"v251,s[40:43],s62 offen lds\n"
M_"v[112:115],a[212:215],v[16:19],v[112:115]\n"
A_"m0,s61,40960\n"
T_"a[148:149],v152 offset:25088\n"
M_"v[112:115],a[216:219],v[32:35],v[112:115]\n"
A_"s62,s84,8192\n"
E_"v67,v67\n"
M_"v[112:115],a[220:223],v[48:51],v[112:115]\n"
T_"a[150:151],v152 offset:29184\n"
C_"v177,v66,v67\n"
M_"v[116:119],a[220:223],v[52:55],v[164:167]\n"
E_"v68,v68\n"
M_"v[116:119],a[216:219],v[36:39],v[116:119]\n"
T_"a[152:153],v153 offset:16896\n"
M_"v[116:119],a[212:215],v[20:23],v[116:119]\n"
E_"v69,v69\n"
M_"v[116:119],a[208:211],v[4:7],v[116:119]\n"
T_"a[154:155],v153 offset:20992\n"
C_"v180,v68,v69\n"
M_"v[120:123],a[208:211],v[8:11],v[168:171]\n"
E_"v70,v70\n"
M_"v[120:123],a[212:215],v[24:27],v[120:123]\n"
T_"a[156:157],v153 offset:25088\n"
M_"v[120:123],a[216:219],v[40:43],v[120:123]\n"
E_"v71,v71\n"
M_"v[120:123],a[220:223],v[56:59],v[120:123]\n"
T_"a[158:159],v153 offset:29184\n"
C_"v181,v70,v71\n"
M_"v[124:127],a[220:223],v[60:63],v[172:175]\n"
E_"v72,v72\n"
M_"v[124:127],a[216:219],v[44:47],v[124:127]\n"
B_"a[224:227],v148 offset:0\n"
M_"v[124:127],a[212:215],v[28:31],v[124:127]\n"
E_"v73,v73\n"
M_"v[124:127],a[208:211],v[12:15],v[124:127]\n"
B_"a[228:231],v149 offset:0\n"
C_"v184,v72,v73\n"
M_"a[160:163],a[176:179],v[128:131],a[160:163]\n"
L_"v251,s[40:43],s62 offen lds\n"
M_"a[160:163],a[176:179],v[192:195],a[160:163]\n"
A_"m0,s61,45056\n"
B_"a[232:235],v150 offset:0\n"
M_"a[164:167],a[176:179],v[132:135],a[164:167]\n"
A_"s62,s84,12288\n"
E_"v74,v74\n"
M_"a[164:167],a[176:179],v[196:199],a[164:167]\n"
A_"s84,s84,0x4000\n"
B_"a[236:239],v151 offset:0\n"
M_"a[168:171],a[176:179],v[136:139],a[168:171]\n"
E_"v75,v75\n"
M_"a[168:171],a[176:179],v[200:203],a[168:171]\n"
T_"v[232:233],v225 offset:1024\n"
C_"v185,v74,v75\n"
M_"a[172:175],a[176:179],v[140:143],a[172:175]\n"
E_"v76,v76\n"
M_"a[172:175],a[176:179],v[204:207],a[172:175]\n"
W_"15)\n"
T_"v[234:235],v225 offset:1536\n"
M_"a[0:3],a[128:131],v[128:131],a[0:3]\n"
E_"v77,v77\n"
M_"a[0:3],a[132:135],v[192:195],a[0:3]\n"
B_"a[240:243],v148 offset:4096\n"
C_"v188,v76,v77\n"
M_"a[4:7],a[132:135],v[196:199],a[4:7]\n"
E_"v78,v78\n"
M_"a[4:7],a[128:131],v[132:135],a[4:7]\n"
B_"a[244:247],v149 offset:4096\n"
M_"a[8:11],a[128:131],v[136:139],a[8:11]\n"
E_"v79,v79\n"
M_"a[8:11],a[132:135],v[200:203],a[8:11]\n"
B_"a[248:251],v150 offset:4096\n"
C_"v189,v78,v79\n"
M_"a[12:15],a[132:135],v[204:207],a[12:15]\n"
E_"v80,v80\n"
M_"a[12:15],a[128:131],v[140:143],a[12:15]\n"
T_"a[128:129],v152 offset:17408\n"
M_"a[16:19],a[136:139],v[128:131],a[16:19]\n"
W_"15)\n"
L_"v251,s[40:43],s62 offen lds\n"
M_"a[16:19],a[140:143],v[192:195],a[16:19]\n"
A_"m0,s60,147456\n"
T_"a[130:131],v152 offset:21504\n"
M_"a[20:23],a[140:143],v[196:199],a[20:23]\n"
E_"v81,v81\n"
M_"a[20:23],a[136:139],v[132:135],a[20:23]\n"
T_"a[132:133],v152 offset:25600\n"
C_"v178,v80,v81\n"
M_"a[24:27],a[136:139],v[136:139],a[24:27]\n"
"s_cmp_eq_u32 s95,9\n"
"s_cselect_b32 s97,s77,s97\n"
M_"a[24:27],a[140:143],v[200:203],a[24:27]\n"
"s_cmp_gt_u32 s95,9\n"
"s_cselect_b32 s97,1073741824,s97\n"
T_"a[134:135],v152 offset:29696\n"
M_"a[28:31],a[140:143],v[204:207],a[28:31]\n"
E_"v82,v82\n"
M_"a[28:31],a[136:139],v[140:143],a[28:31]\n"
T_"a[136:137],v153 offset:17408\n"
M_"a[32:35],a[144:147],v[128:131],a[32:35]\n"
W_"15)\n"
E_"v83,v83\n"
M_"a[32:35],a[148:151],v[192:195],a[32:35]\n"
T_"a[138:139],v153 offset:21504\n"
C_"v179,v82,v83\n"
M_"a[36:39],a[148:151],v[196:199],a[36:39]\n"
W_"9)\n"
O_"v[232:235],v255,s[24:27],s97 offen sc1\n"
M_"a[36:39],a[144:147],v[132:135],a[36:39]\n"
T_"a[140:141],v153 offset:25600\n"
M_"a[40:43],a[144:147],v[136:139],a[40:43]\n"
E_"v84,v84\n"
M_"a[40:43],a[148:151],v[200:203],a[40:43]\n"
T_"a[142:143],v153 offset:29696\n"
M_"a[44:47],a[148:151],v[204:207],a[44:47]\n"
E_"v85,v85\n"
M_"a[44:47],a[144:147],v[140:143],a[44:47]\n"
T_"a[144:145],v152 offset:17920\n"
C_"v182,v84,v85\n"
M_"a[48:51],a[152:155],v[128:131],a[48:51]\n"
L_"v252,s[44:47],s85 offen lds\n"
M_"a[48:51],a[156:159],v[192:195],a[48:51]\n"
T_"a[146:147],v152 offset:22016\n"
M_"a[52:55],a[156:159],v[196:199],a[52:55]\n"
E_"v86,v86\n"
M_"a[52:55],a[152:155],v[132:135],a[52:55]\n"
T_"a[148:149],v152 offset:26112\n"
M_"a[56:59],a[152:155],v[136:139],a[56:59]\n"
"s_cmp_eq_u32 s96,9\n"
"s_cselect_b32 s18,s22,s18\n"
M_"a[56:59],a[156:159],v[200:203],a[56:59]\n"
"s_cmp_eq_u32 s96,9\n"
"s_cselect_b64 s[16:17],s[20:21],s[16:17]\n"
T_"a[150:151],v152 offset:30208\n"
M_"a[60:63],a[156:159],v[204:207],a[60:63]\n"
"s_cmp_eq_u32 s96,9\n"
"s_cselect_b32 s94,0,s94\n"
M_"a[60:63],a[152:155],v[140:143],a[60:63]\n"
W_"10)\n"
T_"a[152:153],v153 offset:17920\n"
M_"a[64:67],a[128:131],v[128:131],a[64:67]\n"
W_"9)\n"
"s_cmp_gt_u32 s96,9\n"
"s_cselect_b32 s18,0,s18\n"
M_"a[64:67],a[132:135],v[192:195],a[64:67]\n"
A_"s96,s96,1\n"
T_"a[154:155],v153 offset:22016\n"
M_"a[68:71],a[132:135],v[196:199],a[68:71]\n"
E_"v87,v87\n"
M_"a[68:71],a[128:131],v[132:135],a[68:71]\n"
T_"a[156:157],v153 offset:26112\n"
C_"v183,v86,v87\n"
M_"a[72:75],a[128:131],v[136:139],a[72:75]\n"
E_"v88,v88\n"
M_"a[72:75],a[132:135],v[200:203],a[72:75]\n"
T_"a[158:159],v153 offset:30208\n"
M_"a[76:79],a[132:135],v[204:207],a[76:79]\n"
E_"v89,v89\n"
M_"a[76:79],a[128:131],v[140:143],a[76:79]\n"
W_"10)\n"
B_"a[252:255],v151 offset:4096\n"
C_"v186,v88,v89\n"
M_"a[80:83],a[136:139],v[128:131],a[80:83]\n"
W_"9)\n"
L_"v252,s[44:47],s85 offen offset:2048 lds\n"
M_"a[80:83],a[140:143],v[192:195],a[80:83]\n"
A_"m0,s60,148480\n"
B_"a[192:195],v148 offset:8192\n"
M_"a[84:87],a[140:143],v[196:199],a[84:87]\n"
A_"s85,s85,0x4000\n"
E_"v90,v90\n"
M_"a[84:87],a[136:139],v[132:135],a[84:87]\n"
B_"a[196:199],v149 offset:8192\n"
M_"a[88:91],a[136:139],v[136:139],a[88:91]\n"
E_"v91,v91\n"
M_"a[88:91],a[140:143],v[200:203],a[88:91]\n"
B_"a[200:203],v150 offset:8192\n"
C_"v187,v90,v91\n"
M_"a[92:95],a[140:143],v[204:207],a[92:95]\n"
E_"v92,v92\n"
M_"a[92:95],a[136:139],v[140:143],a[92:95]\n"
W_"10)\n"
B_"a[204:207],v151 offset:8192\n"
M_"a[96:99],a[144:147],v[128:131],a[96:99]\n"
W_"9)\n"
E_"v93,v93\n"
M_"a[96:99],a[148:151],v[192:195],a[96:99]\n"
B_"a[208:211],v148 offset:12288\n"
C_"v190,v92,v93\n"
M_"a[100:103],a[148:151],v[196:199],a[100:103]\n"
E_"v94,v94\n"
M_"a[100:103],a[144:147],v[132:135],a[100:103]\n"
B_"a[212:215],v149 offset:12288\n"
M_"a[104:107],a[144:147],v[136:139],a[104:107]\n"
E_"v95,v95\n"
M_"a[104:107],a[148:151],v[200:203],a[104:107]\n"
B_"a[216:219],v150 offset:12288\n"
C_"v191,v94,v95\n"
M_"a[108:111],a[148:151],v[204:207],a[108:111]\n"
E_"v96,v96\n"
M_"a[108:111],a[144:147],v[140:143],a[108:111]\n"
W_"10)\n"
B_"a[220:223],v151 offset:12288\n"
M_"a[112:115],a[152:155],v[128:131],a[112:115]\n"
W_"9)\n"
L_"v252,s[44:47],s63 offen lds\n"
M_"a[112:115],a[156:159],v[192:195],a[112:115]\n"
E_"v97,v97\n"
M_"a[116:119],a[156:159],v[196:199],a[116:119]\n"
E_"v98,v98\n"
M_"a[116:119],a[152:155],v[132:135],a[116:119]\n"
E_"v99,v99\n"
M_"a[120:123],a[152:155],v[136:139],a[120:123]\n"
E_"v100,v100\n"
M_"a[120:123],a[156:159],v[200:203],a[120:123]\n"
E_"v101,v101\n"
M_"a[124:127],a[156:159],v[204:207],a[124:127]\n"
E_"v102,v102\n"
M_"a[124:127],a[152:155],v[140:143],a[124:127]\n"
"v_add_u32 v226,1,v226\n"
C_"v208,v96,v97\n"
M_"v[64:67],a[224:227],v[0:3],v[160:163]\n"
E_"v103,v103\n"
M_"v[64:67],a[228:231],v[16:19],v[64:67]\n"
E_"v104,v104\n"
M_"v[64:67],a[232:235],v[32:35],v[64:67]\n"
E_"v105,v105\n"
M_"v[64:67],a[236:239],v[48:51],v[64:67]\n"
E_"v106,v106\n"
M_"v[68:71],a[236:239],v[52:55],v[164:167]\n"
E_"v107,v107\n"
M_"v[68:71],a[232:235],v[36:39],v[68:71]\n"
E_"v108,v108\n"
M_"v[68:71],a[228:231],v[20:23],v[68:71]\n"
"s_waitcnt vmcnt(20) lgkmcnt(0)\n"
"ds_write_b32 v227,v226\n"
C_"v209,v98,v99\n"
M_"v[68:71],a[224:227],v[4:7],v[68:71]\n"
L_"v252,s[44:47],s63 offen offset:2048 lds\n"
M_"v[72:75],a[224:227],v[8:11],v[168:171]\n"
A_"m0,s35,1024\n"
E_"v109,v109\n"
M_"v[72:75],a[228:231],v[24:27],v[72:75]\n"
L_"v224,s[16:19],s94 offen lds\n"
M_"v[72:75],a[232:235],v[40:43],v[72:75]\n"
A_"s94,s94,32\n"
E_"v110,v110\n"
M_"v[72:75],a[236:239],v[56:59],v[72:75]\n"
"s_sub_u32 s95,s94,288\n"
E_"v111,v111\n"
M_"v[76:79],a[236:239],v[60:63],v[172:175]\n"
"s_min_u32 s94,s94,s95\n"
E_"v112,v112\n"
M_"v[76:79],a[232:235],v[44:47],v[76:79]\n"
E_"v113,v113\n"
M_"v[76:79],a[228:231],v[28:31],v[76:79]\n"
E_"v114,v114\n"
M_"v[76:79],a[224:227],v[12:15],v[76:79]\n"
E_"v115,v115\n"
M_"v[80:83],a[240:243],v[0:3],v[160:163]\n"
E_"v116,v116\n"
M_"v[80:83],a[244:247],v[16:19],v[80:83]\n"
E_"v117,v117\n"
M_"v[80:83],a[248:251],v[32:35],v[80:83]\n"
E_"v118,v118\n"
M_"v[80:83],a[252:255],v[48:51],v[80:83]\n"
"ds_read_b32 v241,v240 offset:0\n"
C_"v212,v100,v101\n"
M_"v[84:87],a[252:255],v[52:55],v[164:167]\n"
E_"v119,v119\n"
M_"v[84:87],a[248:251],v[36:39],v[84:87]\n"
C_"v213,v102,v103\n"
C_"v216,v104,v105\n"
M_"v[84:87],a[244:247],v[20:23],v[84:87]\n"
C_"v217,v106,v107\n"
C_"v220,v108,v109\n"
M_"v[84:87],a[240:243],v[4:7],v[84:87]\n"
E_"v120,v120\n"
M_"v[88:91],a[240:243],v[8:11],v[168:171]\n"
C_"v221,v110,v111\n"
C_"v210,v112,v113\n"
M_"v[88:91],a[244:247],v[24:27],v[88:91]\n"
E_"v121,v121\n"
M_"v[88:91],a[248:251],v[40:43],v[88:91]\n"
W_"0)\n"
"v_cmp_lt_u32 vcc,v241,v226\n"
C_"v211,v114,v115\n"
M_"v[88:91],a[252:255],v[56:59],v[88:91]\n"
E_"v122,v122\n"
M_"v[92:95],a[252:255],v[60:63],v[172:175]\n"
"s_cbranch_vccnz SPINBD2_%=\n"
"BACKBD2_%=:\n"
A_"m0,s61,65536\n"
M_"v[92:95],a[248:251],v[44:47],v[92:95]\n"
A_"s62,s84,4096\n"
T_"a[128:129],v152 offset:49152\n"
C_"v214,v116,v117\n"
M_"v[92:95],a[244:247],v[28:31],v[92:95]\n"
"s_sub_u32 s95,s96,4\n"
E_"v123,v123\n"
M_"v[92:95],a[240:243],v[12:15],v[92:95]\n"
"s_lshl_b32 s97,s95,10\n"
T_"a[130:131],v152 offset:53248\n"
C_"v215,v118,v119\n"
M_"v[96:99],a[192:195],v[0:3],v[160:163]\n"
A_"s97,s97,s76\n"
L_"v251,s[40:43],s84 offen lds\n"
M_"v[96:99],a[196:199],v[16:19],v[96:99]\n"
A_"m0,s61,69632\n"
T_"a[132:133],v152 offset:57344\n"
C_"v218,v120,v121\n"
M_"v[96:99],a[200:203],v[32:35],v[96:99]\n"
A_"s63,s85,128\n"
E_"v124,v124\n"
M_"v[96:99],a[204:207],v[48:51],v[96:99]\n"
T_"a[134:135],v152 offset:61440\n"
C_"v219,v122,v123\n"
M_"v[100:103],a[204:207],v[52:55],v[164:167]\n"
E_"v125,v125\n"
M_"v[100:103],a[200:203],v[36:39],v[100:103]\n"
T_"a[136:137],v153 offset:49152\n"
C_"v222,v124,v125\n"
M_"v[100:103],a[196:199],v[20:23],v[100:103]\n"
E_"v126,v126\n"
M_"v[100:103],a[192:195],v[4:7],v[100:103]\n"
T_"a[138:139],v153 offset:53248\n"
M_"v[104:107],a[192:195],v[8:11],v[168:171]\n"
E_"v127,v127\n"
M_"v[104:107],a[196:199],v[24:27],v[104:107]\n"
T_"a[140:141],v153 offset:57344\n"
C_"v223,v126,v127\n"
M_"v[104:107],a[200:203],v[40:43],v[104:107]\n"
E_"v64,v64\n"
M_"v[104:107],a[204:207],v[56:59],v[104:107]\n"
T_"a[142:143],v153 offset:61440\n"
M_"v[108:111],a[204:207],v[60:63],v[172:175]\n"
E_"v65,v65\n"
M_"v[108:111],a[200:203],v[44:47],v[108:111]\n"
T_"a[144:145],v152 offset:49664\n"
C_"v128,v64,v65\n"
M_"v[108:111],a[196:199],v[28:31],v[108:111]\n"
E_"v66,v66\n"
M_"v[108:111],a[192:195],v[12:15],v[108:111]\n"
T_"a[146:147],v152 offset:53760\n"
M_"v[112:115],a[208:211],v[0:3],v[160:163]\n"
L_"v251,s[40:43],s62 offen lds\n"
M_"v[112:115],a[212:215],v[16:19],v[112:115]\n"
A_"m0,s61,73728\n"
T_"a[148:149],v152 offset:57856\n"
M_"v[112:115],a[216:219],v[32:35],v[112:115]\n"
A_"s62,s84,8192\n"
E_"v67,v67\n"
M_"v[112:115],a[220:223],v[48:51],v[112:115]\n"
T_"a[150:151],v152 offset:61952\n"
C_"v129,v66,v67\n"
M_"v[116:119],a[220:223],v[52:55],v[164:167]\n"
E_"v68,v68\n"
M_"v[116:119],a[216:219],v[36:39],v[116:119]\n"
T_"a[152:153],v153 offset:49664\n"
M_"v[116:119],a[212:215],v[20:23],v[116:119]\n"
E_"v69,v69\n"
M_"v[116:119],a[208:211],v[4:7],v[116:119]\n"
T_"a[154:155],v153 offset:53760\n"
C_"v132,v68,v69\n"
M_"v[120:123],a[208:211],v[8:11],v[168:171]\n"
E_"v70,v70\n"
M_"v[120:123],a[212:215],v[24:27],v[120:123]\n"
T_"a[156:157],v153 offset:57856\n"
M_"v[120:123],a[216:219],v[40:43],v[120:123]\n"
E_"v71,v71\n"
M_"v[120:123],a[220:223],v[56:59],v[120:123]\n"
T_"a[158:159],v153 offset:61952\n"
C_"v133,v70,v71\n"
M_"v[124:127],a[220:223],v[60:63],v[172:175]\n"
E_"v72,v72\n"
M_"v[124:127],a[216:219],v[44:47],v[124:127]\n"
B_"a[224:227],v148 offset:32768\n"
M_"v[124:127],a[212:215],v[28:31],v[124:127]\n"
E_"v73,v73\n"
M_"v[124:127],a[208:211],v[12:15],v[124:127]\n"
B_"a[228:231],v149 offset:32768\n"
C_"v136,v72,v73\n"
M_"a[160:163],a[176:179],v[176:179],a[160:163]\n"
L_"v251,s[40:43],s62 offen lds\n"
M_"a[160:163],a[176:179],v[208:211],a[160:163]\n"
A_"m0,s61,77824\n"
B_"a[232:235],v150 offset:32768\n"
M_"a[164:167],a[176:179],v[180:183],a[164:167]\n"
A_"s62,s84,12288\n"
E_"v74,v74\n"
M_"a[164:167],a[176:179],v[212:215],a[164:167]\n"
A_"s84,s84,0x4000\n"
B_"a[236:239],v151 offset:32768\n"
M_"a[168:171],a[176:179],v[184:187],a[168:171]\n"
E_"v75,v75\n"
M_"a[168:171],a[176:179],v[216:219],a[168:171]\n"
T_"v[228:229],v225 offset:2048\n"
C_"v137,v74,v75\n"
M_"a[172:175],a[176:179],v[188:191],a[172:175]\n"
E_"v76,v76\n"
M_"a[172:175],a[176:179],v[220:223],a[172:175]\n"
W_"15)\n"
T_"v[230:231],v225 offset:2560\n"
M_"a[0:3],a[128:131],v[176:179],a[0:3]\n"
E_"v77,v77\n"
M_"a[0:3],a[132:135],v[208:211],a[0:3]\n"
B_"a[240:243],v148 offset:36864\n"
C_"v140,v76,v77\n"
M_"a[4:7],a[132:135],v[212:215],a[4:7]\n"
E_"v78,v78\n"
M_"a[4:7],a[128:131],v[180:183],a[4:7]\n"
B_"a[244:247],v149 offset:36864\n"
M_"a[8:11],a[128:131],v[184:187],a[8:11]\n"
E_"v79,v79\n"
M_"a[8:11],a[132:135],v[216:219],a[8:11]\n"
B_"a[248:251],v150 offset:36864\n"
C_"v141,v78,v79\n"
M_"a[12:15],a[132:135],v[220:223],a[12:15]\n"
E_"v80,v80\n"
M_"a[12:15],a[128:131],v[188:191],a[12:15]\n"
T_"a[128:129],v152 offset:50176\n"
M_"a[16:19],a[136:139],v[176:179],a[16:19]\n"
W_"15)\n"
L_"v251,s[40:43],s62 offen lds\n"
M_"a[16:19],a[140:143],v[208:211],a[16:19]\n"
A_"m0,s60,16384\n"
T_"a[130:131],v152 offset:54272\n"
M_"a[20:23],a[140:143],v[212:215],a[20:23]\n"
E_"v81,v81\n"
M_"a[20:23],a[136:139],v[180:183],a[20:23]\n"
T_"a[132:133],v152 offset:58368\n"
C_"v130,v80,v81\n"
M_"a[24:27],a[136:139],v[184:187],a[24:27]\n"
"s_cmp_eq_u32 s95,9\n"
"s_cselect_b32 s97,s77,s97\n"
M_"a[24:27],a[140:143],v[216:219],a[24:27]\n"
"s_cmp_gt_u32 s95,9\n"
"s_cselect_b32 s97,1073741824,s97\n"
T_"a[134:135],v152 offset:62464\n"
M_"a[28:31],a[140:143],v[220:223],a[28:31]\n"
E_"v82,v82\n"
M_"a[28:31],a[136:139],v[188:191],a[28:31]\n"
T_"a[136:137],v153 offset:50176\n"
M_"a[32:35],a[144:147],v[176:179],a[32:35]\n"
W_"15)\n"
E_"v83,v83\n"
M_"a[32:35],a[148:151],v[208:211],a[32:35]\n"
T_"a[138:139],v153 offset:54272\n"
C_"v131,v82,v83\n"
M_"a[36:39],a[148:151],v[212:215],a[36:39]\n"
W_"9)\n"
O_"v[228:231],v255,s[24:27],s97 offen sc1\n"
M_"a[36:39],a[144:147],v[180:183],a[36:39]\n"
T_"a[140:141],v153 offset:58368\n"
M_"a[40:43],a[144:147],v[184:187],a[40:43]\n"
E_"v84,v84\n"
M_"a[40:43],a[148:151],v[216:219],a[40:43]\n"
T_"a[142:143],v153 offset:62464\n"
M_"a[44:47],a[148:151],v[220:223],a[44:47]\n"
E_"v85,v85\n"
M_"a[44:47],a[144:147],v[188:191],a[44:47]\n"
T_"a[144:145],v152 offset:50688\n"
C_"v134,v84,v85\n"
M_"a[48:51],a[152:155],v[176:179],a[48:51]\n"
L_"v252,s[44:47],s85 offen lds\n"
M_"a[48:51],a[156:159],v[208:211],a[48:51]\n"
T_"a[146:147],v152 offset:54784\n"
M_"a[52:55],a[156:159],v[212:215],a[52:55]\n"
E_"v86,v86\n"
M_"a[52:55],a[152:155],v[180:183],a[52:55]\n"
T_"a[148:149],v152 offset:58880\n"
M_"a[56:59],a[152:155],v[184:187],a[56:59]\n"
"s_cmp_eq_u32 s96,9\n"
"s_cselect_b32 s18,s22,s18\n"
M_"a[56:59],a[156:159],v[216:219],a[56:59]\n"
"s_cmp_eq_u32 s96,9\n"
"s_cselect_b64 s[16:17],s[20:21],s[16:17]\n"
T_"a[150:151],v152 offset:62976\n"
M_"a[60:63],a[156:159],v[220:223],a[60:63]\n"
"s_cmp_eq_u32 s96,9\n"
"s_cselect_b32 s94,0,s94\n"
M_"a[60:63],a[152:155],v[188:191],a[60:63]\n"
W_"10)\n"
T_"a[152:153],v153 offset:50688\n"
M_"a[64:67],a[128:131],v[176:179],a[64:67]\n"
W_"9)\n"
"s_cmp_gt_u32 s96,9\n"
"s_cselect_b32 s18,0,s18\n"
M_"a[64:67],a[132:135],v[208:211],a[64:67]\n"
A_"s96,s96,1\n"
T_"a[154:155],v153 offset:54784\n"
M_"a[68:71],a[132:135],v[212:215],a[68:71]\n"
E_"v87,v87\n"
M_"a[68:71],a[128:131],v[180:183],a[68:71]\n"
T_"a[156:157],v153 offset:58880\n"
C_"v135,v86,v87\n"
M_"a[72:75],a[128:131],v[184:187],a[72:75]\n"
E_"v88,v88\n"
M_"a[72:75],a[132:135],v[216:219],a[72:75]\n"
T_"a[158:159],v153 offset:62976\n"
M_"a[76:79],a[132:135],v[220:223],a[76:79]\n"
E_"v89,v89\n"
M_"a[76:79],a[128:131],v[188:191],a[76:79]\n"
W_"10)\n"
B_"a[252:255],v151 offset:36864\n"
C_"v138,v88,v89\n"
M_"a[80:83],a[136:139],v[176:179],a[80:83]\n"
W_"9)\n"
L_"v252,s[44:47],s85 offen offset:2048 lds\n"
M_"a[80:83],a[140:143],v[208:211],a[80:83]\n"
A_"m0,s60,17408\n"
B_"a[192:195],v148 offset:40960\n"
M_"a[84:87],a[140:143],v[212:215],a[84:87]\n"
A_"s85,s85,0x4000\n"
E_"v90,v90\n"
M_"a[84:87],a[136:139],v[180:183],a[84:87]\n"
B_"a[196:199],v149 offset:40960\n"
M_"a[88:91],a[136:139],v[184:187],a[88:91]\n"
E_"v91,v91\n"
M_"a[88:91],a[140:143],v[216:219],a[88:91]\n"
B_"a[200:203],v150 offset:40960\n"
C_"v139,v90,v91\n"
M_"a[92:95],a[140:143],v[220:223],a[92:95]\n"
E_"v92,v92\n"
M_"a[92:95],a[136:139],v[188:191],a[92:95]\n"
W_"10)\n"
B_"a[204:207],v151 offset:40960\n"
M_"a[96:99],a[144:147],v[176:179],a[96:99]\n"
W_"9)\n"
E_"v93,v93\n"
M_"a[96:99],a[148:151],v[208:211],a[96:99]\n"
B_"a[208:211],v148 offset:45056\n"
C_"v142,v92,v93\n"
M_"a[100:103],a[148:151],v[212:215],a[100:103]\n"
E_"v94,v94\n"
M_"a[100:103],a[144:147],v[180:183],a[100:103]\n"
B_"a[212:215],v149 offset:45056\n"
M_"a[104:107],a[144:147],v[184:187],a[104:107]\n"
E_"v95,v95\n"
M_"a[104:107],a[148:151],v[216:219],a[104:107]\n"
B_"a[216:219],v150 offset:45056\n"
C_"v143,v94,v95\n"
M_"a[108:111],a[148:151],v[220:223],a[108:111]\n"
E_"v96,v96\n"
M_"a[108:111],a[144:147],v[188:191],a[108:111]\n"
W_"10)\n"
B_"a[220:223],v151 offset:45056\n"
M_"a[112:115],a[152:155],v[176:179],a[112:115]\n"
W_"9)\n"
L_"v252,s[44:47],s63 offen lds\n"
M_"a[112:115],a[156:159],v[208:211],a[112:115]\n"
E_"v97,v97\n"
M_"a[116:119],a[156:159],v[212:215],a[116:119]\n"
E_"v98,v98\n"
M_"a[116:119],a[152:155],v[180:183],a[116:119]\n"
E_"v99,v99\n"
M_"a[120:123],a[152:155],v[184:187],a[120:123]\n"
E_"v100,v100\n"
M_"a[120:123],a[156:159],v[216:219],a[120:123]\n"
E_"v101,v101\n"
M_"a[124:127],a[156:159],v[220:223],a[124:127]\n"
E_"v102,v102\n"
M_"a[124:127],a[152:155],v[188:191],a[124:127]\n"
"v_add_u32 v226,1,v226\n"
C_"v192,v96,v97\n"
M_"v[64:67],a[224:227],v[0:3],v[160:163]\n"
E_"v103,v103\n"
M_"v[64:67],a[228:231],v[16:19],v[64:67]\n"
E_"v104,v104\n"
M_"v[64:67],a[232:235],v[32:35],v[64:67]\n"
E_"v105,v105\n"
M_"v[64:67],a[236:239],v[48:51],v[64:67]\n"
E_"v106,v106\n"
M_"v[68:71],a[236:239],v[52:55],v[164:167]\n"
E_"v107,v107\n"
M_"v[68:71],a[232:235],v[36:39],v[68:71]\n"
E_"v108,v108\n"
M_"v[68:71],a[228:231],v[20:23],v[68:71]\n"
"s_waitcnt vmcnt(19) lgkmcnt(0)\n"
"ds_write_b32 v227,v226\n"
C_"v193,v98,v99\n"
M_"v[68:71],a[224:227],v[4:7],v[68:71]\n"
L_"v252,s[44:47],s63 offen offset:2048 lds\n"
M_"v[72:75],a[224:227],v[8:11],v[168:171]\n"
A_"m0,s35,2048\n"
E_"v109,v109\n"
M_"v[72:75],a[228:231],v[24:27],v[72:75]\n"
L_"v224,s[16:19],s94 offen lds\n"
M_"v[72:75],a[232:235],v[40:43],v[72:75]\n"
A_"s94,s94,32\n"
E_"v110,v110\n"
M_"v[72:75],a[236:239],v[56:59],v[72:75]\n"
"s_sub_u32 s95,s94,288\n"
E_"v111,v111\n"
M_"v[76:79],a[236:239],v[60:63],v[172:175]\n"
"s_min_u32 s94,s94,s95\n"
E_"v112,v112\n"
M_"v[76:79],a[232:235],v[44:47],v[76:79]\n"
E_"v113,v113\n"
M_"v[76:79],a[228:231],v[28:31],v[76:79]\n"
E_"v114,v114\n"
M_"v[76:79],a[224:227],v[12:15],v[76:79]\n"
E_"v115,v115\n"
M_"v[80:83],a[240:243],v[0:3],v[160:163]\n"
E_"v116,v116\n"
M_"v[80:83],a[244:247],v[16:19],v[80:83]\n"
E_"v117,v117\n"
M_"v[80:83],a[248:251],v[32:35],v[80:83]\n"
E_"v118,v118\n"
M_"v[80:83],a[252:255],v[48:51],v[80:83]\n"
"ds_read_b32 v241,v240 offset:0\n"
C_"v196,v100,v101\n"
M_"v[84:87],a[252:255],v[52:55],v[164:167]\n"
E_"v119,v119\n"
M_"v[84:87],a[248:251],v[36:39],v[84:87]\n"
C_"v197,v102,v103\n"
C_"v200,v104,v105\n"
M_"v[84:87],a[244:247],v[20:23],v[84:87]\n"
C_"v201,v106,v107\n"
C_"v204,v108,v109\n"
M_"v[84:87],a[240:243],v[4:7],v[84:87]\n"
E_"v120,v120\n"
M_"v[88:91],a[240:243],v[8:11],v[168:171]\n"
C_"v205,v110,v111\n"
C_"v194,v112,v113\n"
M_"v[88:91],a[244:247],v[24:27],v[88:91]\n"
E_"v121,v121\n"
M_"v[88:91],a[248:251],v[40:43],v[88:91]\n"
W_"0)\n"
"v_cmp_lt_u32 vcc,v241,v226\n"
C_"v195,v114,v115\n"
M_"v[88:91],a[252:255],v[56:59],v[88:91]\n"
E_"v122,v122\n"
M_"v[92:95],a[252:255],v[60:63],v[172:175]\n"
"s_cbranch_vccnz SPINBD3_%=\n"
"BACKBD3_%=:\n"
A_"m0,s61,98304\n"
M_"v[92:95],a[248:251],v[44:47],v[92:95]\n"
A_"s62,s84,4096\n"
T_"a[128:129],v154 offset:16384\n"
C_"v198,v116,v117\n"
M_"v[92:95],a[244:247],v[28:31],v[92:95]\n"
"s_sub_u32 s95,s96,4\n"
E_"v123,v123\n"
M_"v[92:95],a[240:243],v[12:15],v[92:95]\n"
"s_lshl_b32 s97,s95,10\n"
T_"a[130:131],v154 offset:20480\n"
C_"v199,v118,v119\n"
M_"v[96:99],a[192:195],v[0:3],v[160:163]\n"
A_"s97,s97,s76\n"
L_"v251,s[40:43],s84 offen lds\n"
M_"v[96:99],a[196:199],v[16:19],v[96:99]\n"
A_"m0,s61,102400\n"
T_"a[132:133],v154 offset:24576\n"
C_"v202,v120,v121\n"
M_"v[96:99],a[200:203],v[32:35],v[96:99]\n"
A_"s63,s85,128\n"
E_"v124,v124\n"
M_"v[96:99],a[204:207],v[48:51],v[96:99]\n"
T_"a[134:135],v154 offset:28672\n"
C_"v203,v122,v123\n"
M_"v[100:103],a[204:207],v[52:55],v[164:167]\n"
E_"v125,v125\n"
M_"v[100:103],a[200:203],v[36:39],v[100:103]\n"
T_"a[136:137],v155 offset:16384\n"
C_"v206,v124,v125\n"
M_"v[100:103],a[196:199],v[20:23],v[100:103]\n"
E_"v126,v126\n"
M_"v[100:103],a[192:195],v[4:7],v[100:103]\n"
T_"a[138:139],v155 offset:20480\n"
M_"v[104:107],a[192:195],v[8:11],v[168:171]\n"
E_"v127,v127\n"
M_"v[104:107],a[196:199],v[24:27],v[104:107]\n"
T_"a[140:141],v155 offset:24576\n"
C_"v207,v126,v127\n"
M_"v[104:107],a[200:203],v[40:43],v[104:107]\n"
E_"v64,v64\n"
M_"v[104:107],a[204:207],v[56:59],v[104:107]\n"
T_"a[142:143],v155 offset:28672\n"
M_"v[108:111],a[204:207],v[60:63],v[172:175]\n"
E_"v65,v65\n"
M_"v[108:111],a[200:203],v[44:47],v[108:111]\n"
T_"a[144:145],v154 offset:16896\n"
C_"v176,v64,v65\n"
M_"v[108:111],a[196:199],v[28:31],v[108:111]\n"
E_"v66,v66\n"
M_"v[108:111],a[192:195],v[12:15],v[108:111]\n"
T_"a[146:147],v154 offset:20992\n"
M_"v[112:115],a[208:211],v[0:3],v[160:163]\n"
L_"v251,s[40:43],s62 offen lds\n"
M_"v[112:115],a[212:215],v[16:19],v[112:115]\n"
A_"m0,s61,106496\n"
T_"a[148:149],v154 offset:25088\n"
M_"v[112:115],a[216:219],v[32:35],v[112:115]\n"
A_"s62,s84,8192\n"
E_"v67,v67\n"
M_"v[112:115],a[220:223],v[48:51],v[112:115]\n"
T_"a[150:151],v154 offset:29184\n"
C_"v177,v66,v67\n"
M_"v[116:119],a[220:223],v[52:55],v[164:167]\n"
E_"v68,v68\n"
M_"v[116:119],a[216:219],v[36:39],v[116:119]\n"
T_"a[152:153],v155 offset:16896\n"
M_"v[116:119],a[212:215],v[20:23],v[116:119]\n"
E_"v69,v69\n"
M_"v[116:119],a[208:211],v[4:7],v[116:119]\n"
T_"a[154:155],v155 offset:20992\n"
C_"v180,v68,v69\n"
M_"v[120:123],a[208:211],v[8:11],v[168:171]\n"
E_"v70,v70\n"
M_"v[120:123],a[212:215],v[24:27],v[120:123]\n"
T_"a[156:157],v155 offset:25088\n"
M_"v[120:123],a[216:219],v[40:43],v[120:123]\n"
E_"v71,v71\n"
M_"v[120:123],a[220:223],v[56:59],v[120:123]\n"
T_"a[158:159],v155 offset:29184\n"
C_"v181,v70,v71\n"
M_"v[124:127],a[220:223],v[60:63],v[172:175]\n"
E_"v72,v72\n"
M_"v[124:127],a[216:219],v[44:47],v[124:127]\n"
B_"a[224:227],v144 offset:0\n"
M_"v[124:127],a[212:215],v[28:31],v[124:127]\n"
E_"v73,v73\n"
M_"v[124:127],a[208:211],v[12:15],v[124:127]\n"
B_"a[228:231],v145 offset:0\n"
C_"v184,v72,v73\n"
M_"a[160:163],a[176:179],v[128:131],a[160:163]\n"
L_"v251,s[40:43],s62 offen lds\n"
M_"a[160:163],a[176:179],v[192:195],a[160:163]\n"
A_"m0,s61,110592\n"
B_"a[232:235],v146 offset:0\n"
M_"a[164:167],a[176:179],v[132:135],a[164:167]\n"
A_"s62,s84,12288\n"
E_"v74,v74\n"
M_"a[164:167],a[176:179],v[196:199],a[164:167]\n"
A_"s84,s84,0x4000\n"
B_"a[236:239],v147 offset:0\n"
M_"a[168:171],a[176:179],v[136:139],a[168:171]\n"
E_"v75,v75\n"
M_"a[168:171],a[176:179],v[200:203],a[168:171]\n"
T_"v[232:233],v225 offset:3072\n"
C_"v185,v74,v75\n"
M_"a[172:175],a[176:179],v[140:143],a[172:175]\n"
E_"v76,v76\n"
M_"a[172:175],a[176:179],v[204:207],a[172:175]\n"
W_"15)\n"
T_"v[234:235],v225 offset:3584\n"
M_"a[0:3],a[128:131],v[128:131],a[0:3]\n"
E_"v77,v77\n"
M_"a[0:3],a[132:135],v[192:195],a[0:3]\n"
B_"a[240:243],v144 offset:4096\n"
C_"v188,v76,v77\n"
M_"a[4:7],a[132:135],v[196:199],a[4:7]\n"
E_"v78,v78\n"
M_"a[4:7],a[128:131],v[132:135],a[4:7]\n"
B_"a[244:247],v145 offset:4096\n"
M_"a[8:11],a[128:131],v[136:139],a[8:11]\n"
E_"v79,v79\n"
M_"a[8:11],a[132:135],v[200:203],a[8:11]\n"
B_"a[248:251],v146 offset:4096\n"
C_"v189,v78,v79\n"
M_"a[12:15],a[132:135],v[204:207],a[12:15]\n"
E_"v80,v80\n"
M_"a[12:15],a[128:131],v[140:143],a[12:15]\n"
T_"a[128:129],v154 offset:17408\n"
M_"a[16:19],a[136:139],v[128:131],a[16:19]\n"
W_"15)\n"
L_"v251,s[40:43],s62 offen lds\n"
M_"a[16:19],a[140:143],v[192:195],a[16:19]\n"
A_"m0,s60,49152\n"
T_"a[130:131],v154 offset:21504\n"
M_"a[20:23],a[140:143],v[196:199],a[20:23]\n"
E_"v81,v81\n"
M_"a[20:23],a[136:139],v[132:135],a[20:23]\n"
T_"a[132:133],v154 offset:25600\n"
C_"v178,v80,v81\n"
M_"a[24:27],a[136:139],v[136:139],a[24:27]\n"
"s_cmp_eq_u32 s95,9\n"
"s_cselect_b32 s97,s77,s97\n"
M_"a[24:27],a[140:143],v[200:203],a[24:27]\n"
"s_cmp_gt_u32 s95,9\n"
"s_cselect_b32 s97,1073741824,s97\n"
T_"a[134:135],v154 offset:29696\n"
M_"a[28:31],a[140:143],v[204:207],a[28:31]\n"
E_"v82,v82\n"
M_"a[28:31],a[136:139],v[140:143],a[28:31]\n"
T_"a[136:137],v155 offset:17408\n"
M_"a[32:35],a[144:147],v[128:131],a[32:35]\n"
W_"15)\n"
E_"v83,v83\n"
M_"a[32:35],a[148:151],v[192:195],a[32:35]\n"
T_"a[138:139],v155 offset:21504\n"
C_"v179,v82,v83\n"
M_"a[36:39],a[148:151],v[196:199],a[36:39]\n"
W_"9)\n"
O_"v[232:235],v255,s[24:27],s97 offen sc1\n"
M_"a[36:39],a[144:147],v[132:135],a[36:39]\n"
T_"a[140:141],v155 offset:25600\n"
M_"a[40:43],a[144:147],v[136:139],a[40:43]\n"
E_"v84,v84\n"
M_"a[40:43],a[148:151],v[200:203],a[40:43]\n"
T_"a[142:143],v155 offset:29696\n"
M_"a[44:47],a[148:151],v[204:207],a[44:47]\n"
E_"v85,v85\n"
M_"a[44:47],a[144:147],v[140:143],a[44:47]\n"
T_"a[144:145],v154 offset:17920\n"
C_"v182,v84,v85\n"
M_"a[48:51],a[152:155],v[128:131],a[48:51]\n"
L_"v252,s[44:47],s85 offen lds\n"
M_"a[48:51],a[156:159],v[192:195],a[48:51]\n"
T_"a[146:147],v154 offset:22016\n"
M_"a[52:55],a[156:159],v[196:199],a[52:55]\n"
E_"v86,v86\n"
M_"a[52:55],a[152:155],v[132:135],a[52:55]\n"
T_"a[148:149],v154 offset:26112\n"
M_"a[56:59],a[152:155],v[136:139],a[56:59]\n"
"s_cmp_eq_u32 s96,9\n"
"s_cselect_b32 s18,s22,s18\n"
M_"a[56:59],a[156:159],v[200:203],a[56:59]\n"
"s_cmp_eq_u32 s96,9\n"
"s_cselect_b64 s[16:17],s[20:21],s[16:17]\n"
T_"a[150:151],v154 offset:30208\n"
M_"a[60:63],a[156:159],v[204:207],a[60:63]\n"
"s_cmp_eq_u32 s96,9\n"
"s_cselect_b32 s94,0,s94\n"
M_"a[60:63],a[152:155],v[140:143],a[60:63]\n"
W_"10)\n"
T_"a[152:153],v155 offset:17920\n"
M_"a[64:67],a[128:131],v[128:131],a[64:67]\n"
W_"9)\n"
"s_cmp_gt_u32 s96,9\n"
"s_cselect_b32 s18,0,s18\n"
M_"a[64:67],a[132:135],v[192:195],a[64:67]\n"
A_"s96,s96,1\n"
T_"a[154:155],v155 offset:22016\n"
M_"a[68:71],a[132:135],v[196:199],a[68:71]\n"
E_"v87,v87\n"
M_"a[68:71],a[128:131],v[132:135],a[68:71]\n"
T_"a[156:157],v155 offset:26112\n"
C_"v183,v86,v87\n"
M_"a[72:75],a[128:131],v[136:139],a[72:75]\n"
E_"v88,v88\n"
M_"a[72:75],a[132:135],v[200:203],a[72:75]\n"
T_"a[158:159],v155 offset:30208\n"
M_"a[76:79],a[132:135],v[204:207],a[76:79]\n"
E_"v89,v89\n"
M_"a[76:79],a[128:131],v[140:143],a[76:79]\n"
W_"10)\n"
B_"a[252:255],v147 offset:4096\n"
C_"v186,v88,v89\n"
M_"a[80:83],a[136:139],v[128:131],a[80:83]\n"
W_"9)\n"
L_"v252,s[44:47],s85 offen offset:2048 lds\n"
M_"a[80:83],a[140:143],v[192:195],a[80:83]\n"
A_"m0,s60,50176\n"
B_"a[192:195],v144 offset:8192\n"
M_"a[84:87],a[140:143],v[196:199],a[84:87]\n"
A_"s85,s85,0x4000\n"
E_"v90,v90\n"
M_"a[84:87],a[136:139],v[132:135],a[84:87]\n"
B_"a[196:199],v145 offset:8192\n"
M_"a[88:91],a[136:139],v[136:139],a[88:91]\n"
E_"v91,v91\n"
M_"a[88:91],a[140:143],v[200:203],a[88:91]\n"
B_"a[200:203],v146 offset:8192\n"
C_"v187,v90,v91\n"
M_"a[92:95],a[140:143],v[204:207],a[92:95]\n"
E_"v92,v92\n"
M_"a[92:95],a[136:139],v[140:143],a[92:95]\n"
W_"10)\n"
B_"a[204:207],v147 offset:8192\n"
M_"a[96:99],a[144:147],v[128:131],a[96:99]\n"
W_"9)\n"
E_"v93,v93\n"
M_"a[96:99],a[148:151],v[192:195],a[96:99]\n"
B_"a[208:211],v144 offset:12288\n"
C_"v190,v92,v93\n"
M_"a[100:103],a[148:151],v[196:199],a[100:103]\n"
E_"v94,v94\n"
M_"a[100:103],a[144:147],v[132:135],a[100:103]\n"
B_"a[212:215],v145 offset:12288\n"
M_"a[104:107],a[144:147],v[136:139],a[104:107]\n"
E_"v95,v95\n"
M_"a[104:107],a[148:151],v[200:203],a[104:107]\n"
B_"a[216:219],v146 offset:12288\n"
C_"v191,v94,v95\n"
M_"a[108:111],a[148:151],v[204:207],a[108:111]\n"
E_"v96,v96\n"
M_"a[108:111],a[144:147],v[140:143],a[108:111]\n"
W_"10)\n"
B_"a[220:223],v147 offset:12288\n"
M_"a[112:115],a[152:155],v[128:131],a[112:115]\n"
W_"9)\n"
L_"v252,s[44:47],s63 offen lds\n"
M_"a[112:115],a[156:159],v[192:195],a[112:115]\n"
E_"v97,v97\n"
M_"a[116:119],a[156:159],v[196:199],a[116:119]\n"
E_"v98,v98\n"
M_"a[116:119],a[152:155],v[132:135],a[116:119]\n"
E_"v99,v99\n"
M_"a[120:123],a[152:155],v[136:139],a[120:123]\n"
E_"v100,v100\n"
M_"a[120:123],a[156:159],v[200:203],a[120:123]\n"
E_"v101,v101\n"
M_"a[124:127],a[156:159],v[204:207],a[124:127]\n"
E_"v102,v102\n"
M_"a[124:127],a[152:155],v[140:143],a[124:127]\n"
"s_sub_u32 s86,s86,1\n"
"s_cbranch_scc0 LOOP_%=\n"
M_"v[64:67],a[224:227],v[0:3],v[160:163]\n"
"v_add_u32 v226,1,v226\n"
C_"v208,v96,v97\n"
M_"v[64:67],a[228:231],v[16:19],v[64:67]\n"
E_"v103,v103\n"
M_"v[64:67],a[232:235],v[32:35],v[64:67]\n"
E_"v104,v104\n"
M_"v[64:67],a[236:239],v[48:51],v[64:67]\n"
E_"v105,v105\n"
M_"v[68:71],a[236:239],v[52:55],v[164:167]\n"
E_"v106,v106\n"
M_"v[68:71],a[232:235],v[36:39],v[68:71]\n"
E_"v107,v107\n"
M_"v[68:71],a[228:231],v[20:23],v[68:71]\n"
"s_waitcnt vmcnt(19) lgkmcnt(0)\n"
"ds_write_b32 v227,v226\n"
C_"v209,v98,v99\n"
M_"v[68:71],a[224:227],v[4:7],v[68:71]\n"
L_"v252,s[44:47],s63 offen offset:2048 lds\n"
M_"v[72:75],a[224:227],v[8:11],v[168:171]\n"
A_"m0,s35,3072\n"
E_"v108,v108\n"
M_"v[72:75],a[228:231],v[24:27],v[72:75]\n"
L_"v224,s[16:19],s94 offen lds\n"
M_"v[72:75],a[232:235],v[40:43],v[72:75]\n"
A_"s94,s94,32\n"
E_"v109,v109\n"
M_"v[72:75],a[236:239],v[56:59],v[72:75]\n"
"s_sub_u32 s95,s94,288\n"
E_"v110,v110\n"
M_"v[76:79],a[236:239],v[60:63],v[172:175]\n"
"s_min_u32 s94,s94,s95\n"
E_"v111,v111\n"
M_"v[76:79],a[232:235],v[44:47],v[76:79]\n"
E_"v112,v112\n"
M_"v[76:79],a[228:231],v[28:31],v[76:79]\n"
E_"v113,v113\n"
M_"v[76:79],a[224:227],v[12:15],v[76:79]\n"
E_"v114,v114\n"
M_"v[80:83],a[240:243],v[0:3],v[160:163]\n"
E_"v115,v115\n"
M_"v[80:83],a[244:247],v[16:19],v[80:83]\n"
E_"v116,v116\n"
M_"v[80:83],a[248:251],v[32:35],v[80:83]\n"
E_"v117,v117\n"
M_"v[80:83],a[252:255],v[48:51],v[80:83]\n"
"ds_read_b32 v241,v240 offset:0\n"
C_"v212,v100,v101\n"
M_"v[84:87],a[252:255],v[52:55],v[164:167]\n"
E_"v118,v118\n"
M_"v[84:87],a[248:251],v[36:39],v[84:87]\n"
E_"v119,v119\n"
M_"v[84:87],a[244:247],v[20:23],v[84:87]\n"
E_"v64,v64\n"
M_"v[84:87],a[240:243],v[4:7],v[84:87]\n"
E_"v120,v120\n"
M_"v[88:91],a[240:243],v[8:11],v[168:171]\n"
E_"v65,v65\n"
M_"v[88:91],a[244:247],v[24:27],v[88:91]\n"
C_"v213,v102,v103\n"
C_"v216,v104,v105\n"
M_"v[88:91],a[248:251],v[40:43],v[88:91]\n"
W_"0)\n"
"v_cmp_lt_u32 vcc,v241,v226\n"
C_"v217,v106,v107\n"
M_"v[88:91],a[252:255],v[56:59],v[88:91]\n"
E_"v121,v121\n"
M_"v[92:95],a[252:255],v[60:63],v[172:175]\n"
"s_cbranch_vccnz SPINBI0_%=\n"
"BACKBI0_%=:\n"
A_"m0,s60,81920\n"
M_"v[92:95],a[248:251],v[44:47],v[92:95]\n"
A_"s63,s85,128\n"
T_"a[128:129],v236 offset:0\n"
C_"v220,v108,v109\n"
M_"v[92:95],a[244:247],v[28:31],v[92:95]\n"
"s_lshr_b32 s88,%[bid],5\n"
E_"v122,v122\n"
M_"v[92:95],a[240:243],v[12:15],v[92:95]\n"
"s_lshl_b32 s88,s88,2\n"
T_"a[130:131],v236 offset:4096\n"
C_"v221,v110,v111\n"
M_"v[96:99],a[192:195],v[0:3],v[160:163]\n"
A_"s88,s88,s33\n"
L_"v252,s[44:47],s85 offen lds\n"
M_"v[96:99],a[196:199],v[16:19],v[96:99]\n"
"s_and_b32 s89,%[bid],7\n"
T_"a[132:133],v236 offset:8192\n"
C_"v210,v112,v113\n"
M_"v[96:99],a[200:203],v[32:35],v[96:99]\n"
"s_lshl_b32 s88,s88,19\n"
E_"v123,v123\n"
M_"v[96:99],a[204:207],v[48:51],v[96:99]\n"
"s_lshl_b32 s89,s89,14\n"
T_"a[134:135],v236 offset:12288\n"
C_"v211,v114,v115\n"
M_"v[100:103],a[204:207],v[52:55],v[164:167]\n"
A_"s88,s88,s89\n"
E_"v124,v124\n"
M_"v[100:103],a[200:203],v[36:39],v[100:103]\n"
"s_bfe_u32 s89,%[bid],0x20003\n"
T_"a[136:137],v237 offset:0\n"
C_"v214,v116,v117\n"
M_"v[100:103],a[196:199],v[20:23],v[100:103]\n"
"s_lshl_b32 s89,s89,12\n"
E_"v125,v125\n"
M_"v[100:103],a[192:195],v[4:7],v[100:103]\n"
A_"s88,s88,s89\n"
T_"a[138:139],v237 offset:4096\n"
C_"v215,v118,v119\n"
M_"v[104:107],a[192:195],v[8:11],v[168:171]\n"
A_"s88,s88,25165824\n"
E_"v126,v126\n"
M_"v[104:107],a[196:199],v[24:27],v[104:107]\n"
"s_and_b32 s76,%[bid],1\n"
T_"a[140:141],v237 offset:8192\n"
C_"v218,v120,v121\n"
M_"v[104:107],a[200:203],v[40:43],v[104:107]\n"
"s_bfe_u32 s70,%[bid],0x20001\n"
E_"v127,v127\n"
M_"v[104:107],a[204:207],v[56:59],v[104:107]\n"
"s_mul_i32 s34,s70,1296\n"
T_"a[142:143],v237 offset:12288\n"
C_"v219,v122,v123\n"
M_"v[108:111],a[204:207],v[60:63],v[172:175]\n"
"s_bfe_u32 s77,%[bid],0x20003\n"
E_"v66,v66\n"
M_"v[108:111],a[200:203],v[44:47],v[108:111]\n"
"s_mul_i32 s38,s70,36864\n"
T_"a[144:145],v236 offset:512\n"
C_"v222,v124,v125\n"
M_"v[108:111],a[196:199],v[28:31],v[108:111]\n"
"s_mul_i32 s35,s33,9216\n"
E_"v67,v67\n"
M_"v[108:111],a[192:195],v[12:15],v[108:111]\n"
S_"s50,73728\n"
T_"a[146:147],v236 offset:4608\n"
C_"v223,v126,v127\n"
M_"v[112:115],a[208:211],v[0:3],v[160:163]\n"
S_"s51,0x20000\n"
L_"v252,s[44:47],s85 offen offset:2048 lds\n"
M_"v[112:115],a[212:215],v[16:19],v[112:115]\n"
A_"m0,s60,82944\n"
T_"a[148:149],v236 offset:8704\n"
"v_lshlrev_b32 v224,1,v255\n"
M_"v[112:115],a[216:219],v[32:35],v[112:115]\n"
A_"s85,s85,0x4000\n"
E_"v68,v68\n"
M_"v[112:115],a[220:223],v[48:51],v[112:115]\n"
S_"s39,0x20000\n"
T_"a[150:151],v236 offset:12800\n"
C_"v128,v64,v65\n"
M_"v[116:119],a[220:223],v[52:55],v[164:167]\n"
S_"s54,397312\n"
E_"v69,v69\n"
M_"v[116:119],a[216:219],v[36:39],v[116:119]\n"
S_"s55,0x20000\n"
T_"a[152:153],v237 offset:512\n"
C_"v129,v66,v67\n"
M_"v[116:119],a[212:215],v[20:23],v[116:119]\n"
S_"s30,163840\n"
E_"v70,v70\n"
M_"v[116:119],a[208:211],v[4:7],v[116:119]\n"
S_"s31,0x20000\n"
T_"a[154:155],v237 offset:4608\n"
C_"v132,v68,v69\n"
M_"v[120:123],a[208:211],v[8:11],v[168:171]\n"
E_"v71,v71\n"
M_"v[120:123],a[212:215],v[24:27],v[120:123]\n"
T_"a[156:157],v237 offset:8704\n"
C_"v133,v70,v71\n"
M_"v[120:123],a[216:219],v[40:43],v[120:123]\n"
E_"v72,v72\n"
M_"v[120:123],a[220:223],v[56:59],v[120:123]\n"
T_"a[158:159],v237 offset:12800\n"
"v_lshrrev_b32 v250,1,v255\n"
M_"v[124:127],a[220:223],v[60:63],v[172:175]\n"
E_"v73,v73\n"
M_"v[124:127],a[216:219],v[44:47],v[124:127]\n"
B_"a[224:227],v144 offset:32768\n"
C_"v136,v72,v73\n"
M_"v[124:127],a[212:215],v[28:31],v[124:127]\n"
E_"v74,v74\n"
M_"v[124:127],a[208:211],v[12:15],v[124:127]\n"
B_"a[228:231],v145 offset:32768\n"
"v_add_u32 v249,0x10000,v255\n"
M_"a[160:163],a[176:179],v[176:179],a[160:163]\n"
L_"v252,s[44:47],s63 offen lds\n"
M_"a[160:163],a[176:179],v[208:211],a[160:163]\n"
B_"a[232:235],v146 offset:32768\n"
"v_add_u32 v242,0x20000,v255\n"
M_"a[164:167],a[176:179],v[180:183],a[164:167]\n"
E_"v75,v75\n"
M_"a[164:167],a[176:179],v[212:215],a[164:167]\n"
B_"a[236:239],v147 offset:32768\n"
C_"v137,v74,v75\n"
M_"a[168:171],a[176:179],v[184:187],a[168:171]\n"
E_"v76,v76\n"
M_"a[168:171],a[176:179],v[216:219],a[168:171]\n"
B_"a[240:243],v144 offset:36864\n"
"v_bfe_u32 v248,v255,4,4\n"
M_"a[172:175],a[176:179],v[188:191],a[172:175]\n"
E_"v77,v77\n"
M_"a[172:175],a[176:179],v[220:223],a[172:175]\n"
W_"15)\n"
B_"a[244:247],v145 offset:36864\n"
C_"v140,v76,v77\n"
M_"a[0:3],a[128:131],v[176:179],a[0:3]\n"
E_"v78,v78\n"
M_"a[0:3],a[132:135],v[208:211],a[0:3]\n"
B_"a[248:251],v146 offset:36864\n"
"v_mul_u32_u24 v248,0x480,v248\n"
M_"a[4:7],a[132:135],v[212:215],a[4:7]\n"
E_"v79,v79\n"
M_"a[4:7],a[128:131],v[180:183],a[4:7]\n"
B_"a[252:255],v147 offset:36864\n"
C_"v141,v78,v79\n"
M_"a[8:11],a[128:131],v[184:187],a[8:11]\n"
E_"v80,v80\n"
M_"a[8:11],a[132:135],v[216:219],a[8:11]\n"
B_"a[192:195],v144 offset:40960\n"
"v_lshrrev_b32 v247,8,v255\n"
M_"a[12:15],a[132:135],v[220:223],a[12:15]\n"
E_"v81,v81\n"
M_"a[12:15],a[128:131],v[188:191],a[12:15]\n"
T_"a[128:129],v236 offset:1024\n"
C_"v130,v80,v81\n"
M_"a[16:19],a[136:139],v[176:179],a[16:19]\n"
W_"15)\n"
L_"v252,s[44:47],s63 offen offset:2048 lds\n"
M_"a[16:19],a[140:143],v[208:211],a[16:19]\n"
S_"s63,8192\n"
T_"a[130:131],v236 offset:5120\n"
"v_lshl_add_u32 v248,v247,3,v248\n"
M_"a[20:23],a[140:143],v[212:215],a[20:23]\n"
L_"v[228:231],v224,s[56:59],s63 offen sc1\n"
M_"a[20:23],a[136:139],v[180:183],a[20:23]\n"
T_"a[132:133],v236 offset:9216\n"
"v_add_u32 v247,18432,v248\n"
M_"a[24:27],a[136:139],v[184:187],a[24:27]\n"
L_"v[232:235],v224,s[56:59],s63 offen offset:16 sc1\n"
M_"a[24:27],a[140:143],v[216:219],a[24:27]\n"
T_"a[134:135],v236 offset:13312\n"
M_"a[28:31],a[140:143],v[220:223],a[28:31]\n"
E_"v82,v82\n"
M_"a[28:31],a[136:139],v[188:191],a[28:31]\n"
T_"a[136:137],v237 offset:1024\n"
M_"a[32:35],a[144:147],v[176:179],a[32:35]\n"
W_"15)\n"
E_"v83,v83\n"
M_"a[32:35],a[148:151],v[208:211],a[32:35]\n"
T_"a[138:139],v237 offset:5120\n"
C_"v131,v82,v83\n"
M_"a[36:39],a[148:151],v[212:215],a[36:39]\n"
E_"v84,v84\n"
M_"a[36:39],a[144:147],v[180:183],a[36:39]\n"
T_"a[140:141],v237 offset:9216\n"
M_"a[40:43],a[144:147],v[184:187],a[40:43]\n"
E_"v85,v85\n"
M_"a[40:43],a[148:151],v[216:219],a[40:43]\n"
T_"a[142:143],v237 offset:13312\n"
C_"v134,v84,v85\n"
M_"a[44:47],a[148:151],v[220:223],a[44:47]\n"
E_"v86,v86\n"
M_"a[44:47],a[144:147],v[188:191],a[44:47]\n"
T_"a[144:145],v236 offset:1536\n"
M_"a[48:51],a[152:155],v[176:179],a[48:51]\n"
W_"15)\n"
E_"v87,v87\n"
M_"a[48:51],a[156:159],v[208:211],a[48:51]\n"
T_"a[146:147],v236 offset:5632\n"
C_"v135,v86,v87\n"
M_"a[52:55],a[156:159],v[212:215],a[52:55]\n"
E_"v88,v88\n"
M_"a[52:55],a[152:155],v[180:183],a[52:55]\n"
T_"a[148:149],v236 offset:9728\n"
M_"a[56:59],a[152:155],v[184:187],a[56:59]\n"
E_"v89,v89\n"
M_"a[56:59],a[156:159],v[216:219],a[56:59]\n"
T_"a[150:151],v236 offset:13824\n"
C_"v138,v88,v89\n"
M_"a[60:63],a[156:159],v[220:223],a[60:63]\n"
E_"v90,v90\n"
M_"a[60:63],a[152:155],v[188:191],a[60:63]\n"
W_"10)\n"
T_"a[152:153],v237 offset:1536\n"
M_"a[64:67],a[128:131],v[176:179],a[64:67]\n"
W_"9)\n"
E_"v91,v91\n"
M_"a[64:67],a[132:135],v[208:211],a[64:67]\n"
T_"a[154:155],v237 offset:5632\n"
C_"v139,v90,v91\n"
M_"a[68:71],a[132:135],v[212:215],a[68:71]\n"
E_"v92,v92\n"
M_"a[68:71],a[128:131],v[180:183],a[68:71]\n"
T_"a[156:157],v237 offset:9728\n"
M_"a[72:75],a[128:131],v[184:187],a[72:75]\n"
E_"v93,v93\n"
M_"a[72:75],a[132:135],v[216:219],a[72:75]\n"
T_"a[158:159],v237 offset:13824\n"
C_"v142,v92,v93\n"
M_"a[76:79],a[132:135],v[220:223],a[76:79]\n"
E_"v94,v94\n"
M_"a[76:79],a[128:131],v[188:191],a[76:79]\n"
W_"10)\n"
B_"a[196:199],v145 offset:40960\n"
M_"a[80:83],a[136:139],v[176:179],a[80:83]\n"
W_"9)\n"
E_"v95,v95\n"
M_"a[80:83],a[140:143],v[208:211],a[80:83]\n"
B_"a[200:203],v146 offset:40960\n"
C_"v143,v94,v95\n"
M_"a[84:87],a[140:143],v[212:215],a[84:87]\n"
E_"v96,v96\n"
M_"a[84:87],a[136:139],v[180:183],a[84:87]\n"
B_"a[204:207],v147 offset:40960\n"
M_"a[88:91],a[136:139],v[184:187],a[88:91]\n"
E_"v97,v97\n"
M_"a[88:91],a[140:143],v[216:219],a[88:91]\n"
B_"a[208:211],v144 offset:45056\n"
C_"v192,v96,v97\n"
M_"a[92:95],a[140:143],v[220:223],a[92:95]\n"
E_"v98,v98\n"
M_"a[92:95],a[136:139],v[188:191],a[92:95]\n"
W_"10)\n"
B_"a[212:215],v145 offset:45056\n"
M_"a[96:99],a[144:147],v[176:179],a[96:99]\n"
W_"9)\n"
E_"v99,v99\n"
M_"a[96:99],a[148:151],v[208:211],a[96:99]\n"
B_"a[216:219],v146 offset:45056\n"
C_"v193,v98,v99\n"
M_"a[100:103],a[148:151],v[212:215],a[100:103]\n"
E_"v100,v100\n"
M_"a[100:103],a[144:147],v[180:183],a[100:103]\n"
B_"a[220:223],v147 offset:45056\n"
M_"a[104:107],a[144:147],v[184:187],a[104:107]\n"
E_"v101,v101\n"
M_"a[104:107],a[148:151],v[216:219],a[104:107]\n"
E_"v102,v102\n"
M_"a[108:111],a[148:151],v[220:223],a[108:111]\n"
E_"v103,v103\n"
M_"a[108:111],a[144:147],v[188:191],a[108:111]\n"
W_"9)\n"
E_"v104,v104\n"
M_"a[112:115],a[152:155],v[176:179],a[112:115]\n"
W_"7)\n"
E_"v105,v105\n"
M_"a[112:115],a[156:159],v[208:211],a[112:115]\n"
E_"v106,v106\n"
M_"a[116:119],a[156:159],v[212:215],a[116:119]\n"
E_"v107,v107\n"
M_"a[116:119],a[152:155],v[180:183],a[116:119]\n"
E_"v108,v108\n"
M_"a[120:123],a[152:155],v[184:187],a[120:123]\n"
E_"v109,v109\n"
M_"a[120:123],a[156:159],v[216:219],a[120:123]\n"
E_"v110,v110\n"
M_"a[124:127],a[156:159],v[220:223],a[124:127]\n"
E_"v111,v111\n"
M_"a[124:127],a[152:155],v[188:191],a[124:127]\n"
"v_add_u32 v226,1,v226\n"
C_"v196,v100,v101\n"
M_"v[64:67],a[224:227],v[0:3],v[160:163]\n"
E_"v112,v112\n"
M_"v[64:67],a[228:231],v[16:19],v[64:67]\n"
E_"v113,v113\n"
M_"v[64:67],a[232:235],v[32:35],v[64:67]\n"
E_"v114,v114\n"
M_"v[64:67],a[236:239],v[48:51],v[64:67]\n"
A_"s52,s82,s88\n"
"s_addc_u32 s53,s83,0\n"
C_"v197,v102,v103\n"
M_"v[68:71],a[236:239],v[52:55],v[164:167]\n"
"s_lshr_b32 s88,%[bid],5\n"
E_"v115,v115\n"
M_"v[68:71],a[232:235],v[36:39],v[68:71]\n"
"s_lshl_b32 s88,s88,1\n"
E_"v116,v116\n"
M_"v[68:71],a[228:231],v[20:23],v[68:71]\n"
"s_waitcnt vmcnt(17) lgkmcnt(0)\n"
"ds_write_b32 v227,v226\n"
A_"s76,s76,s88\n"
M_"v[68:71],a[224:227],v[4:7],v[68:71]\n"
"s_mul_i32 s88,s33,36\n"
E_"v117,v117\n"
M_"v[72:75],a[224:227],v[8:11],v[168:171]\n"
A_"s34,s34,s88\n"
E_"v118,v118\n"
M_"v[72:75],a[228:231],v[24:27],v[72:75]\n"
"s_mul_i32 s88,s77,9\n"
"s_lshl_b32 s89,s76,2\n"
C_"v200,v104,v105\n"
M_"v[72:75],a[232:235],v[40:43],v[72:75]\n"
A_"s34,s34,s88\n"
A_"s89,s89,s33\n"
C_"v201,v106,v107\n"
M_"v[72:75],a[236:239],v[56:59],v[72:75]\n"
"s_mul_i32 s88,s89,294912\n"
E_"v119,v119\n"
M_"v[76:79],a[236:239],v[60:63],v[172:175]\n"
A_"s88,s88,s38\n"
E_"v120,v120\n"
M_"v[76:79],a[232:235],v[44:47],v[76:79]\n"
A_"s88,s88,4194304\n"
E_"v121,v121\n"
M_"v[76:79],a[228:231],v[28:31],v[76:79]\n"
A_"s36,s82,s88\n"
"s_addc_u32 s37,s83,0\n"
C_"v204,v108,v109\n"
M_"v[76:79],a[224:227],v[12:15],v[76:79]\n"
"s_lshl_b32 s88,s89,18\n"
E_"v122,v122\n"
M_"v[80:83],a[240:243],v[0:3],v[160:163]\n"
"s_lshl_b32 s89,s70,15\n"
E_"v123,v123\n"
M_"v[80:83],a[244:247],v[16:19],v[80:83]\n"
A_"s88,s88,s89\n"
E_"v124,v124\n"
M_"v[80:83],a[248:251],v[32:35],v[80:83]\n"
"s_lshl_b32 s71,s76,2\n"
A_"s88,s88,25165824\n"
C_"v205,v110,v111\n"
M_"v[80:83],a[252:255],v[48:51],v[80:83]\n"
A_"s71,s71,s77\n"
"ds_read_b32 v241,v240 offset:0\n"
C_"v194,v112,v113\n"
M_"v[84:87],a[252:255],v[52:55],v[164:167]\n"
A_"s28,s82,s88\n"
"s_addc_u32 s29,s83,0\n"
C_"v195,v114,v115\n"
M_"v[84:87],a[248:251],v[36:39],v[84:87]\n"
"s_lshl_b32 s88,s71,2\n"
E_"v125,v125\n"
M_"v[84:87],a[244:247],v[20:23],v[84:87]\n"
A_"s88,s88,s33\n"
E_"v126,v126\n"
M_"v[84:87],a[240:243],v[4:7],v[84:87]\n"
"s_mul_i32 s88,s88,73728\n"
E_"v127,v127\n"
M_"v[88:91],a[240:243],v[8:11],v[168:171]\n"
A_"s88,s88,54525952\n"
E_"v64,v64\n"
M_"v[88:91],a[244:247],v[24:27],v[88:91]\n"
A_"s48,s82,s88\n"
"s_addc_u32 s49,s83,0\n"
C_"v198,v116,v117\n"
M_"v[88:91],a[248:251],v[40:43],v[88:91]\n"
"s_lshl_b32 s34,s34,10\n"
W_"0)\n"
"v_cmp_lt_u32 vcc,v241,v226\n"
C_"v199,v118,v119\n"
M_"v[88:91],a[252:255],v[56:59],v[88:91]\n"
"s_and_b32 s49,s49,0xffff\n"
E_"v65,v65\n"
M_"v[92:95],a[252:255],v[60:63],v[172:175]\n"
"s_cbranch_vccnz SPINBI1_%=\n"
"BACKBI1_%=:\n"
"s_nop 0\n"
"s_waitcnt vmcnt(0)\n"
"v_cmp_eq_u64 vcc,s[66:67],v[228:229]\n"
"v_cmp_eq_u64 s[68:69],s[66:67],v[230:231]\n"
"s_nop 3\n"
"s_and_b64 vcc,vcc,s[68:69]\n"
"v_cmp_eq_u64 s[68:69],s[66:67],v[232:233]\n"
"s_nop 3\n"
"s_and_b64 vcc,vcc,s[68:69]\n"
"v_cmp_eq_u64 s[68:69],s[66:67],v[234:235]\n"
"s_nop 3\n"
"s_and_b64 vcc,vcc,s[68:69]\n"
"s_nop 3\n"
"s_cmp_eq_u64 vcc,-1\n"
"s_cbranch_scc0 SPINH_%=\n"
"BACKH_%=:\n"
M_"v[92:95],a[248:251],v[44:47],v[92:95]\n"
A_"m0,s60,147456\n"
T_"a[128:129],v152 offset:16384\n"
C_"v202,v120,v121\n"
M_"v[92:95],a[244:247],v[28:31],v[92:95]\n"
A_"s63,s85,128\n"
E_"v66,v66\n"
M_"v[92:95],a[240:243],v[12:15],v[92:95]\n"
"s_and_b32 s37,s37,0xffff\n"
T_"a[130:131],v152 offset:20480\n"
C_"v203,v122,v123\n"
M_"v[96:99],a[192:195],v[0:3],v[160:163]\n"
S_"s38,184320\n"
L_"v252,s[44:47],s85 offen lds\n"
M_"v[96:99],a[196:199],v[16:19],v[96:99]\n"
"s_and_b32 s53,s53,0xffff\n"
T_"a[132:133],v152 offset:24576\n"
C_"v206,v124,v125\n"
M_"v[96:99],a[200:203],v[32:35],v[96:99]\n"
"s_and_b32 s29,s29,0xffff\n"
E_"v67,v67\n"
M_"v[96:99],a[204:207],v[48:51],v[96:99]\n"
T_"a[134:135],v152 offset:28672\n"
C_"v207,v126,v127\n"
M_"v[100:103],a[204:207],v[52:55],v[164:167]\n"
E_"v68,v68\n"
M_"v[100:103],a[200:203],v[36:39],v[100:103]\n"
T_"a[136:137],v153 offset:16384\n"
C_"v176,v64,v65\n"
M_"v[100:103],a[196:199],v[20:23],v[100:103]\n"
E_"v69,v69\n"
M_"v[100:103],a[192:195],v[4:7],v[100:103]\n"
T_"a[138:139],v153 offset:20480\n"
C_"v177,v66,v67\n"
M_"v[104:107],a[192:195],v[8:11],v[168:171]\n"
E_"v70,v70\n"
M_"v[104:107],a[196:199],v[24:27],v[104:107]\n"
T_"a[140:141],v153 offset:24576\n"
C_"v180,v68,v69\n"
M_"v[104:107],a[200:203],v[40:43],v[104:107]\n"
E_"v71,v71\n"
M_"v[104:107],a[204:207],v[56:59],v[104:107]\n"
T_"a[142:143],v153 offset:28672\n"
C_"v181,v70,v71\n"
M_"v[108:111],a[204:207],v[60:63],v[172:175]\n"
E_"v72,v72\n"
M_"v[108:111],a[200:203],v[44:47],v[108:111]\n"
T_"a[144:145],v152 offset:16896\n"
M_"v[108:111],a[196:199],v[28:31],v[108:111]\n"
E_"v73,v73\n"
M_"v[108:111],a[192:195],v[12:15],v[108:111]\n"
T_"a[146:147],v152 offset:20992\n"
C_"v184,v72,v73\n"
M_"v[112:115],a[208:211],v[0:3],v[160:163]\n"
L_"v252,s[44:47],s85 offen offset:2048 lds\n"
M_"v[112:115],a[212:215],v[16:19],v[112:115]\n"
A_"m0,s60,148480\n"
T_"a[148:149],v152 offset:25088\n"
M_"v[112:115],a[216:219],v[32:35],v[112:115]\n"
A_"s85,s85,0x4000\n"
E_"v74,v74\n"
M_"v[112:115],a[220:223],v[48:51],v[112:115]\n"
T_"a[150:151],v152 offset:29184\n"
M_"v[116:119],a[220:223],v[52:55],v[164:167]\n"
E_"v75,v75\n"
M_"v[116:119],a[216:219],v[36:39],v[116:119]\n"
T_"a[152:153],v153 offset:16896\n"
C_"v185,v74,v75\n"
M_"v[116:119],a[212:215],v[20:23],v[116:119]\n"
E_"v76,v76\n"
M_"v[116:119],a[208:211],v[4:7],v[116:119]\n"
T_"a[154:155],v153 offset:20992\n"
M_"v[120:123],a[208:211],v[8:11],v[168:171]\n"
E_"v77,v77\n"
M_"v[120:123],a[212:215],v[24:27],v[120:123]\n"
T_"a[156:157],v153 offset:25088\n"
C_"v188,v76,v77\n"
M_"v[120:123],a[216:219],v[40:43],v[120:123]\n"
E_"v78,v78\n"
M_"v[120:123],a[220:223],v[56:59],v[120:123]\n"
T_"a[158:159],v153 offset:29184\n"
M_"v[124:127],a[220:223],v[60:63],v[172:175]\n"
E_"v79,v79\n"
M_"v[124:127],a[216:219],v[44:47],v[124:127]\n"
B_"a[224:227],v148 offset:0\n"
C_"v189,v78,v79\n"
M_"v[124:127],a[212:215],v[28:31],v[124:127]\n"
E_"v80,v80\n"
M_"v[124:127],a[208:211],v[12:15],v[124:127]\n"
B_"a[228:231],v149 offset:0\n"
M_"a[160:163],a[176:179],v[128:131],a[160:163]\n"
L_"v252,s[44:47],s63 offen lds\n"
M_"a[160:163],a[176:179],v[192:195],a[160:163]\n"
B_"a[232:235],v150 offset:0\n"
M_"a[164:167],a[176:179],v[132:135],a[164:167]\n"
E_"v81,v81\n"
M_"a[164:167],a[176:179],v[196:199],a[164:167]\n"
B_"a[236:239],v151 offset:0\n"
C_"v178,v80,v81\n"
M_"a[168:171],a[176:179],v[136:139],a[168:171]\n"
E_"v82,v82\n"
M_"a[168:171],a[176:179],v[200:203],a[168:171]\n"
B_"a[240:243],v148 offset:4096\n"
M_"a[172:175],a[176:179],v[140:143],a[172:175]\n"
E_"v83,v83\n"
M_"a[172:175],a[176:179],v[204:207],a[172:175]\n"
W_"15)\n"
B_"a[244:247],v149 offset:4096\n"
C_"v179,v82,v83\n"
M_"a[0:3],a[128:131],v[128:131],a[0:3]\n"
E_"v84,v84\n"
M_"a[0:3],a[132:135],v[192:195],a[0:3]\n"
B_"a[248:251],v150 offset:4096\n"
M_"a[4:7],a[132:135],v[196:199],a[4:7]\n"
E_"v85,v85\n"
M_"a[4:7],a[128:131],v[132:135],a[4:7]\n"
B_"a[252:255],v151 offset:4096\n"
C_"v182,v84,v85\n"
M_"a[8:11],a[128:131],v[136:139],a[8:11]\n"
E_"v86,v86\n"
M_"a[8:11],a[132:135],v[200:203],a[8:11]\n"
B_"a[192:195],v148 offset:8192\n"
M_"a[12:15],a[132:135],v[204:207],a[12:15]\n"
E_"v87,v87\n"
M_"a[12:15],a[128:131],v[140:143],a[12:15]\n"
T_"a[128:129],v152 offset:17408\n"
C_"v183,v86,v87\n"
M_"a[16:19],a[136:139],v[128:131],a[16:19]\n"
W_"15)\n"
L_"v252,s[44:47],s63 offen offset:2048 lds\n"
M_"a[16:19],a[140:143],v[192:195],a[16:19]\n"
T_"a[130:131],v152 offset:21504\n"
M_"a[20:23],a[140:143],v[196:199],a[20:23]\n"
E_"v88,v88\n"
M_"a[20:23],a[136:139],v[132:135],a[20:23]\n"
T_"a[132:133],v152 offset:25600\n"
M_"a[24:27],a[136:139],v[136:139],a[24:27]\n"
E_"v89,v89\n"
M_"a[24:27],a[140:143],v[200:203],a[24:27]\n"
T_"a[134:135],v152 offset:29696\n"
C_"v186,v88,v89\n"
M_"a[28:31],a[140:143],v[204:207],a[28:31]\n"
E_"v90,v90\n"
M_"a[28:31],a[136:139],v[140:143],a[28:31]\n"
T_"a[136:137],v153 offset:17408\n"
M_"a[32:35],a[144:147],v[128:131],a[32:35]\n"
W_"15)\n"
E_"v91,v91\n"
M_"a[32:35],a[148:151],v[192:195],a[32:35]\n"
T_"a[138:139],v153 offset:21504\n"
C_"v187,v90,v91\n"
M_"a[36:39],a[148:151],v[196:199],a[36:39]\n"
E_"v92,v92\n"
M_"a[36:39],a[144:147],v[132:135],a[36:39]\n"
T_"a[140:141],v153 offset:25600\n"
M_"a[40:43],a[144:147],v[136:139],a[40:43]\n"
E_"v93,v93\n"
M_"a[40:43],a[148:151],v[200:203],a[40:43]\n"
T_"a[142:143],v153 offset:29696\n"
C_"v190,v92,v93\n"
M_"a[44:47],a[148:151],v[204:207],a[44:47]\n"
E_"v94,v94\n"
M_"a[44:47],a[144:147],v[140:143],a[44:47]\n"
T_"a[144:145],v152 offset:17920\n"
M_"a[48:51],a[152:155],v[128:131],a[48:51]\n"
W_"15)\n"
E_"v95,v95\n"
M_"a[48:51],a[156:159],v[192:195],a[48:51]\n"
T_"a[146:147],v152 offset:22016\n"
C_"v191,v94,v95\n"
M_"a[52:55],a[156:159],v[196:199],a[52:55]\n"
E_"v96,v96\n"
M_"a[52:55],a[152:155],v[132:135],a[52:55]\n"
T_"a[148:149],v152 offset:26112\n"
M_"a[56:59],a[152:155],v[136:139],a[56:59]\n"
E_"v97,v97\n"
M_"a[56:59],a[156:159],v[200:203],a[56:59]\n"
T_"a[150:151],v152 offset:30208\n"
C_"v208,v96,v97\n"
M_"a[60:63],a[156:159],v[204:207],a[60:63]\n"
E_"v98,v98\n"
M_"a[60:63],a[152:155],v[140:143],a[60:63]\n"
W_"10)\n"
T_"a[152:153],v153 offset:17920\n"
M_"a[64:67],a[128:131],v[128:131],a[64:67]\n"
W_"9)\n"
E_"v99,v99\n"
M_"a[64:67],a[132:135],v[192:195],a[64:67]\n"
T_"a[154:155],v153 offset:22016\n"
C_"v209,v98,v99\n"
M_"a[68:71],a[132:135],v[196:199],a[68:71]\n"
E_"v100,v100\n"
M_"a[68:71],a[128:131],v[132:135],a[68:71]\n"
T_"a[156:157],v153 offset:26112\n"
M_"a[72:75],a[128:131],v[136:139],a[72:75]\n"
E_"v101,v101\n"
M_"a[72:75],a[132:135],v[200:203],a[72:75]\n"
T_"a[158:159],v153 offset:30208\n"
C_"v212,v100,v101\n"
M_"a[76:79],a[132:135],v[204:207],a[76:79]\n"
E_"v102,v102\n"
M_"a[76:79],a[128:131],v[140:143],a[76:79]\n"
W_"10)\n"
B_"a[196:199],v149 offset:8192\n"
M_"a[80:83],a[136:139],v[128:131],a[80:83]\n"
W_"9)\n"
E_"v103,v103\n"
M_"a[80:83],a[140:143],v[192:195],a[80:83]\n"
B_"a[200:203],v150 offset:8192\n"
C_"v213,v102,v103\n"
M_"a[84:87],a[140:143],v[196:199],a[84:87]\n"
E_"v104,v104\n"
M_"a[84:87],a[136:139],v[132:135],a[84:87]\n"
B_"a[204:207],v151 offset:8192\n"
M_"a[88:91],a[136:139],v[136:139],a[88:91]\n"
E_"v105,v105\n"
M_"a[88:91],a[140:143],v[200:203],a[88:91]\n"
B_"a[208:211],v148 offset:12288\n"
C_"v216,v104,v105\n"
M_"a[92:95],a[140:143],v[204:207],a[92:95]\n"
E_"v106,v106\n"
M_"a[92:95],a[136:139],v[140:143],a[92:95]\n"
W_"10)\n"
B_"a[212:215],v149 offset:12288\n"
M_"a[96:99],a[144:147],v[128:131],a[96:99]\n"
W_"9)\n"
E_"v107,v107\n"
M_"a[96:99],a[148:151],v[192:195],a[96:99]\n"
B_"a[216:219],v150 offset:12288\n"
C_"v217,v106,v107\n"
M_"a[100:103],a[148:151],v[196:199],a[100:103]\n"
E_"v108,v108\n"
M_"a[100:103],a[144:147],v[132:135],a[100:103]\n"
B_"a[220:223],v151 offset:12288\n"
M_"a[104:107],a[144:147],v[136:139],a[104:107]\n"
E_"v109,v109\n"
M_"a[104:107],a[148:151],v[200:203],a[104:107]\n"
E_"v110,v110\n"
M_"a[108:111],a[148:151],v[204:207],a[108:111]\n"
E_"v111,v111\n"
M_"a[108:111],a[144:147],v[140:143],a[108:111]\n"
W_"9)\n"
E_"v112,v112\n"
M_"a[112:115],a[152:155],v[128:131],a[112:115]\n"
W_"7)\n"
E_"v113,v113\n"
M_"a[112:115],a[156:159],v[192:195],a[112:115]\n"
E_"v114,v114\n"
M_"a[116:119],a[156:159],v[196:199],a[116:119]\n"
E_"v115,v115\n"
M_"a[116:119],a[152:155],v[132:135],a[116:119]\n"
E_"v116,v116\n"
M_"a[120:123],a[152:155],v[136:139],a[120:123]\n"
E_"v117,v117\n"
M_"a[120:123],a[156:159],v[200:203],a[120:123]\n"
E_"v118,v118\n"
M_"a[124:127],a[156:159],v[204:207],a[124:127]\n"
E_"v119,v119\n"
M_"a[124:127],a[152:155],v[140:143],a[124:127]\n"
"v_add_u32 v226,1,v226\n"
C_"v220,v108,v109\n"
M_"v[64:67],a[224:227],v[0:3],v[160:163]\n"
E_"v120,v120\n"
M_"v[64:67],a[228:231],v[16:19],v[64:67]\n"
E_"v121,v121\n"
M_"v[64:67],a[232:235],v[32:35],v[64:67]\n"
E_"v122,v122\n"
M_"v[64:67],a[236:239],v[48:51],v[64:67]\n"
E_"v123,v123\n"
M_"v[68:71],a[236:239],v[52:55],v[164:167]\n"
E_"v124,v124\n"
M_"v[68:71],a[232:235],v[36:39],v[68:71]\n"
E_"v125,v125\n"
M_"v[68:71],a[228:231],v[20:23],v[68:71]\n"
W_"0)\n"
"ds_write_b32 v227,v226\n"
C_"v221,v110,v111\n"
M_"v[68:71],a[224:227],v[4:7],v[68:71]\n"
E_"v126,v126\n"
M_"v[72:75],a[224:227],v[8:11],v[168:171]\n"
E_"v127,v127\n"
M_"v[72:75],a[228:231],v[24:27],v[72:75]\n"
E_"v64,v64\n"
M_"v[72:75],a[232:235],v[40:43],v[72:75]\n"
E_"v65,v65\n"
M_"v[72:75],a[236:239],v[56:59],v[72:75]\n"
E_"v66,v66\n"
M_"v[76:79],a[236:239],v[60:63],v[172:175]\n"
E_"v67,v67\n"
M_"v[76:79],a[232:235],v[44:47],v[76:79]\n"
E_"v68,v68\n"
M_"v[76:79],a[228:231],v[28:31],v[76:79]\n"
E_"v69,v69\n"
M_"v[76:79],a[224:227],v[12:15],v[76:79]\n"
E_"v70,v70\n"
M_"v[80:83],a[240:243],v[0:3],v[160:163]\n"
E_"v71,v71\n"
M_"v[80:83],a[244:247],v[16:19],v[80:83]\n"
E_"v72,v72\n"
M_"v[80:83],a[248:251],v[32:35],v[80:83]\n"
E_"v73,v73\n"
M_"v[80:83],a[252:255],v[48:51],v[80:83]\n"
"ds_read_b32 v241,v240 offset:0\n"
C_"v210,v112,v113\n"
M_"v[84:87],a[252:255],v[52:55],v[164:167]\n"
E_"v74,v74\n"
M_"v[84:87],a[248:251],v[36:39],v[84:87]\n"
E_"v75,v75\n"
M_"v[84:87],a[244:247],v[20:23],v[84:87]\n"
E_"v76,v76\n"
M_"v[84:87],a[240:243],v[4:7],v[84:87]\n"
E_"v77,v77\n"
M_"v[88:91],a[240:243],v[8:11],v[168:171]\n"
E_"v78,v78\n"
M_"v[88:91],a[244:247],v[24:27],v[88:91]\n"
E_"v79,v79\n"
M_"v[88:91],a[248:251],v[40:43],v[88:91]\n"
W_"0)\n"
"v_cmp_lt_u32 vcc,v241,v226\n"
C_"v211,v114,v115\n"
M_"v[88:91],a[252:255],v[56:59],v[88:91]\n"
E_"v80,v80\n"
M_"v[92:95],a[252:255],v[60:63],v[172:175]\n"
"s_cbranch_vccnz SPINBI2_%=\n"
"BACKBI2_%=:\n"
A_"m0,s35,0\n"
M_"v[92:95],a[248:251],v[44:47],v[92:95]\n"
A_"s62,s34,0\n"
T_"a[128:129],v152 offset:49152\n"
"v_mov_b32 v228,-1\n"
M_"v[92:95],a[244:247],v[28:31],v[92:95]\n"
"s_lshl_b32 s95,s70,1\n"
E_"v81,v81\n"
M_"v[92:95],a[240:243],v[12:15],v[92:95]\n"
T_"a[130:131],v152 offset:53248\n"
"v_mov_b32 v229,-1\n"
M_"v[96:99],a[192:195],v[0:3],v[160:163]\n"
L_"v255,s[24:27],s62 offen sc1 lds\n"
M_"v[96:99],a[196:199],v[16:19],v[96:99]\n"
T_"a[132:133],v152 offset:57344\n"
"v_mov_b32 v230,-1\n"
M_"v[96:99],a[200:203],v[32:35],v[96:99]\n"
"v_mov_b32 v231,-1\n"
C_"v214,v116,v117\n"
M_"v[96:99],a[204:207],v[48:51],v[96:99]\n"
T_"a[134:135],v152 offset:61440\n"
C_"v215,v118,v119\n"
M_"v[100:103],a[204:207],v[52:55],v[164:167]\n"
"s_cmp_eq_u32 s96,9\n"
"s_cselect_b32 s18,s22,s18\n"
C_"v218,v120,v121\n"
M_"v[100:103],a[200:203],v[36:39],v[100:103]\n"
"s_cmp_eq_u32 s96,9\n"
"s_cselect_b32 s94,0,s94\n"
T_"a[136:137],v153 offset:49152\n"
M_"v[100:103],a[196:199],v[20:23],v[100:103]\n"
"s_cmp_eq_u32 s96,9\n"
"s_cselect_b64 s[16:17],s[20:21],s[16:17]\n"
"s_min_u32 s94,s70,1\n"
M_"v[100:103],a[192:195],v[4:7],v[100:103]\n"
"s_cmp_gt_u32 s96,9\n"
"s_cselect_b32 s18,0,s18\n"
T_"a[138:139],v153 offset:53248\n"
M_"v[104:107],a[192:195],v[8:11],v[168:171]\n"
A_"s94,s94,s95\n"
L_"v255,s[24:27],s62 offen offset:1024 sc1 lds\n"
M_"v[104:107],a[196:199],v[24:27],v[104:107]\n"
A_"s96,s70,1\n"
"s_lshl_b32 s94,s94,10\n"
T_"a[140:141],v153 offset:57344\n"
M_"v[104:107],a[200:203],v[40:43],v[104:107]\n"
"s_and_b32 s96,s96,3\n"
A_"s94,s94,36864\n"
C_"v219,v122,v123\n"
M_"v[104:107],a[204:207],v[56:59],v[104:107]\n"
"s_mul_i32 s96,s96,9216\n"
T_"a[142:143],v153 offset:61440\n"
C_"v222,v124,v125\n"
M_"v[108:111],a[204:207],v[60:63],v[172:175]\n"
A_"s96,s96,s94\n"
E_"v82,v82\n"
M_"v[108:111],a[200:203],v[44:47],v[108:111]\n"
T_"a[144:145],v152 offset:49664\n"
C_"v223,v126,v127\n"
M_"v[108:111],a[196:199],v[28:31],v[108:111]\n"
O_"v[228:231],v255,s[48:51],s96 offen sc1\n"
M_"v[108:111],a[192:195],v[12:15],v[108:111]\n"
T_"a[146:147],v152 offset:53760\n"
C_"v128,v64,v65\n"
M_"v[112:115],a[208:211],v[0:3],v[160:163]\n"
L_"v255,s[24:27],s62 offen offset:2048 sc1 lds\n"
M_"v[112:115],a[212:215],v[16:19],v[112:115]\n"
T_"a[148:149],v152 offset:57856\n"
C_"v129,v66,v67\n"
M_"v[112:115],a[216:219],v[32:35],v[112:115]\n"
E_"v83,v83\n"
M_"v[112:115],a[220:223],v[48:51],v[112:115]\n"
T_"a[150:151],v152 offset:61952\n"
C_"v132,v68,v69\n"
M_"v[116:119],a[220:223],v[52:55],v[164:167]\n"
E_"v84,v84\n"
M_"v[116:119],a[216:219],v[36:39],v[116:119]\n"
T_"a[152:153],v153 offset:49664\n"
C_"v133,v70,v71\n"
M_"v[116:119],a[212:215],v[20:23],v[116:119]\n"
E_"v85,v85\n"
M_"v[116:119],a[208:211],v[4:7],v[116:119]\n"
T_"a[154:155],v153 offset:53760\n"
C_"v136,v72,v73\n"
M_"v[120:123],a[208:211],v[8:11],v[168:171]\n"
L_"v255,s[24:27],s62 offen offset:3072 sc1 lds\n"
M_"v[120:123],a[212:215],v[24:27],v[120:123]\n"
A_"m0,s35,4096\n"
T_"a[156:157],v153 offset:57856\n"
C_"v137,v74,v75\n"
M_"v[120:123],a[216:219],v[40:43],v[120:123]\n"
A_"s62,s34,4096\n"
O_"v[228:231],v255,s[48:51],s96 offen offset:1024 sc1\n"
M_"v[120:123],a[220:223],v[56:59],v[120:123]\n"
T_"a[158:159],v153 offset:61952\n"
C_"v140,v76,v77\n"
M_"v[124:127],a[220:223],v[60:63],v[172:175]\n"
E_"v86,v86\n"
M_"v[124:127],a[216:219],v[44:47],v[124:127]\n"
B_"a[224:227],v148 offset:32768\n"
C_"v141,v78,v79\n"
M_"v[124:127],a[212:215],v[28:31],v[124:127]\n"
E_"v87,v87\n"
M_"v[124:127],a[208:211],v[12:15],v[124:127]\n"
B_"a[228:231],v149 offset:32768\n"
C_"v130,v80,v81\n"
M_"a[160:163],a[176:179],v[176:179],a[160:163]\n"
L_"v255,s[24:27],s62 offen sc1 lds\n"
M_"a[160:163],a[176:179],v[208:211],a[160:163]\n"
"s_cmp_eq_u32 s70,0\n"
"s_cselect_b32 s95,0,1073741824\n"
B_"a[232:235],v150 offset:32768\n"
M_"a[164:167],a[176:179],v[180:183],a[164:167]\n"
A_"s97,s96,s95\n"
E_"v88,v88\n"
M_"a[164:167],a[176:179],v[212:215],a[164:167]\n"
A_"s96,s70,2\n"
B_"a[236:239],v151 offset:32768\n"
C_"v131,v82,v83\n"
M_"a[168:171],a[176:179],v[184:187],a[168:171]\n"
"s_and_b32 s96,s96,3\n"
O_"v[228:231],v255,s[48:51],s97 offen offset:2048 sc1\n"
M_"a[168:171],a[176:179],v[216:219],a[168:171]\n"
"s_mul_i32 s96,s96,9216\n"
B_"a[240:243],v148 offset:36864\n"
C_"v134,v84,v85\n"
M_"a[172:175],a[176:179],v[188:191],a[172:175]\n"
A_"s96,s96,s94\n"
E_"v89,v89\n"
M_"a[172:175],a[176:179],v[220:223],a[172:175]\n"
W_"15)\n"
B_"a[244:247],v149 offset:36864\n"
C_"v135,v86,v87\n"
M_"a[0:3],a[128:131],v[176:179],a[0:3]\n"
A_"s97,s96,s95\n"
L_"v255,s[24:27],s62 offen offset:1024 sc1 lds\n"
M_"a[0:3],a[132:135],v[208:211],a[0:3]\n"
B_"a[248:251],v150 offset:36864\n"
C_"v138,v88,v89\n"
M_"a[4:7],a[132:135],v[212:215],a[4:7]\n"
E_"v90,v90\n"
M_"a[4:7],a[128:131],v[180:183],a[4:7]\n"
B_"a[252:255],v151 offset:36864\n"
M_"a[8:11],a[128:131],v[184:187],a[8:11]\n"
E_"v91,v91\n"
M_"a[8:11],a[132:135],v[216:219],a[8:11]\n"
B_"a[192:195],v148 offset:40960\n"
C_"v139,v90,v91\n"
M_"a[12:15],a[132:135],v[220:223],a[12:15]\n"
E_"v92,v92\n"
M_"a[12:15],a[128:131],v[188:191],a[12:15]\n"
T_"a[128:129],v152 offset:50176\n"
M_"a[16:19],a[136:139],v[176:179],a[16:19]\n"
W_"15)\n"
L_"v255,s[24:27],s62 offen offset:2048 sc1 lds\n"
M_"a[16:19],a[140:143],v[208:211],a[16:19]\n"
T_"a[130:131],v152 offset:54272\n"
M_"a[20:23],a[140:143],v[212:215],a[20:23]\n"
O_"v[228:231],v255,s[48:51],s96 offen sc1\n"
M_"a[20:23],a[136:139],v[180:183],a[20:23]\n"
T_"a[132:133],v152 offset:58368\n"
M_"a[24:27],a[136:139],v[184:187],a[24:27]\n"
E_"v93,v93\n"
M_"a[24:27],a[140:143],v[216:219],a[24:27]\n"
T_"a[134:135],v152 offset:62464\n"
C_"v142,v92,v93\n"
M_"a[28:31],a[140:143],v[220:223],a[28:31]\n"
E_"v94,v94\n"
M_"a[28:31],a[136:139],v[188:191],a[28:31]\n"
T_"a[136:137],v153 offset:50176\n"
M_"a[32:35],a[144:147],v[176:179],a[32:35]\n"
W_"15)\n"
L_"v255,s[24:27],s62 offen offset:3072 sc1 lds\n"
M_"a[32:35],a[148:151],v[208:211],a[32:35]\n"
A_"m0,s35,8192\n"
T_"a[138:139],v153 offset:54272\n"
M_"a[36:39],a[148:151],v[212:215],a[36:39]\n"
A_"s62,s34,8192\n"
E_"v95,v95\n"
M_"a[36:39],a[144:147],v[180:183],a[36:39]\n"
T_"a[140:141],v153 offset:58368\n"
C_"v143,v94,v95\n"
M_"a[40:43],a[144:147],v[184:187],a[40:43]\n"
O_"v[228:231],v255,s[48:51],s96 offen offset:1024 sc1\n"
M_"a[40:43],a[148:151],v[216:219],a[40:43]\n"
A_"s96,s70,3\n"
T_"a[142:143],v153 offset:62464\n"
M_"a[44:47],a[148:151],v[220:223],a[44:47]\n"
"s_and_b32 s96,s96,3\n"
E_"v96,v96\n"
M_"a[44:47],a[144:147],v[188:191],a[44:47]\n"
"s_mul_i32 s96,s96,9216\n"
T_"a[144:145],v152 offset:50688\n"
M_"a[48:51],a[152:155],v[176:179],a[48:51]\n"
W_"15)\n"
L_"v255,s[24:27],s62 offen sc1 lds\n"
M_"a[48:51],a[156:159],v[208:211],a[48:51]\n"
A_"m0,s35,114688\n"
T_"a[146:147],v152 offset:54784\n"
M_"a[52:55],a[156:159],v[212:215],a[52:55]\n"
A_"s62,s34,446464\n"
E_"v97,v97\n"
M_"a[52:55],a[152:155],v[180:183],a[52:55]\n"
A_"s96,s96,s94\n"
T_"a[148:149],v152 offset:58880\n"
C_"v192,v96,v97\n"
M_"a[56:59],a[152:155],v[184:187],a[56:59]\n"
E_"v98,v98\n"
M_"a[56:59],a[156:159],v[216:219],a[56:59]\n"
T_"a[150:151],v152 offset:62976\n"
M_"a[60:63],a[156:159],v[220:223],a[60:63]\n"
E_"v99,v99\n"
M_"a[60:63],a[152:155],v[188:191],a[60:63]\n"
W_"10)\n"
T_"a[152:153],v153 offset:50688\n"
C_"v193,v98,v99\n"
M_"a[64:67],a[128:131],v[176:179],a[64:67]\n"
W_"9)\n"
L_"v255,s[24:27],s62 offen sc1 lds\n"
M_"a[64:67],a[132:135],v[208:211],a[64:67]\n"
T_"a[154:155],v153 offset:54784\n"
M_"a[68:71],a[132:135],v[212:215],a[68:71]\n"
O_"v[228:231],v255,s[48:51],s97 offen offset:2048 sc1\n"
M_"a[68:71],a[128:131],v[180:183],a[68:71]\n"
A_"s97,s96,s95\n"
T_"a[156:157],v153 offset:58880\n"
M_"a[72:75],a[128:131],v[184:187],a[72:75]\n"
E_"v100,v100\n"
M_"a[72:75],a[132:135],v[216:219],a[72:75]\n"
T_"a[158:159],v153 offset:62976\n"
M_"a[76:79],a[132:135],v[220:223],a[76:79]\n"
E_"v101,v101\n"
M_"a[76:79],a[128:131],v[188:191],a[76:79]\n"
W_"10)\n"
B_"a[196:199],v149 offset:40960\n"
C_"v196,v100,v101\n"
M_"a[80:83],a[136:139],v[176:179],a[80:83]\n"
W_"9)\n"
L_"v255,s[24:27],s62 offen offset:1024 sc1 lds\n"
M_"a[80:83],a[140:143],v[208:211],a[80:83]\n"
B_"a[200:203],v150 offset:40960\n"
M_"a[84:87],a[140:143],v[212:215],a[84:87]\n"
E_"v102,v102\n"
M_"a[84:87],a[136:139],v[180:183],a[84:87]\n"
B_"a[204:207],v151 offset:40960\n"
M_"a[88:91],a[136:139],v[184:187],a[88:91]\n"
O_"v[228:231],v255,s[48:51],s96 offen sc1\n"
M_"a[88:91],a[140:143],v[216:219],a[88:91]\n"
B_"a[208:211],v148 offset:45056\n"
M_"a[92:95],a[140:143],v[220:223],a[92:95]\n"
E_"v103,v103\n"
M_"a[92:95],a[136:139],v[188:191],a[92:95]\n"
W_"10)\n"
B_"a[212:215],v149 offset:45056\n"
C_"v197,v102,v103\n"
M_"a[96:99],a[144:147],v[176:179],a[96:99]\n"
W_"9)\n"
L_"v255,s[24:27],s62 offen offset:2048 sc1 lds\n"
M_"a[96:99],a[148:151],v[208:211],a[96:99]\n"
B_"a[216:219],v150 offset:45056\n"
M_"a[100:103],a[148:151],v[212:215],a[100:103]\n"
E_"v104,v104\n"
M_"a[100:103],a[144:147],v[180:183],a[100:103]\n"
B_"a[220:223],v151 offset:45056\n"
M_"a[104:107],a[144:147],v[184:187],a[104:107]\n"
E_"v105,v105\n"
M_"a[104:107],a[148:151],v[216:219],a[104:107]\n"
E_"v106,v106\n"
M_"a[108:111],a[148:151],v[220:223],a[108:111]\n"
E_"v107,v107\n"
M_"a[108:111],a[144:147],v[188:191],a[108:111]\n"
W_"9)\n"
L_"v255,s[24:27],s62 offen offset:3072 sc1 lds\n"
M_"a[112:115],a[152:155],v[176:179],a[112:115]\n"
W_"7)\n"
O_"v[228:231],v255,s[48:51],s96 offen offset:1024 sc1\n"
M_"a[112:115],a[156:159],v[208:211],a[112:115]\n"
E_"v108,v108\n"
M_"a[116:119],a[156:159],v[212:215],a[116:119]\n"
E_"v109,v109\n"
M_"a[116:119],a[152:155],v[180:183],a[116:119]\n"
E_"v110,v110\n"
M_"a[120:123],a[152:155],v[184:187],a[120:123]\n"
E_"v111,v111\n"
M_"a[120:123],a[156:159],v[216:219],a[120:123]\n"
E_"v112,v112\n"
M_"a[124:127],a[156:159],v[220:223],a[124:127]\n"
E_"v113,v113\n"
M_"a[124:127],a[152:155],v[188:191],a[124:127]\n"
"v_add_u32 v226,1,v226\n"
C_"v200,v104,v105\n"
M_"v[64:67],a[224:227],v[0:3],v[160:163]\n"
E_"v114,v114\n"
M_"v[64:67],a[228:231],v[16:19],v[64:67]\n"
E_"v115,v115\n"
M_"v[64:67],a[232:235],v[32:35],v[64:67]\n"
E_"v116,v116\n"
M_"v[64:67],a[236:239],v[48:51],v[64:67]\n"
O_"v[228:231],v255,s[48:51],s97 offen offset:2048 sc1\n"
M_"v[68:71],a[236:239],v[52:55],v[164:167]\n"
E_"v117,v117\n"
M_"v[68:71],a[232:235],v[36:39],v[68:71]\n"
E_"v118,v118\n"
M_"v[68:71],a[228:231],v[20:23],v[68:71]\n"
W_"0)\n"
"ds_write_b32 v227,v226\n"
C_"v201,v106,v107\n"
M_"v[68:71],a[224:227],v[4:7],v[68:71]\n"
E_"v119,v119\n"
M_"v[72:75],a[224:227],v[8:11],v[168:171]\n"
E_"v120,v120\n"
M_"v[72:75],a[228:231],v[24:27],v[72:75]\n"
E_"v121,v121\n"
M_"v[72:75],a[232:235],v[40:43],v[72:75]\n"
E_"v122,v122\n"
M_"v[72:75],a[236:239],v[56:59],v[72:75]\n"
E_"v123,v123\n"
M_"v[76:79],a[236:239],v[60:63],v[172:175]\n"
E_"v124,v124\n"
M_"v[76:79],a[232:235],v[44:47],v[76:79]\n"
E_"v125,v125\n"
M_"v[76:79],a[228:231],v[28:31],v[76:79]\n"
E_"v126,v126\n"
M_"v[76:79],a[224:227],v[12:15],v[76:79]\n"
E_"v127,v127\n"
M_"v[80:83],a[240:243],v[0:3],v[160:163]\n"
E_"v64,v64\n"
M_"v[80:83],a[244:247],v[16:19],v[80:83]\n"
E_"v65,v65\n"
M_"v[80:83],a[248:251],v[32:35],v[80:83]\n"
E_"v66,v66\n"
M_"v[80:83],a[252:255],v[48:51],v[80:83]\n"
"ds_read_b32 v241,v240 offset:0\n"
C_"v204,v108,v109\n"
M_"v[84:87],a[252:255],v[52:55],v[164:167]\n"
E_"v67,v67\n"
M_"v[84:87],a[248:251],v[36:39],v[84:87]\n"
E_"v68,v68\n"
M_"v[84:87],a[244:247],v[20:23],v[84:87]\n"
E_"v69,v69\n"
M_"v[84:87],a[240:243],v[4:7],v[84:87]\n"
E_"v80,v80\n"
M_"v[88:91],a[240:243],v[8:11],v[168:171]\n"
E_"v81,v81\n"
M_"v[88:91],a[244:247],v[24:27],v[88:91]\n"
E_"v82,v82\n"
M_"v[88:91],a[248:251],v[40:43],v[88:91]\n"
W_"0)\n"
"v_cmp_lt_u32 vcc,v241,v226\n"
C_"v205,v110,v111\n"
M_"v[88:91],a[252:255],v[56:59],v[88:91]\n"
E_"v83,v83\n"
M_"v[92:95],a[252:255],v[60:63],v[172:175]\n"
"s_cbranch_vccnz SPINBI3_%=\n"
"BACKBI3_%=:\n"
A_"m0,s35,36864\n"
M_"v[92:95],a[248:251],v[44:47],v[92:95]\n"
A_"s62,s34,147456\n"
T_"a[128:129],v154 offset:16384\n"
C_"v194,v112,v113\n"
M_"v[92:95],a[244:247],v[28:31],v[92:95]\n"
S_"s63,0\n"
E_"v70,v70\n"
M_"v[92:95],a[240:243],v[12:15],v[92:95]\n"
T_"a[130:131],v154 offset:20480\n"
C_"v195,v114,v115\n"
M_"v[96:99],a[192:195],v[0:3],v[160:163]\n"
L_"v255,s[24:27],s62 offen sc1 lds\n"
M_"v[96:99],a[196:199],v[16:19],v[96:99]\n"
T_"a[132:133],v154 offset:24576\n"
C_"v198,v116,v117\n"
M_"v[96:99],a[200:203],v[32:35],v[96:99]\n"
L_"v255,s[24:27],s62 offen offset:1024 sc1 lds\n"
M_"v[96:99],a[204:207],v[48:51],v[96:99]\n"
T_"a[134:135],v154 offset:28672\n"
C_"v199,v118,v119\n"
M_"v[100:103],a[204:207],v[52:55],v[164:167]\n"
E_"v71,v71\n"
M_"v[100:103],a[200:203],v[36:39],v[100:103]\n"
T_"a[136:137],v155 offset:16384\n"
C_"v202,v120,v121\n"
M_"v[100:103],a[196:199],v[20:23],v[100:103]\n"
L_"v255,s[24:27],s62 offen offset:2048 sc1 lds\n"
M_"v[100:103],a[192:195],v[4:7],v[100:103]\n"
T_"a[138:139],v155 offset:20480\n"
C_"v203,v122,v123\n"
M_"v[104:107],a[192:195],v[8:11],v[168:171]\n"
L_"v255,s[24:27],s62 offen offset:3072 sc1 lds\n"
M_"v[104:107],a[196:199],v[24:27],v[104:107]\n"
A_"m0,s35,40960\n"
A_"s62,s34,151552\n"
T_"a[140:141],v155 offset:24576\n"
M_"v[104:107],a[200:203],v[40:43],v[104:107]\n"
E_"v96,v96\n"
M_"v[104:107],a[204:207],v[56:59],v[104:107]\n"
T_"a[142:143],v155 offset:28672\n"
C_"v206,v124,v125\n"
M_"v[108:111],a[204:207],v[60:63],v[172:175]\n"
L_"v255,s[24:27],s62 offen sc1 lds\n"
M_"v[108:111],a[200:203],v[44:47],v[108:111]\n"
T_"a[144:145],v154 offset:16896\n"
C_"v207,v126,v127\n"
M_"v[108:111],a[196:199],v[28:31],v[108:111]\n"
L_"v255,s[24:27],s62 offen offset:1024 sc1 lds\n"
M_"v[108:111],a[192:195],v[12:15],v[108:111]\n"
T_"a[146:147],v154 offset:20992\n"
C_"v176,v64,v65\n"
M_"v[112:115],a[208:211],v[0:3],v[160:163]\n"
E_"v97,v97\n"
M_"v[112:115],a[212:215],v[16:19],v[112:115]\n"
T_"a[148:149],v154 offset:25088\n"
C_"v177,v66,v67\n"
M_"v[112:115],a[216:219],v[32:35],v[112:115]\n"
L_"v255,s[24:27],s62 offen offset:2048 sc1 lds\n"
M_"v[112:115],a[220:223],v[48:51],v[112:115]\n"
T_"a[150:151],v154 offset:29184\n"
C_"v178,v80,v81\n"
M_"v[116:119],a[220:223],v[52:55],v[164:167]\n"
L_"v255,s[24:27],s62 offen offset:3072 sc1 lds\n"
M_"v[116:119],a[216:219],v[36:39],v[116:119]\n"
A_"m0,s35,45056\n"
A_"s62,s34,155648\n"
T_"a[152:153],v155 offset:16896\n"
M_"v[116:119],a[212:215],v[20:23],v[116:119]\n"
E_"v98,v98\n"
M_"v[116:119],a[208:211],v[4:7],v[116:119]\n"
T_"a[154:155],v155 offset:20992\n"
C_"v179,v82,v83\n"
M_"v[120:123],a[208:211],v[8:11],v[168:171]\n"
L_"v255,s[24:27],s62 offen sc1 lds\n"
M_"v[120:123],a[212:215],v[24:27],v[120:123]\n"
A_"m0,s35,110592\n"
A_"s62,s34,442368\n"
T_"a[156:157],v155 offset:25088\n"
M_"v[120:123],a[216:219],v[40:43],v[120:123]\n"
L_"v255,s[24:27],s62 offen sc1 lds\n"
M_"v[120:123],a[220:223],v[56:59],v[120:123]\n"
T_"a[158:159],v155 offset:29184\n"
C_"v208,v96,v97\n"
M_"v[124:127],a[220:223],v[60:63],v[172:175]\n"
E_"v99,v99\n"
M_"v[124:127],a[216:219],v[44:47],v[124:127]\n"
L_"v255,s[24:27],s62 offen offset:1024 sc1 lds\n"
M_"v[124:127],a[212:215],v[28:31],v[124:127]\n"
E_"v112,v112\n"
M_"v[124:127],a[208:211],v[12:15],v[124:127]\n"
E_"v113,v113\n"
M_"a[160:163],a[176:179],v[128:131],a[160:163]\n"
L_"v255,s[24:27],s62 offen offset:2048 sc1 lds\n"
M_"a[160:163],a[176:179],v[192:195],a[160:163]\n"
E_"v114,v114\n"
M_"a[164:167],a[176:179],v[132:135],a[164:167]\n"
E_"v115,v115\n"
M_"a[164:167],a[176:179],v[196:199],a[164:167]\n"
L_"v255,s[24:27],s62 offen offset:3072 sc1 lds\n"
M_"a[168:171],a[176:179],v[136:139],a[168:171]\n"
E_"v84,v84\n"
M_"a[168:171],a[176:179],v[200:203],a[168:171]\n"
E_"v85,v85\n"
M_"a[172:175],a[176:179],v[140:143],a[172:175]\n"
L_"a[192:195],v255,s[36:39],s63 offen sc1\n"
M_"a[172:175],a[176:179],v[204:207],a[172:175]\n"
W_"14)\n"
E_"v86,v86\n"
M_"a[0:3],a[128:131],v[128:131],a[0:3]\n"
W_"12)\n"
E_"v87,v87\n"
M_"a[0:3],a[132:135],v[192:195],a[0:3]\n"
E_"v100,v100\n"
M_"a[4:7],a[132:135],v[196:199],a[4:7]\n"
E_"v101,v101\n"
M_"a[4:7],a[128:131],v[132:135],a[4:7]\n"
E_"v102,v102\n"
M_"a[8:11],a[128:131],v[136:139],a[8:11]\n"
E_"v103,v103\n"
M_"a[8:11],a[132:135],v[200:203],a[8:11]\n"
E_"v116,v116\n"
M_"a[12:15],a[132:135],v[204:207],a[12:15]\n"
L_"a[196:199],v255,s[36:39],s63 offen offset:1024 sc1\n"
M_"a[12:15],a[128:131],v[140:143],a[12:15]\n"
W_"10)\n"
T_"a[128:129],v154 offset:17408\n"
C_"v209,v98,v99\n"
M_"a[16:19],a[136:139],v[128:131],a[16:19]\n"
W_"9)\n"
E_"v117,v117\n"
M_"a[16:19],a[140:143],v[192:195],a[16:19]\n"
T_"a[130:131],v154 offset:21504\n"
C_"v210,v112,v113\n"
M_"a[20:23],a[140:143],v[196:199],a[20:23]\n"
E_"v118,v118\n"
M_"a[20:23],a[136:139],v[132:135],a[20:23]\n"
T_"a[132:133],v154 offset:25600\n"
C_"v211,v114,v115\n"
M_"a[24:27],a[136:139],v[136:139],a[24:27]\n"
E_"v119,v119\n"
M_"a[24:27],a[140:143],v[200:203],a[24:27]\n"
T_"a[134:135],v154 offset:29696\n"
C_"v180,v68,v69\n"
M_"a[28:31],a[140:143],v[204:207],a[28:31]\n"
L_"a[200:203],v255,s[36:39],s63 offen offset:2048 sc1\n"
M_"a[28:31],a[136:139],v[140:143],a[28:31]\n"
W_"10)\n"
T_"a[136:137],v155 offset:17408\n"
C_"v181,v70,v71\n"
M_"a[32:35],a[144:147],v[128:131],a[32:35]\n"
W_"9)\n"
E_"v72,v72\n"
M_"a[32:35],a[148:151],v[192:195],a[32:35]\n"
T_"a[138:139],v155 offset:21504\n"
C_"v182,v84,v85\n"
M_"a[36:39],a[148:151],v[196:199],a[36:39]\n"
E_"v73,v73\n"
M_"a[36:39],a[144:147],v[132:135],a[36:39]\n"
T_"a[140:141],v155 offset:25600\n"
C_"v183,v86,v87\n"
M_"a[40:43],a[144:147],v[136:139],a[40:43]\n"
E_"v74,v74\n"
M_"a[40:43],a[148:151],v[200:203],a[40:43]\n"
T_"a[142:143],v155 offset:29696\n"
C_"v212,v100,v101\n"
M_"a[44:47],a[148:151],v[204:207],a[44:47]\n"
L_"a[204:207],v255,s[36:39],s63 offen offset:3072 sc1\n"
M_"a[44:47],a[144:147],v[140:143],a[44:47]\n"
W_"10)\n"
T_"a[144:145],v154 offset:17920\n"
C_"v213,v102,v103\n"
M_"a[48:51],a[152:155],v[128:131],a[48:51]\n"
W_"9)\n"
E_"v75,v75\n"
M_"a[48:51],a[156:159],v[192:195],a[48:51]\n"
S_"s63,147456\n"
T_"a[146:147],v154 offset:22016\n"
C_"v214,v116,v117\n"
M_"a[52:55],a[156:159],v[196:199],a[52:55]\n"
E_"v88,v88\n"
M_"a[52:55],a[152:155],v[132:135],a[52:55]\n"
T_"a[148:149],v154 offset:26112\n"
C_"v215,v118,v119\n"
M_"a[56:59],a[152:155],v[136:139],a[56:59]\n"
E_"v89,v89\n"
M_"a[56:59],a[156:159],v[200:203],a[56:59]\n"
T_"a[150:151],v154 offset:30208\n"
C_"v184,v72,v73\n"
M_"a[60:63],a[156:159],v[204:207],a[60:63]\n"
L_"a[208:211],v255,s[36:39],s63 offen sc1\n"
M_"a[60:63],a[152:155],v[140:143],a[60:63]\n"
W_"10)\n"
T_"a[152:153],v155 offset:17920\n"
C_"v185,v74,v75\n"
M_"a[64:67],a[128:131],v[128:131],a[64:67]\n"
W_"9)\n"
E_"v90,v90\n"
M_"a[64:67],a[132:135],v[192:195],a[64:67]\n"
T_"a[154:155],v155 offset:22016\n"
C_"v186,v88,v89\n"
M_"a[68:71],a[132:135],v[196:199],a[68:71]\n"
E_"v91,v91\n"
M_"a[68:71],a[128:131],v[132:135],a[68:71]\n"
T_"a[156:157],v155 offset:26112\n"
C_"v187,v90,v91\n"
M_"a[72:75],a[128:131],v[136:139],a[72:75]\n"
E_"v104,v104\n"
M_"a[72:75],a[132:135],v[200:203],a[72:75]\n"
T_"a[158:159],v155 offset:30208\n"
M_"a[76:79],a[132:135],v[204:207],a[76:79]\n"
L_"a[212:215],v255,s[36:39],s63 offen offset:1024 sc1\n"
M_"a[76:79],a[128:131],v[140:143],a[76:79]\n"
W_"10)\n"
E_"v105,v105\n"
M_"a[80:83],a[136:139],v[128:131],a[80:83]\n"
W_"8)\n"
E_"v106,v106\n"
M_"a[80:83],a[140:143],v[192:195],a[80:83]\n"
E_"v107,v107\n"
M_"a[84:87],a[140:143],v[196:199],a[84:87]\n"
E_"v120,v120\n"
M_"a[84:87],a[136:139],v[132:135],a[84:87]\n"
E_"v121,v121\n"
M_"a[88:91],a[136:139],v[136:139],a[88:91]\n"
E_"v122,v122\n"
M_"a[88:91],a[140:143],v[200:203],a[88:91]\n"
E_"v123,v123\n"
M_"a[92:95],a[140:143],v[204:207],a[92:95]\n"
L_"a[216:219],v255,s[36:39],s63 offen offset:2048 sc1\n"
M_"a[92:95],a[136:139],v[140:143],a[92:95]\n"
W_"6)\n"
E_"v76,v76\n"
M_"a[96:99],a[144:147],v[128:131],a[96:99]\n"
W_"4)\n"
E_"v77,v77\n"
M_"a[96:99],a[148:151],v[192:195],a[96:99]\n"
E_"v78,v78\n"
M_"a[100:103],a[148:151],v[196:199],a[100:103]\n"
E_"v79,v79\n"
M_"a[100:103],a[144:147],v[132:135],a[100:103]\n"
E_"v92,v92\n"
M_"a[104:107],a[144:147],v[136:139],a[104:107]\n"
E_"v93,v93\n"
M_"a[104:107],a[148:151],v[200:203],a[104:107]\n"
E_"v94,v94\n"
M_"a[108:111],a[148:151],v[204:207],a[108:111]\n"
L_"a[220:223],v255,s[36:39],s63 offen offset:3072 sc1\n"
M_"a[108:111],a[144:147],v[140:143],a[108:111]\n"
W_"2)\n"
E_"v95,v95\n"
M_"a[112:115],a[152:155],v[128:131],a[112:115]\n"
W_"0)\n"
E_"v108,v108\n"
M_"a[112:115],a[156:159],v[192:195],a[112:115]\n"
S_"s63,4096\n"
C_"v216,v104,v105\n"
C_"v217,v106,v107\n"
M_"a[116:119],a[156:159],v[196:199],a[116:119]\n"
C_"v218,v120,v121\n"
C_"v219,v122,v123\n"
M_"a[116:119],a[152:155],v[132:135],a[116:119]\n"
C_"v188,v76,v77\n"
C_"v189,v78,v79\n"
M_"a[120:123],a[152:155],v[136:139],a[120:123]\n"
C_"v190,v92,v93\n"
C_"v191,v94,v95\n"
M_"a[120:123],a[156:159],v[200:203],a[120:123]\n"
E_"v109,v109\n"
M_"a[124:127],a[156:159],v[204:207],a[124:127]\n"
"buffer_load_dwordx2 a[224:225],v250,s[36:39],s63 offen sc1\n"
M_"a[124:127],a[152:155],v[140:143],a[124:127]\n"
"s_sub_u32 s86,s86,1\n"
"s_nop 0\n"
"s_waitcnt vmcnt(30)\n"
"s_barrier\n"
M_"a[160:163],a[176:179],v[176:179],a[160:163]\n"
A_"m0,s35,73728\n"
A_"s62,s34,294912\n"
T_"a[128:129],v236 offset:0\n"
M_"a[160:163],a[176:179],v[208:211],a[160:163]\n"
S_"s91,131072\n"
T_"a[130:131],v236 offset:4096\n"
C_"v220,v108,v109\n"
M_"a[164:167],a[176:179],v[180:183],a[164:167]\n"
S_"s92,262144\n"
T_"a[132:133],v236 offset:8192\n"
"v_mov_b32 v254,0x207\n"
M_"a[164:167],a[176:179],v[212:215],a[164:167]\n"
S_"s93,393216\n"
T_"a[134:135],v236 offset:12288\n"
R_"v156,a160\n"
M_"a[168:171],a[176:179],v[184:187],a[168:171]\n"
"s_lshl_b32 s61,s70,1\n"
T_"a[136:137],v237 offset:0\n"
"v_cmp_nle_f32 s[98:99],v156,s87\n"
M_"a[168:171],a[176:179],v[216:219],a[168:171]\n"
"s_lshl_b32 s71,s71,2\n"
T_"a[138:139],v237 offset:4096\n"
R_"v157,a164\n"
M_"a[172:175],a[176:179],v[188:191],a[172:175]\n"
A_"s71,s71,s33\n"
T_"a[140:141],v237 offset:8192\n"
"v_cmp_nle_f32 s[68:69],v157,s87\n"
E_"v110,v110\n"
E_"v111,v111\n"
E_"v124,v124\n"
E_"v125,v125\n"
E_"v126,v126\n"
E_"v127,v127\n"
C_"v221,v110,v111\n"
C_"v222,v124,v125\n"
C_"v223,v126,v127\n"
"s_nop 1\n"
M_"a[172:175],a[176:179],v[220:223],a[172:175]\n"
W_"5)\n"
T_"a[142:143],v237 offset:12288\n"
R_"v158,a168\n"
M_"a[0:3],a[128:131],v[176:179],a[0:3]\n"
W_"4)\n"
T_"a[144:145],v236 offset:512\n"
"v_lshrrev_b32 v251,4,v255\n"
M_"a[0:3],a[132:135],v[208:211],a[0:3]\n"
"s_or_b64 s[98:99],s[98:99],s[68:69]\n"
T_"a[146:147],v236 offset:4608\n"
R_"v159,a172\n"
M_"a[4:7],a[132:135],v[212:215],a[4:7]\n"
"s_lshl_b32 s71,s71,5\n"
T_"a[148:149],v236 offset:8704\n"
"v_cmp_nle_f32 s[68:69],v158,s87\n"
M_"a[4:7],a[128:131],v[180:183],a[4:7]\n"
T_"a[150:151],v236 offset:12800\n"
R_"v128,a0\n"
M_"a[8:11],a[128:131],v[184:187],a[8:11]\n"
"s_or_b64 s[98:99],s[98:99],s[68:69]\n"
T_"a[152:153],v237 offset:512\n"
R_"v129,a1\n"
M_"a[8:11],a[132:135],v[216:219],a[8:11]\n"
T_"a[154:155],v237 offset:4608\n"
R_"v130,a2\n"
M_"a[12:15],a[132:135],v[220:223],a[12:15]\n"
T_"a[156:157],v237 offset:8704\n"
R_"v131,a3\n"
M_"a[12:15],a[128:131],v[188:191],a[12:15]\n"
W_"9)\n"
T_"a[158:159],v237 offset:12800\n"
R_"v136,a4\n"
M_"a[16:19],a[136:139],v[176:179],a[16:19]\n"
W_"8)\n"
T_"a[128:129],v236 offset:1024\n"
R_"v137,a5\n"
M_"a[16:19],a[140:143],v[208:211],a[16:19]\n"
T_"a[130:131],v236 offset:5120\n"
R_"v138,a6\n"
M_"a[20:23],a[140:143],v[212:215],a[20:23]\n"
T_"a[132:133],v236 offset:9216\n"
R_"v139,a7\n"
M_"a[20:23],a[136:139],v[180:183],a[20:23]\n"
T_"a[134:135],v236 offset:13312\n"
R_"v192,a8\n"
M_"a[24:27],a[136:139],v[184:187],a[24:27]\n"
B_"a[160:163],v255 offset:0\n"
R_"v132,a16\n"
M_"a[24:27],a[140:143],v[216:219],a[24:27]\n"
B_"a[164:167],v255 offset:9216\n"
R_"v133,a17\n"
M_"a[28:31],a[140:143],v[220:223],a[28:31]\n"
T_"a[140:141],v237 offset:9216\n"
R_"v134,a18\n"
M_"a[28:31],a[136:139],v[188:191],a[28:31]\n"
T_"a[136:137],v237 offset:1024\n"
R_"v135,a19\n"
"v_rcp_f32 v156,v156\n"
"s_nop 0\n"
U_"v128,v128,v156\n"
U_"v129,v129,v156\n"
U_"v130,v130,v156\n"
U_"v131,v131,v156\n"
U_"v132,v132,v156\n"
U_"v133,v133,v156\n"
U_"v134,v134,v156\n"
U_"v135,v135,v156\n"
C_"v64,v128,v129\n"
C_"v65,v130,v131\n"
C_"v66,v132,v133\n"
C_"v67,v134,v135\n"
O_"v[64:67],v255,s[52:55],0 offen sc1\n"
"s_nop 0\n"
M_"a[180:183],a[176:179],v[64:67],0\n"
W_"14)\n"
T_"a[138:139],v237 offset:5120\n"
R_"v140,a20\n"
M_"a[32:35],a[144:147],v[176:179],a[32:35]\n"
W_"13)\n"
T_"a[142:143],v237 offset:13312\n"
R_"v141,a21\n"
M_"a[32:35],a[148:151],v[208:211],a[32:35]\n"
B_"a[168:171],v255 offset:18432\n"
R_"v142,a22\n"
M_"a[36:39],a[148:151],v[212:215],a[36:39]\n"
B_"a[172:175],v255 offset:27648\n"
R_"v143,a23\n"
M_"a[36:39],a[144:147],v[180:183],a[36:39]\n"
L_"v255,s[24:27],s62 offen sc1 lds\n"
M_"a[40:43],a[144:147],v[184:187],a[40:43]\n"
"buffer_load_dwordx2 a[226:227],v250,s[36:39],s63 offen offset:512 sc1\n"
M_"a[40:43],a[148:151],v[216:219],a[40:43]\n"
L_"v255,s[24:27],s62 offen offset:1024 sc1 lds\n"
M_"a[44:47],a[148:151],v[220:223],a[44:47]\n"
T_"a[148:149],v236 offset:9728\n"
R_"v193,a9\n"
M_"a[44:47],a[144:147],v[188:191],a[44:47]\n"
W_"15)\n"
T_"a[144:145],v236 offset:1536\n"
R_"v194,a10\n"
M_"a[48:51],a[152:155],v[176:179],a[48:51]\n"
W_"14)\n"
T_"a[146:147],v236 offset:5632\n"
R_"v195,a11\n"
M_"a[48:51],a[156:159],v[208:211],a[48:51]\n"
T_"a[150:151],v236 offset:13824\n"
R_"v196,a24\n"
"v_rcp_f32 v157,v157\n"
"s_nop 0\n"
U_"v136,v136,v157\n"
U_"v137,v137,v157\n"
U_"v138,v138,v157\n"
U_"v139,v139,v157\n"
U_"v140,v140,v157\n"
U_"v141,v141,v157\n"
U_"v142,v142,v157\n"
U_"v143,v143,v157\n"
C_"v68,v136,v137\n"
C_"v69,v138,v139\n"
C_"v70,v140,v141\n"
C_"v71,v142,v143\n"
O_"v[68:71],v255,s[52:55],s91 offen sc1\n"
"s_nop 0\n"
M_"a[180:183],a[176:179],v[68:71],a[180:183]\n"
L_"a[228:231],v255,s[36:39],s63 offen offset:1024 sc1\n"
M_"a[52:55],a[156:159],v[212:215],a[52:55]\n"
L_"v255,s[24:27],s62 offen offset:2048 sc1 lds\n"
M_"a[52:55],a[152:155],v[180:183],a[52:55]\n"
L_"v255,s[24:27],s62 offen offset:3072 sc1 lds\n"
"v_rcp_f32 v158,v158\n"
R_"v197,a25\n"
R_"v198,a26\n"
R_"v199,a27\n"
U_"v192,v192,v158\n"
U_"v193,v193,v158\n"
U_"v194,v194,v158\n"
U_"v195,v195,v158\n"
U_"v196,v196,v158\n"
U_"v197,v197,v158\n"
U_"v198,v198,v158\n"
U_"v199,v199,v158\n"
C_"v72,v192,v193\n"
C_"v73,v194,v195\n"
C_"v74,v196,v197\n"
C_"v75,v198,v199\n"
O_"v[72:75],v255,s[52:55],s92 offen sc1\n"
"s_nop 0\n"
M_"a[180:183],a[176:179],v[72:75],a[180:183]\n"
A_"m0,s35,77824\n"
L_"a[232:235],v255,s[36:39],s63 offen offset:2048 sc1\n"
M_"a[56:59],a[152:155],v[184:187],a[56:59]\n"
A_"s62,s34,299008\n"
L_"a[236:239],v255,s[36:39],s63 offen offset:3072 sc1\n"
M_"a[56:59],a[156:159],v[216:219],a[56:59]\n"
S_"s63,151552\n"
L_"v255,s[24:27],s62 offen sc1 lds\n"
"v_cmp_nle_f32 s[68:69],v159,s87\n"
"v_rcp_f32 v159,v159\n"
R_"v200,a12\n"
R_"v201,a13\n"
R_"v202,a14\n"
R_"v203,a15\n"
R_"v204,a28\n"
R_"v205,a29\n"
R_"v206,a30\n"
R_"v207,a31\n"
U_"v200,v200,v159\n"
U_"v201,v201,v159\n"
U_"v202,v202,v159\n"
U_"v203,v203,v159\n"
U_"v204,v204,v159\n"
U_"v205,v205,v159\n"
U_"v206,v206,v159\n"
U_"v207,v207,v159\n"
C_"v76,v200,v201\n"
C_"v77,v202,v203\n"
C_"v78,v204,v205\n"
C_"v79,v206,v207\n"
O_"v[76:79],v255,s[52:55],s93 offen sc1\n"
"s_nop 0\n"
M_"a[180:183],a[176:179],v[76:79],a[180:183]\n"
"s_or_b64 s[98:99],s[98:99],s[68:69]\n"
L_"v255,s[24:27],s62 offen offset:1024 sc1 lds\n"
M_"a[60:63],a[156:159],v[220:223],a[60:63]\n"
T_"a[156:157],v237 offset:9728\n"
R_"v160,a32\n"
M_"a[60:63],a[152:155],v[188:191],a[60:63]\n"
T_"a[152:153],v237 offset:1536\n"
R_"v161,a33\n"
R_"v162,a34\n"
R_"v163,a35\n"
R_"v164,a48\n"
R_"v165,a49\n"
R_"v166,a50\n"
R_"v167,a51\n"
U_"v160,v160,v156\n"
U_"v161,v161,v156\n"
U_"v162,v162,v156\n"
U_"v163,v163,v156\n"
U_"v164,v164,v156\n"
U_"v165,v165,v156\n"
U_"v166,v166,v156\n"
U_"v167,v167,v156\n"
C_"v80,v160,v161\n"
C_"v81,v162,v163\n"
C_"v82,v164,v165\n"
C_"v83,v166,v167\n"
O_"v[80:83],v255,s[52:55],0 offen offset:1024 sc1\n"
"s_nop 0\n"
M_"a[180:183],a[176:179],v[80:83],a[180:183]\n"
W_"15)\n"
T_"a[154:155],v237 offset:5632\n"
R_"v168,a36\n"
M_"a[64:67],a[128:131],v[176:179],a[64:67]\n"
W_"15)\n"
T_"a[158:159],v237 offset:13824\n"
R_"v169,a37\n"
M_"a[64:67],a[132:135],v[208:211],a[64:67]\n"
"buffer_load_dwordx2 a[240:241],v250,s[36:39],s63 offen sc1\n"
M_"a[68:71],a[132:135],v[212:215],a[68:71]\n"
L_"v255,s[24:27],s62 offen offset:2048 sc1 lds\n"
M_"a[68:71],a[128:131],v[180:183],a[68:71]\n"
L_"v255,s[24:27],s62 offen offset:3072 sc1 lds\n"
M_"a[72:75],a[128:131],v[184:187],a[72:75]\n"
A_"m0,s35,81920\n"
"buffer_load_dwordx2 a[242:243],v250,s[36:39],s63 offen offset:512 sc1\n"
M_"a[72:75],a[132:135],v[216:219],a[72:75]\n"
A_"s62,s34,303104\n"
R_"v170,a38\n"
R_"v171,a39\n"
M_"a[76:79],a[132:135],v[220:223],a[76:79]\n"
B_"a[132:135],v255 offset:10240\n"
R_"v172,a52\n"
M_"a[76:79],a[128:131],v[188:191],a[76:79]\n"
W_"12)\n"
B_"a[128:131],v255 offset:1024\n"
R_"v173,a53\n"
M_"a[80:83],a[136:139],v[176:179],a[80:83]\n"
W_"12)\n"
L_"v255,s[24:27],s62 offen sc1 lds\n"
M_"a[80:83],a[140:143],v[208:211],a[80:83]\n"
A_"m0,s35,118784\n"
L_"a[244:247],v255,s[36:39],s63 offen offset:1024 sc1\n"
M_"a[84:87],a[140:143],v[212:215],a[84:87]\n"
A_"s62,s34,450560\n"
R_"v174,a54\n"
R_"v175,a55\n"
M_"a[84:87],a[136:139],v[180:183],a[84:87]\n"
L_"v255,s[24:27],s62 offen sc1 lds\n"
M_"a[88:91],a[136:139],v[184:187],a[88:91]\n"
A_"m0,s35,0\n"
U_"v168,v168,v157\n"
U_"v169,v169,v157\n"
M_"a[88:91],a[140:143],v[216:219],a[88:91]\n"
A_"s62,s34,589824\n"
U_"v170,v170,v157\n"
U_"v171,v171,v157\n"
M_"a[92:95],a[140:143],v[220:223],a[92:95]\n"
B_"a[140:143],v255 offset:28672\n"
U_"v172,v172,v157\n"
M_"a[92:95],a[136:139],v[188:191],a[92:95]\n"
W_"8)\n"
B_"a[136:139],v255 offset:19456\n"
U_"v173,v173,v157\n"
M_"a[96:99],a[144:147],v[176:179],a[96:99]\n"
W_"8)\n"
U_"v174,v174,v157\n"
U_"v175,v175,v157\n"
M_"a[96:99],a[148:151],v[208:211],a[96:99]\n"
L_"a[248:251],v255,s[36:39],s63 offen offset:2048 sc1\n"
M_"a[100:103],a[148:151],v[212:215],a[100:103]\n"
C_"v84,v168,v169\n"
C_"v85,v170,v171\n"
M_"a[100:103],a[144:147],v[180:183],a[100:103]\n"
C_"v86,v172,v173\n"
C_"v87,v174,v175\n"
M_"a[104:107],a[144:147],v[184:187],a[104:107]\n"
R_"v128,a40\n"
R_"v129,a41\n"
M_"a[104:107],a[148:151],v[216:219],a[104:107]\n"
O_"v[84:87],v255,s[52:55],s91 offen offset:1024 sc1\n"
M_"a[108:111],a[148:151],v[220:223],a[108:111]\n"
B_"a[148:151],v255 offset:11264\n"
R_"v130,a42\n"
M_"a[108:111],a[144:147],v[188:191],a[108:111]\n"
W_"6)\n"
B_"a[144:147],v255 offset:2048\n"
R_"v131,a43\n"
M_"a[112:115],a[152:155],v[176:179],a[112:115]\n"
W_"6)\n"
R_"v132,a56\n"
R_"v133,a57\n"
M_"a[112:115],a[156:159],v[208:211],a[112:115]\n"
L_"a[252:255],v255,s[36:39],s63 offen offset:3072 sc1\n"
M_"a[116:119],a[156:159],v[212:215],a[116:119]\n"
S_"s63,8192\n"
R_"v134,a58\n"
R_"v135,a59\n"
M_"a[116:119],a[152:155],v[180:183],a[116:119]\n"
U_"v128,v128,v158\n"
U_"v129,v129,v158\n"
M_"a[120:123],a[152:155],v[184:187],a[120:123]\n"
U_"v130,v130,v158\n"
U_"v131,v131,v158\n"
M_"a[120:123],a[156:159],v[216:219],a[120:123]\n"
U_"v132,v132,v158\n"
U_"v133,v133,v158\n"
M_"a[124:127],a[156:159],v[220:223],a[124:127]\n"
B_"a[156:159],v255 offset:29696\n"
U_"v134,v134,v158\n"
M_"a[124:127],a[152:155],v[188:191],a[124:127]\n"
"s_waitcnt vmcnt(33)\n"
B_"a[152:155],v255 offset:20480\n"
U_"v135,v135,v158\n"
M_"a[0:3],a[160:163],a[192:195],0\n"
C_"v88,v128,v129\n"
C_"v89,v130,v131\n"
M_"a[180:183],a[176:179],v[84:87],a[180:183]\n"
"s_waitcnt vmcnt(32)\n"
C_"v90,v132,v133\n"
C_"v91,v134,v135\n"
M_"a[0:3],a[164:167],a[196:199],a[0:3]\n"
"s_waitcnt vmcnt(31)\n"
O_"v[88:91],v255,s[52:55],s92 offen offset:1024 sc1\n"
M_"a[0:3],a[168:171],a[200:203],a[0:3]\n"
"s_waitcnt vmcnt(31)\n"
R_"v136,a44\n"
R_"v137,a45\n"
M_"a[0:3],a[172:175],a[204:207],a[0:3]\n"
"s_waitcnt vmcnt(27)\n"
R_"v138,a46\n"
R_"v139,a47\n"
M_"a[4:7],a[172:175],a[220:223],0\n"
B_"a[172:175],v255 offset:30720\n"
R_"v140,a60\n"
M_"a[4:7],a[168:171],a[216:219],a[4:7]\n"
B_"a[168:171],v255 offset:21504\n"
R_"v141,a61\n"
M_"a[4:7],a[164:167],a[212:215],a[4:7]\n"
B_"a[164:167],v255 offset:12288\n"
R_"v142,a62\n"
M_"a[4:7],a[160:163],a[208:211],a[4:7]\n"
W_"9)\n"
B_"a[160:163],v255 offset:3072\n"
R_"v143,a63\n"
M_"a[8:11],a[128:131],a[192:195],0\n"
U_"v136,v136,v159\n"
U_"v137,v137,v159\n"
M_"a[8:11],a[132:135],a[196:199],a[8:11]\n"
W_"8)\n"
U_"v138,v138,v159\n"
U_"v139,v139,v159\n"
M_"a[8:11],a[136:139],a[200:203],a[8:11]\n"
U_"v140,v140,v159\n"
U_"v141,v141,v159\n"
M_"a[8:11],a[140:143],a[204:207],a[8:11]\n"
U_"v142,v142,v159\n"
U_"v143,v143,v159\n"
M_"a[12:15],a[140:143],a[220:223],0\n"
B_"a[140:143],v255 offset:31744\n"
C_"v92,v136,v137\n"
M_"a[180:183],a[176:179],v[88:91],a[180:183]\n"
C_"v93,v138,v139\n"
C_"v94,v140,v141\n"
M_"a[12:15],a[136:139],a[216:219],a[12:15]\n"
B_"a[136:139],v255 offset:22528\n"
C_"v95,v142,v143\n"
M_"a[12:15],a[132:135],a[212:215],a[12:15]\n"
B_"a[132:135],v255 offset:13312\n"
R_"v192,a64\n"
M_"a[12:15],a[128:131],a[208:211],a[12:15]\n"
W_"9)\n"
B_"a[128:131],v255 offset:4096\n"
R_"v193,a65\n"
M_"a[16:19],a[144:147],a[192:195],0\n"
O_"v[92:95],v255,s[52:55],s93 offen offset:1024 sc1\n"
M_"a[16:19],a[148:151],a[196:199],a[16:19]\n"
W_"8)\n"
R_"v194,a66\n"
R_"v195,a67\n"
M_"a[16:19],a[152:155],a[200:203],a[16:19]\n"
R_"v196,a80\n"
R_"v197,a81\n"
M_"a[16:19],a[156:159],a[204:207],a[16:19]\n"
R_"v198,a82\n"
R_"v199,a83\n"
M_"a[20:23],a[156:159],a[220:223],0\n"
B_"a[156:159],v255 offset:32768\n"
U_"v192,v192,v156\n"
M_"a[20:23],a[152:155],a[216:219],a[20:23]\n"
B_"a[152:155],v255 offset:23552\n"
U_"v193,v193,v156\n"
M_"a[20:23],a[148:151],a[212:215],a[20:23]\n"
B_"a[148:151],v255 offset:14336\n"
U_"v194,v194,v156\n"
M_"a[20:23],a[144:147],a[208:211],a[20:23]\n"
W_"7)\n"
B_"a[144:147],v255 offset:5120\n"
U_"v195,v195,v156\n"
M_"a[24:27],a[160:163],a[192:195],0\n"
U_"v196,v196,v156\n"
U_"v197,v197,v156\n"
M_"a[180:183],a[176:179],v[92:95],a[180:183]\n"
U_"v198,v198,v156\n"
U_"v199,v199,v156\n"
M_"a[24:27],a[164:167],a[196:199],a[24:27]\n"
C_"v96,v192,v193\n"
C_"v97,v194,v195\n"
M_"a[24:27],a[168:171],a[200:203],a[24:27]\n"
C_"v98,v196,v197\n"
C_"v99,v198,v199\n"
M_"a[24:27],a[172:175],a[204:207],a[24:27]\n"
O_"v[96:99],v255,s[52:55],0 offen offset:2048 sc1\n"
M_"a[28:31],a[172:175],a[220:223],0\n"
B_"a[172:175],v255 offset:33792\n"
R_"v200,a68\n"
M_"a[28:31],a[168:171],a[216:219],a[28:31]\n"
B_"a[168:171],v255 offset:24576\n"
R_"v201,a69\n"
M_"a[28:31],a[164:167],a[212:215],a[28:31]\n"
B_"a[164:167],v255 offset:15360\n"
R_"v202,a70\n"
M_"a[28:31],a[160:163],a[208:211],a[28:31]\n"
W_"7)\n"
B_"a[160:163],v255 offset:6144\n"
R_"v203,a71\n"
M_"a[32:35],a[128:131],a[192:195],0\n"
R_"v204,a84\n"
R_"v205,a85\n"
M_"a[32:35],a[132:135],a[196:199],a[32:35]\n"
R_"v206,a86\n"
R_"v207,a87\n"
M_"a[32:35],a[136:139],a[200:203],a[32:35]\n"
U_"v200,v200,v157\n"
U_"v201,v201,v157\n"
M_"a[32:35],a[140:143],a[204:207],a[32:35]\n"
U_"v202,v202,v157\n"
U_"v203,v203,v157\n"
M_"a[36:39],a[140:143],a[220:223],0\n"
B_"a[140:143],v255 offset:34816\n"
U_"v204,v204,v157\n"
M_"a[180:183],a[176:179],v[96:99],a[180:183]\n"
U_"v205,v205,v157\n"
U_"v206,v206,v157\n"
M_"a[36:39],a[136:139],a[216:219],a[36:39]\n"
B_"a[136:139],v255 offset:25600\n"
U_"v207,v207,v157\n"
M_"a[36:39],a[132:135],a[212:215],a[36:39]\n"
B_"a[132:135],v255 offset:16384\n"
C_"v100,v200,v201\n"
M_"a[36:39],a[128:131],a[208:211],a[36:39]\n"
W_"7)\n"
B_"a[128:131],v255 offset:7168\n"
C_"v101,v202,v203\n"
M_"a[40:43],a[144:147],a[192:195],0\n"
C_"v102,v204,v205\n"
C_"v103,v206,v207\n"
M_"a[40:43],a[148:151],a[196:199],a[40:43]\n"
O_"v[100:103],v255,s[52:55],s91 offen offset:2048 sc1\n"
M_"a[40:43],a[152:155],a[200:203],a[40:43]\n"
R_"v160,a72\n"
R_"v161,a73\n"
M_"a[40:43],a[156:159],a[204:207],a[40:43]\n"
R_"v162,a74\n"
R_"v163,a75\n"
M_"a[44:47],a[156:159],a[220:223],0\n"
B_"a[156:159],v255 offset:35840\n"
R_"v164,a88\n"
M_"a[44:47],a[152:155],a[216:219],a[44:47]\n"
B_"a[152:155],v255 offset:26624\n"
R_"v165,a89\n"
M_"a[44:47],a[148:151],a[212:215],a[44:47]\n"
B_"a[148:151],v255 offset:17408\n"
R_"v166,a90\n"
M_"a[44:47],a[144:147],a[208:211],a[44:47]\n"
W_"7)\n"
B_"a[144:147],v255 offset:8192\n"
R_"v167,a91\n"
M_"a[48:51],a[160:163],a[192:195],0\n"
U_"v160,v160,v158\n"
U_"v161,v161,v158\n"
M_"a[180:183],a[176:179],v[100:103],a[180:183]\n"
U_"v162,v162,v158\n"
U_"v163,v163,v158\n"
M_"a[48:51],a[164:167],a[196:199],a[48:51]\n"
U_"v164,v164,v158\n"
U_"v165,v165,v158\n"
M_"a[48:51],a[168:171],a[200:203],a[48:51]\n"
U_"v166,v166,v158\n"
U_"v167,v167,v158\n"
M_"a[48:51],a[172:175],a[204:207],a[48:51]\n"
C_"v104,v160,v161\n"
C_"v105,v162,v163\n"
M_"a[52:55],a[172:175],a[220:223],0\n"
C_"v106,v164,v165\n"
C_"v107,v166,v167\n"
M_"a[52:55],a[168:171],a[216:219],a[52:55]\n"
O_"v[104:107],v255,s[52:55],s92 offen offset:2048 sc1\n"
M_"a[52:55],a[164:167],a[212:215],a[52:55]\n"
L_"a[72:75],v255,s[36:39],s63 offen sc1\n"
M_"a[52:55],a[160:163],a[208:211],a[52:55]\n"
W_"4)\n"
R_"v168,a76\n"
R_"v169,a77\n"
M_"a[56:59],a[128:131],a[192:195],0\n"
R_"v170,a78\n"
R_"v171,a79\n"
M_"a[56:59],a[132:135],a[196:199],a[56:59]\n"
R_"v172,a92\n"
R_"v173,a93\n"
M_"a[56:59],a[136:139],a[200:203],a[56:59]\n"
W_"0)\n"
"s_barrier\n"
B_"a[160:163],v255 offset:36864\n"
M_"a[56:59],a[140:143],a[204:207],a[56:59]\n"
B_"a[164:167],v255 offset:46080\n"
R_"v174,a94\n"
M_"a[60:63],a[140:143],a[220:223],0\n"
B_"a[168:171],v255 offset:55296\n"
R_"v175,a95\n"
M_"a[180:183],a[176:179],v[104:107],a[180:183]\n"
B_"a[172:175],v255 offset:64512\n"
U_"v168,v168,v159\n"
M_"a[60:63],a[136:139],a[216:219],a[60:63]\n"
B_"a[136:139],v255 offset:56320\n"
U_"v169,v169,v159\n"
M_"a[60:63],a[132:135],a[212:215],a[60:63]\n"
B_"a[132:135],v255 offset:47104\n"
U_"v170,v170,v159\n"
M_"a[60:63],a[128:131],a[208:211],a[60:63]\n"
B_"a[128:131],v255 offset:37888\n"
U_"v171,v171,v159\n"
M_"a[64:67],a[144:147],a[192:195],0\n"
B_"a[140:143],v249 offset:0\n"
U_"v172,v172,v159\n"
M_"a[64:67],a[148:151],a[196:199],a[64:67]\n"
U_"v173,v173,v159\n"
U_"v174,v174,v159\n"
M_"a[64:67],a[152:155],a[200:203],a[64:67]\n"
U_"v175,v175,v159\n"
C_"v108,v168,v169\n"
M_"a[64:67],a[156:159],a[204:207],a[64:67]\n"
C_"v109,v170,v171\n"
C_"v110,v172,v173\n"
M_"a[68:71],a[156:159],a[220:223],0\n"
B_"a[156:159],v249 offset:1024\n"
C_"v111,v174,v175\n"
M_"a[68:71],a[152:155],a[216:219],a[68:71]\n"
B_"a[152:155],v255 offset:57344\n"
R_"v128,a96\n"
M_"a[68:71],a[148:151],a[212:215],a[68:71]\n"
B_"a[148:151],v255 offset:48128\n"
R_"v129,a97\n"
M_"a[68:71],a[144:147],a[208:211],a[68:71]\n"
"s_waitcnt vmcnt(28) lgkmcnt(10)\n"
B_"a[144:147],v255 offset:38912\n"
R_"v130,a98\n"
M_"a[0:3],a[160:163],a[224:227],a[0:3]\n"
O_"v[108:111],v255,s[52:55],s93 offen offset:2048 sc1\n"
M_"a[180:183],a[176:179],v[108:111],a[180:183]\n"
"s_waitcnt vmcnt(26) lgkmcnt(10)\n"
L_"a[76:79],v255,s[36:39],s63 offen offset:1024 sc1\n"
M_"a[0:3],a[164:167],a[228:231],a[0:3]\n"
"s_waitcnt vmcnt(23) lgkmcnt(9)\n"
L_"a[80:83],v255,s[36:39],s63 offen offset:2048 sc1\n"
M_"a[0:3],a[168:171],a[232:235],a[0:3]\n"
"s_waitcnt vmcnt(23) lgkmcnt(8)\n"
L_"a[84:87],v255,s[36:39],s63 offen offset:3072 sc1\n"
M_"a[0:3],a[172:175],a[236:239],a[0:3]\n"
"s_waitcnt vmcnt(10)\n"
R_"v131,a99\n"
R_"v132,a112\n"
M_"a[4:7],a[172:175],a[252:255],a[4:7]\n"
S_"s63,155648\n"
B_"a[172:175],v249 offset:2048\n"
R_"v133,a113\n"
M_"a[4:7],a[168:171],a[248:251],a[4:7]\n"
B_"a[168:171],v255 offset:58368\n"
R_"v134,a114\n"
M_"a[4:7],a[164:167],a[244:247],a[4:7]\n"
B_"a[164:167],v255 offset:49152\n"
R_"v135,a115\n"
M_"a[4:7],a[160:163],a[240:243],a[4:7]\n"
W_"8)\n"
B_"a[160:163],v255 offset:39936\n"
U_"v128,v128,v156\n"
M_"a[8:11],a[128:131],a[224:227],a[8:11]\n"
U_"v129,v129,v156\n"
U_"v130,v130,v156\n"
M_"a[8:11],a[132:135],a[228:231],a[8:11]\n"
U_"v131,v131,v156\n"
U_"v132,v132,v156\n"
M_"a[8:11],a[136:139],a[232:235],a[8:11]\n"
W_"8)\n"
U_"v133,v133,v156\n"
U_"v134,v134,v156\n"
M_"a[8:11],a[140:143],a[236:239],a[8:11]\n"
U_"v135,v135,v156\n"
C_"v112,v128,v129\n"
M_"a[12:15],a[140:143],a[252:255],a[12:15]\n"
B_"a[140:143],v249 offset:3072\n"
C_"v113,v130,v131\n"
C_"v114,v132,v133\n"
C_"v115,v134,v135\n"
O_"v[112:115],v255,s[52:55],0 offen offset:3072 sc1\n"
"s_nop 0\n"
M_"a[180:183],a[176:179],v[112:115],a[180:183]\n"
L_"a[88:91],v255,s[36:39],s63 offen sc1\n"
M_"a[12:15],a[136:139],a[248:251],a[12:15]\n"
B_"a[136:139],v255 offset:59392\n"
R_"v136,a100\n"
M_"a[12:15],a[132:135],a[244:247],a[12:15]\n"
B_"a[132:135],v255 offset:50176\n"
R_"v137,a101\n"
M_"a[12:15],a[128:131],a[240:243],a[12:15]\n"
W_"7)\n"
B_"a[128:131],v255 offset:40960\n"
R_"v138,a102\n"
M_"a[16:19],a[144:147],a[224:227],a[16:19]\n"
L_"a[92:95],v255,s[36:39],s63 offen offset:1024 sc1\n"
M_"a[16:19],a[148:151],a[228:231],a[16:19]\n"
L_"a[184:187],v255,s[36:39],s63 offen offset:2048 sc1\n"
M_"a[16:19],a[152:155],a[232:235],a[16:19]\n"
R_"v139,a103\n"
R_"v140,a116\n"
M_"a[16:19],a[156:159],a[236:239],a[16:19]\n"
R_"v141,a117\n"
R_"v142,a118\n"
M_"a[20:23],a[156:159],a[252:255],a[20:23]\n"
B_"a[156:159],v249 offset:4096\n"
R_"v143,a119\n"
M_"a[20:23],a[152:155],a[248:251],a[20:23]\n"
B_"a[152:155],v255 offset:60416\n"
U_"v136,v136,v157\n"
M_"a[20:23],a[148:151],a[244:247],a[20:23]\n"
B_"a[148:151],v255 offset:51200\n"
U_"v137,v137,v157\n"
M_"a[20:23],a[144:147],a[240:243],a[20:23]\n"
W_"7)\n"
B_"a[144:147],v255 offset:41984\n"
U_"v138,v138,v157\n"
M_"a[24:27],a[160:163],a[224:227],a[24:27]\n"
L_"a[188:191],v255,s[36:39],s63 offen offset:3072 sc1\n"
M_"a[24:27],a[164:167],a[228:231],a[24:27]\n"
U_"v139,v139,v157\n"
U_"v140,v140,v157\n"
M_"a[24:27],a[168:171],a[232:235],a[24:27]\n"
U_"v141,v141,v157\n"
U_"v142,v142,v157\n"
M_"a[24:27],a[172:175],a[236:239],a[24:27]\n"
U_"v143,v143,v157\n"
C_"v116,v136,v137\n"
M_"a[28:31],a[172:175],a[252:255],a[28:31]\n"
B_"a[172:175],v249 offset:5120\n"
C_"v117,v138,v139\n"
M_"a[28:31],a[168:171],a[248:251],a[28:31]\n"
B_"a[168:171],v255 offset:61440\n"
C_"v118,v140,v141\n"
M_"a[28:31],a[164:167],a[244:247],a[28:31]\n"
B_"a[164:167],v255 offset:52224\n"
C_"v119,v142,v143\n"
M_"a[28:31],a[160:163],a[240:243],a[28:31]\n"
W_"7)\n"
B_"a[160:163],v255 offset:43008\n"
R_"v192,a104\n"
M_"a[32:35],a[128:131],a[224:227],a[32:35]\n"
O_"v[116:119],v255,s[52:55],s91 offen offset:3072 sc1\n"
M_"a[32:35],a[132:135],a[228:231],a[32:35]\n"
L_"v255,s[24:27],s62 offen sc1 lds\n"
M_"a[32:35],a[136:139],a[232:235],a[32:35]\n"
L_"v255,s[24:27],s62 offen offset:1024 sc1 lds\n"
M_"a[32:35],a[140:143],a[236:239],a[32:35]\n"
R_"v193,a105\n"
R_"v194,a106\n"
M_"a[36:39],a[140:143],a[252:255],a[36:39]\n"
B_"a[140:143],v249 offset:6144\n"
R_"v195,a107\n"
M_"a[36:39],a[136:139],a[248:251],a[36:39]\n"
B_"a[136:139],v255 offset:62464\n"
R_"v196,a120\n"
M_"a[36:39],a[132:135],a[244:247],a[36:39]\n"
B_"a[132:135],v255 offset:53248\n"
R_"v197,a121\n"
M_"a[36:39],a[128:131],a[240:243],a[36:39]\n"
W_"7)\n"
B_"a[128:131],v255 offset:44032\n"
R_"v198,a122\n"
M_"a[40:43],a[144:147],a[224:227],a[40:43]\n"
R_"v199,a123\n"
U_"v192,v192,v158\n"
M_"a[180:183],a[176:179],v[116:119],a[180:183]\n"
L_"v255,s[24:27],s62 offen offset:2048 sc1 lds\n"
U_"v193,v193,v158\n"
U_"v194,v194,v158\n"
U_"v195,v195,v158\n"
U_"v196,v196,v158\n"
U_"v197,v197,v158\n"
U_"v198,v198,v158\n"
U_"v199,v199,v158\n"
C_"v120,v192,v193\n"
C_"v121,v194,v195\n"
C_"v122,v196,v197\n"
C_"v123,v198,v199\n"
O_"v[120:123],v255,s[52:55],s92 offen offset:3072 sc1\n"
"s_nop 0\n"
M_"a[180:183],a[176:179],v[120:123],a[180:183]\n"
L_"v255,s[24:27],s62 offen offset:3072 sc1 lds\n"
R_"v200,a108\n"
R_"v201,a109\n"
R_"v202,a110\n"
R_"v203,a111\n"
R_"v204,a124\n"
R_"v205,a125\n"
R_"v206,a126\n"
R_"v207,a127\n"
U_"v200,v200,v159\n"
U_"v201,v201,v159\n"
U_"v202,v202,v159\n"
U_"v203,v203,v159\n"
U_"v204,v204,v159\n"
U_"v205,v205,v159\n"
U_"v206,v206,v159\n"
U_"v207,v207,v159\n"
C_"v124,v200,v201\n"
C_"v125,v202,v203\n"
C_"v126,v204,v205\n"
C_"v127,v206,v207\n"
O_"v[124:127],v255,s[52:55],s93 offen offset:3072 sc1\n"
"s_nop 0\n"
M_"a[180:183],a[176:179],v[124:127],a[180:183]\n"
A_"m0,s35,4096\n"
A_"s62,s34,593920\n"
L_"v255,s[24:27],s62 offen sc1 lds\n"
L_"v255,s[24:27],s62 offen offset:1024 sc1 lds\n"
L_"v255,s[24:27],s62 offen offset:2048 sc1 lds\n"
L_"v255,s[24:27],s62 offen offset:3072 sc1 lds\n"
A_"m0,s35,8192\n"
A_"s62,s34,598016\n"
L_"v255,s[24:27],s62 offen sc1 lds\n"
R_"v253,a180\n"
"v_cmp_class_f32 s[68:69],v253,v254\n"
"s_nop 3\n"
"s_or_b64 s[98:99],s[98:99],s[68:69]\n"
"s_cbranch_scc1 FALLBACK_%=\n"
"REJOIN_%=:\n"
M_"a[40:43],a[148:151],a[228:231],a[40:43]\n"
S_"s63,12288\n"
"v_min_u32 v252,3,v251\n"
"v_bfe_u32 v234,v255,6,2\n"
M_"a[40:43],a[152:155],a[232:235],a[40:43]\n"
A_"m0,s35,36864\n"
L_"v[32:35],v255,s[36:39],s63 offen sc1\n"
M_"a[40:43],a[156:159],a[236:239],a[40:43]\n"
A_"s62,s34,737280\n"
"v_min_u32 v251,7,v251\n"
"v_lshlrev_b32 v252,3,v252\n"
M_"a[44:47],a[156:159],a[252:255],a[44:47]\n"
"s_min_u32 s88,s70,1\n"
B_"a[156:159],v249 offset:7168\n"
"v_lshlrev_b32 v251,3,v251\n"
M_"a[44:47],a[152:155],a[248:251],a[44:47]\n"
A_"s61,s61,s88\n"
B_"a[152:155],v255 offset:63488\n"
"v_lshrrev_b32 v235,8,v255\n"
M_"a[44:47],a[148:151],a[244:247],a[44:47]\n"
"s_lshl_b32 s88,s76,2\n"
B_"a[148:151],v255 offset:54272\n"
"v_xor_b32 v234,v234,v235\n"
M_"a[44:47],a[144:147],a[240:243],a[44:47]\n"
W_"7)\n"
B_"a[144:147],v255 offset:45056\n"
"v_bfe_u32 v235,v255,4,1\n"
M_"a[48:51],a[160:163],a[224:227],a[48:51]\n"
A_"s88,s88,s33\n"
"buffer_load_dwordx2 v[36:37],v250,s[36:39],s63 offen offset:1024 sc1\n"
M_"a[48:51],a[164:167],a[228:231],a[48:51]\n"
"s_mul_i32 s88,s88,36864\n"
"buffer_load_dwordx2 v[38:39],v250,s[36:39],s63 offen offset:1536 sc1\n"
M_"a[48:51],a[168:171],a[232:235],a[48:51]\n"
"s_mul_i32 s89,s77,288\n"
L_"v[40:43],v255,s[36:39],s63 offen offset:2048 sc1\n"
M_"a[48:51],a[172:175],a[236:239],a[48:51]\n"
A_"s88,s88,s89\n"
"v_min_u32 v234,1,v234\n"
"v_lshlrev_b32 v235,4,v235\n"
M_"a[52:55],a[172:175],a[252:255],a[52:55]\n"
"s_lshl_b32 s89,s61,5\n"
"v_mov_b32 v238,0x3f80\n"
"v_add_u32 v234,-1,v234\n"
M_"a[52:55],a[168:171],a[248:251],a[52:55]\n"
A_"s88,s88,s89\n"
L_"v[44:47],v255,s[36:39],s63 offen offset:3072 sc1\n"
M_"a[52:55],a[164:167],a[244:247],a[52:55]\n"
S_"s63,159744\n"
"v_lshlrev_b32 v235,v235,v238\n"
"v_mov_b32 v239,0\n"
M_"a[52:55],a[160:163],a[240:243],a[52:55]\n"
W_"4)\n"
"v_and_b32 v234,v234,v235\n"
"v_mov_b32 v238,0\n"
M_"a[56:59],a[128:131],a[224:227],a[56:59]\n"
"s_lshr_b32 s85,s76,1\n"
L_"v[48:51],v255,s[36:39],s63 offen sc1\n"
M_"a[56:59],a[132:135],a[228:231],a[56:59]\n"
"s_lshl_b32 s85,s85,8\n"
"v_bfe_u32 v235,v255,5,1\n"
"v_mov_b32 v253,0xc0be28b1\n"
M_"a[56:59],a[136:139],a[232:235],a[56:59]\n"
W_"0)\n"
"s_barrier\n"
B_"a[160:163],v249 offset:8192\n"
M_"a[56:59],a[140:143],a[236:239],a[56:59]\n"
"s_lshl_b32 s61,s61,10\n"
B_"a[164:167],v249 offset:17408\n"
"v_add_u32 v235,-1,v235\n"
M_"a[60:63],a[140:143],a[252:255],a[60:63]\n"
A_"s87,s61,2048\n"
B_"a[168:171],v249 offset:26624\n"
"v_and_b32 v236,v234,v235\n"
M_"a[60:63],a[136:139],a[248:251],a[60:63]\n"
"s_sub_u32 s86,s87,9216\n"
B_"a[172:175],v249 offset:35840\n"
"v_xor_b32 v237,v236,v234\n"
M_"a[60:63],a[132:135],a[244:247],a[60:63]\n"
"s_mul_i32 s84,s70,9216\n"
B_"a[132:135],v249 offset:18432\n"
"v_mov_b32 v234,0\n"
M_"a[60:63],a[128:131],a[240:243],a[60:63]\n"
"s_min_u32 s87,s87,s86\n"
B_"a[128:131],v249 offset:9216\n"
"v_mov_b32 v235,0\n"
M_"a[64:67],a[144:147],a[224:227],a[64:67]\n"
A_"s87,s87,s84\n"
B_"a[136:139],v249 offset:27648\n"
M_"a[64:67],a[148:151],a[228:231],a[64:67]\n"
S_"s42,35808\n"
B_"a[140:143],v249 offset:36864\n"
M_"a[64:67],a[152:155],a[232:235],a[64:67]\n"
S_"s43,0x20000\n"
"buffer_load_dwordx2 v[52:53],v250,s[36:39],s63 offen offset:1024 sc1\n"
M_"a[64:67],a[156:159],a[236:239],a[64:67]\n"
S_"s46,35808\n"
"buffer_load_dwordx2 v[54:55],v250,s[36:39],s63 offen offset:1536 sc1\n"
M_"a[68:71],a[156:159],a[252:255],a[68:71]\n"
S_"s47,0x20000\n"
B_"a[156:159],v249 offset:37888\n"
M_"a[68:71],a[152:155],a[248:251],a[68:71]\n"
B_"a[152:155],v249 offset:28672\n"
M_"a[68:71],a[148:151],a[244:247],a[68:71]\n"
B_"a[148:151],v249 offset:19456\n"
M_"a[68:71],a[144:147],a[240:243],a[68:71]\n"
"s_waitcnt vmcnt(29) lgkmcnt(10)\n"
B_"a[144:147],v249 offset:10240\n"
M_"a[0:3],a[160:163],a[72:75],a[0:3]\n"
"s_waitcnt vmcnt(27) lgkmcnt(10)\n"
L_"v[56:59],v255,s[36:39],s63 offen offset:2048 sc1\n"
M_"a[0:3],a[164:167],a[76:79],a[0:3]\n"
"s_waitcnt vmcnt(27) lgkmcnt(9)\n"
L_"v[60:63],v255,s[36:39],s63 offen offset:3072 sc1\n"
M_"a[0:3],a[168:171],a[80:83],a[0:3]\n"
"s_waitcnt vmcnt(27) lgkmcnt(8)\n"
L_"v255,s[24:27],s62 offen sc1 lds\n"
M_"a[0:3],a[172:175],a[84:87],a[0:3]\n"
"s_waitcnt vmcnt(23)\n"
L_"v255,s[24:27],s62 offen offset:1024 sc1 lds\n"
M_"a[4:7],a[172:175],a[188:191],a[4:7]\n"
S_"s63,16384\n"
B_"a[172:175],v249 offset:38912\n"
M_"a[4:7],a[168:171],a[184:187],a[4:7]\n"
B_"a[168:171],v249 offset:29696\n"
M_"a[4:7],a[164:167],a[92:95],a[4:7]\n"
B_"a[164:167],v249 offset:20480\n"
M_"a[4:7],a[160:163],a[88:91],a[4:7]\n"
W_"9)\n"
B_"a[160:163],v249 offset:11264\n"
M_"a[8:11],a[128:131],a[72:75],a[8:11]\n"
L_"v255,s[24:27],s62 offen offset:2048 sc1 lds\n"
M_"a[8:11],a[132:135],a[76:79],a[8:11]\n"
W_"9)\n"
L_"v255,s[24:27],s62 offen offset:3072 sc1 lds\n"
M_"a[8:11],a[136:139],a[80:83],a[8:11]\n"
W_"8)\n"
A_"m0,s35,40960\n"
A_"s62,s34,741376\n"
M_"a[8:11],a[140:143],a[84:87],a[8:11]\n"
L_"v255,s[24:27],s62 offen sc1 lds\n"
M_"a[12:15],a[140:143],a[188:191],a[12:15]\n"
B_"a[140:143],v249 offset:39936\n"
M_"a[12:15],a[136:139],a[184:187],a[12:15]\n"
B_"a[136:139],v249 offset:30720\n"
M_"a[12:15],a[132:135],a[92:95],a[12:15]\n"
B_"a[132:135],v249 offset:21504\n"
M_"a[12:15],a[128:131],a[88:91],a[12:15]\n"
W_"7)\n"
B_"a[128:131],v249 offset:12288\n"
M_"a[16:19],a[144:147],a[72:75],a[16:19]\n"
L_"v255,s[24:27],s62 offen offset:1024 sc1 lds\n"
M_"a[16:19],a[148:151],a[76:79],a[16:19]\n"
L_"v255,s[24:27],s62 offen offset:2048 sc1 lds\n"
M_"a[16:19],a[152:155],a[80:83],a[16:19]\n"
L_"v255,s[24:27],s62 offen offset:3072 sc1 lds\n"
M_"a[16:19],a[156:159],a[84:87],a[16:19]\n"
A_"m0,s35,45056\n"
A_"s62,s34,745472\n"
M_"a[20:23],a[156:159],a[188:191],a[20:23]\n"
B_"a[156:159],v249 offset:40960\n"
M_"a[20:23],a[152:155],a[184:187],a[20:23]\n"
B_"a[152:155],v249 offset:31744\n"
M_"a[20:23],a[148:151],a[92:95],a[20:23]\n"
B_"a[148:151],v249 offset:22528\n"
M_"a[20:23],a[144:147],a[88:91],a[20:23]\n"
W_"7)\n"
B_"a[144:147],v249 offset:13312\n"
M_"a[24:27],a[160:163],a[72:75],a[24:27]\n"
L_"v255,s[24:27],s62 offen sc1 lds\n"
M_"a[24:27],a[164:167],a[76:79],a[24:27]\n"
A_"m0,s35,73728\n"
L_"v[136:139],v255,s[36:39],s63 offen sc1\n"
M_"a[24:27],a[168:171],a[80:83],a[24:27]\n"
A_"s62,s34,884736\n"
L_"v[140:143],v255,s[36:39],s63 offen offset:1024 sc1\n"
M_"a[24:27],a[172:175],a[84:87],a[24:27]\n"
L_"v[144:147],v255,s[36:39],s63 offen offset:2048 sc1\n"
M_"a[28:31],a[172:175],a[188:191],a[28:31]\n"
B_"a[172:175],v249 offset:41984\n"
M_"a[28:31],a[168:171],a[184:187],a[28:31]\n"
B_"a[168:171],v249 offset:32768\n"
M_"a[28:31],a[164:167],a[92:95],a[28:31]\n"
B_"a[164:167],v249 offset:23552\n"
M_"a[28:31],a[160:163],a[88:91],a[28:31]\n"
W_"7)\n"
B_"a[160:163],v249 offset:14336\n"
M_"a[32:35],a[128:131],a[72:75],a[32:35]\n"
L_"v[148:151],v255,s[36:39],s63 offen offset:3072 sc1\n"
M_"a[32:35],a[132:135],a[76:79],a[32:35]\n"
S_"s63,163840\n"
A_"s40,s64,s88\n"
"s_addc_u32 s41,s65,0\n"
M_"a[32:35],a[136:139],a[80:83],a[32:35]\n"
"s_and_b32 s41,s41,0xffff\n"
L_"v[152:155],v255,s[36:39],s63 offen sc1\n"
M_"a[32:35],a[140:143],a[84:87],a[32:35]\n"
L_"v[156:159],v255,s[36:39],s63 offen offset:1024 sc1\n"
M_"a[36:39],a[140:143],a[188:191],a[36:39]\n"
B_"a[140:143],v249 offset:43008\n"
M_"a[36:39],a[136:139],a[184:187],a[36:39]\n"
B_"a[136:139],v249 offset:33792\n"
M_"a[36:39],a[132:135],a[92:95],a[36:39]\n"
B_"a[132:135],v249 offset:24576\n"
M_"a[36:39],a[128:131],a[88:91],a[36:39]\n"
W_"7)\n"
B_"a[128:131],v249 offset:15360\n"
M_"a[40:43],a[144:147],a[72:75],a[40:43]\n"
L_"v[160:163],v255,s[36:39],s63 offen offset:2048 sc1\n"
M_"a[40:43],a[148:151],a[76:79],a[40:43]\n"
L_"v[164:167],v255,s[36:39],s63 offen offset:3072 sc1\n"
M_"a[40:43],a[152:155],a[80:83],a[40:43]\n"
S_"s63,20480\n"
A_"s44,s80,s88\n"
"s_addc_u32 s45,s81,0\n"
M_"a[40:43],a[156:159],a[84:87],a[40:43]\n"
"s_lshl_b32 s88,s70,6\n"
L_"a[192:195],v255,s[36:39],s63 offen sc1\n"
M_"a[44:47],a[156:159],a[188:191],a[44:47]\n"
A_"s85,s85,s88\n"
B_"a[156:159],v249 offset:44032\n"
M_"a[44:47],a[152:155],a[184:187],a[44:47]\n"
A_"s88,s74,12288\n"
B_"a[152:155],v249 offset:34816\n"
M_"a[44:47],a[148:151],a[92:95],a[44:47]\n"
A_"s85,s85,12288\n"
B_"a[148:151],v249 offset:25600\n"
M_"a[44:47],a[144:147],a[88:91],a[44:47]\n"
W_"7)\n"
B_"a[144:147],v249 offset:16384\n"
M_"a[48:51],a[160:163],a[72:75],a[48:51]\n"
"s_lshl_b32 s74,s70,3\n"
L_"a[196:199],v255,s[36:39],s63 offen offset:1024 sc1\n"
M_"a[48:51],a[164:167],a[76:79],a[48:51]\n"
A_"s74,s74,s71\n"
"buffer_load_dwordx2 a[200:201],v250,s[36:39],s63 offen offset:2048 sc1\n"
M_"a[48:51],a[168:171],a[80:83],a[48:51]\n"
"s_and_b32 s45,s45,0xffff\n"
"buffer_load_dwordx2 a[202:203],v250,s[36:39],s63 offen offset:2560 sc1\n"
M_"a[48:51],a[172:175],a[84:87],a[48:51]\n"
L_"a[204:207],v255,s[36:39],s63 offen offset:3072 sc1\n"
M_"a[52:55],a[172:175],a[188:191],a[52:55]\n"
S_"s63,167936\n"
"s_cmp_eq_u32 s70,0\n"
"s_cselect_b32 s60,0,1073741824\n"
M_"a[52:55],a[168:171],a[184:187],a[52:55]\n"
"s_xor_b32 s86,s60,1073741824\n"
L_"a[208:211],v255,s[36:39],s63 offen sc1\n"
M_"a[52:55],a[164:167],a[92:95],a[52:55]\n"
A_"s87,s87,s86\n"
L_"a[212:215],v255,s[36:39],s63 offen offset:1024 sc1\n"
M_"a[52:55],a[160:163],a[88:91],a[52:55]\n"
W_"4)\n"
"buffer_load_dwordx2 a[216:217],v250,s[36:39],s63 offen offset:2048 sc1\n"
M_"a[56:59],a[128:131],a[72:75],a[56:59]\n"
"buffer_load_dwordx2 a[218:219],v250,s[36:39],s63 offen offset:2560 sc1\n"
M_"a[56:59],a[132:135],a[76:79],a[56:59]\n"
L_"a[220:223],v255,s[36:39],s63 offen offset:3072 sc1\n"
M_"a[56:59],a[136:139],a[80:83],a[56:59]\n"
W_"0)\n"
"s_barrier\n"
B_"a[160:163],v249 offset:45056\n"
M_"a[56:59],a[140:143],a[84:87],a[56:59]\n"
S_"s63,24576\n"
B_"a[164:167],v249 offset:54272\n"
M_"a[60:63],a[140:143],a[188:191],a[60:63]\n"
B_"a[168:171],v249 offset:63488\n"
M_"a[60:63],a[136:139],a[184:187],a[60:63]\n"
B_"a[172:175],v242 offset:7168\n"
M_"a[60:63],a[132:135],a[92:95],a[60:63]\n"
B_"a[132:135],v249 offset:55296\n"
M_"a[60:63],a[128:131],a[88:91],a[60:63]\n"
B_"a[128:131],v249 offset:46080\n"
M_"a[64:67],a[144:147],a[72:75],a[64:67]\n"
B_"a[136:139],v249 offset:64512\n"
M_"a[64:67],a[148:151],a[76:79],a[64:67]\n"
B_"a[140:143],v242 offset:8192\n"
M_"a[64:67],a[152:155],a[80:83],a[64:67]\n"
M_"a[64:67],a[156:159],a[84:87],a[64:67]\n"
M_"a[68:71],a[156:159],a[188:191],a[68:71]\n"
B_"a[156:159],v242 offset:9216\n"
M_"a[68:71],a[152:155],a[184:187],a[68:71]\n"
B_"a[152:155],v242 offset:0\n"
M_"a[68:71],a[148:151],a[92:95],a[68:71]\n"
B_"a[148:151],v249 offset:56320\n"
M_"a[68:71],a[144:147],a[88:91],a[68:71]\n"
"s_waitcnt vmcnt(36) lgkmcnt(10)\n"
B_"a[144:147],v249 offset:47104\n"
M_"a[0:3],a[160:163],v[32:35],a[0:3]\n"
"s_waitcnt vmcnt(34) lgkmcnt(10)\n"
L_"v255,s[24:27],s62 offen sc1 lds\n"
M_"a[0:3],a[164:167],v[36:39],a[0:3]\n"
"s_waitcnt vmcnt(34) lgkmcnt(9)\n"
M_"a[0:3],a[168:171],v[40:43],a[0:3]\n"
"s_waitcnt vmcnt(33) lgkmcnt(8)\n"
M_"a[0:3],a[172:175],v[44:47],a[0:3]\n"
"s_waitcnt vmcnt(28)\n"
M_"a[4:7],a[172:175],v[60:63],a[4:7]\n"
B_"a[172:175],v242 offset:10240\n"
M_"a[4:7],a[168:171],v[56:59],a[4:7]\n"
B_"a[168:171],v242 offset:1024\n"
M_"a[4:7],a[164:167],v[52:55],a[4:7]\n"
B_"a[164:167],v249 offset:57344\n"
M_"a[4:7],a[160:163],v[48:51],a[4:7]\n"
W_"9)\n"
B_"a[160:163],v249 offset:48128\n"
M_"a[8:11],a[128:131],v[32:35],a[8:11]\n"
L_"v255,s[24:27],s62 offen offset:1024 sc1 lds\n"
M_"a[8:11],a[132:135],v[36:39],a[8:11]\n"
W_"9)\n"
L_"v255,s[24:27],s62 offen offset:2048 sc1 lds\n"
M_"a[8:11],a[136:139],v[40:43],a[8:11]\n"
W_"8)\n"
M_"a[8:11],a[140:143],v[44:47],a[8:11]\n"
M_"a[12:15],a[140:143],v[60:63],a[12:15]\n"
B_"a[140:143],v242 offset:11264\n"
M_"a[12:15],a[136:139],v[56:59],a[12:15]\n"
B_"a[136:139],v242 offset:2048\n"
M_"a[12:15],a[132:135],v[52:55],a[12:15]\n"
B_"a[132:135],v249 offset:58368\n"
M_"a[12:15],a[128:131],v[48:51],a[12:15]\n"
W_"7)\n"
B_"a[128:131],v249 offset:49152\n"
M_"a[16:19],a[144:147],v[32:35],a[16:19]\n"
L_"v255,s[24:27],s62 offen offset:3072 sc1 lds\n"
M_"a[16:19],a[148:151],v[36:39],a[16:19]\n"
A_"m0,s35,77824\n"
A_"s62,s34,888832\n"
M_"a[16:19],a[152:155],v[40:43],a[16:19]\n"
L_"v255,s[24:27],s62 offen sc1 lds\n"
M_"a[16:19],a[156:159],v[44:47],a[16:19]\n"
M_"a[20:23],a[156:159],v[60:63],a[20:23]\n"
B_"a[156:159],v242 offset:12288\n"
M_"a[20:23],a[152:155],v[56:59],a[20:23]\n"
B_"a[152:155],v242 offset:3072\n"
M_"a[20:23],a[148:151],v[52:55],a[20:23]\n"
B_"a[148:151],v249 offset:59392\n"
M_"a[20:23],a[144:147],v[48:51],a[20:23]\n"
W_"7)\n"
B_"a[144:147],v249 offset:50176\n"
M_"a[24:27],a[160:163],v[32:35],a[24:27]\n"
L_"v255,s[24:27],s62 offen offset:1024 sc1 lds\n"
M_"a[24:27],a[164:167],v[36:39],a[24:27]\n"
L_"v255,s[24:27],s62 offen offset:2048 sc1 lds\n"
M_"a[24:27],a[168:171],v[40:43],a[24:27]\n"
M_"a[24:27],a[172:175],v[44:47],a[24:27]\n"
M_"a[28:31],a[172:175],v[60:63],a[28:31]\n"
B_"a[172:175],v242 offset:13312\n"
M_"a[28:31],a[168:171],v[56:59],a[28:31]\n"
B_"a[168:171],v242 offset:4096\n"
M_"a[28:31],a[164:167],v[52:55],a[28:31]\n"
B_"a[164:167],v249 offset:60416\n"
M_"a[28:31],a[160:163],v[48:51],a[28:31]\n"
W_"7)\n"
B_"a[160:163],v249 offset:51200\n"
M_"a[32:35],a[128:131],v[32:35],a[32:35]\n"
L_"v255,s[24:27],s62 offen offset:3072 sc1 lds\n"
M_"a[32:35],a[132:135],v[36:39],a[32:35]\n"
A_"m0,s35,81920\n"
A_"s62,s34,892928\n"
M_"a[32:35],a[136:139],v[40:43],a[32:35]\n"
L_"v255,s[24:27],s62 offen sc1 lds\n"
M_"a[32:35],a[140:143],v[44:47],a[32:35]\n"
A_"m0,s35,110592\n"
A_"s62,s34,1032192\n"
M_"a[36:39],a[140:143],v[60:63],a[36:39]\n"
B_"a[140:143],v242 offset:14336\n"
M_"a[36:39],a[136:139],v[56:59],a[36:39]\n"
B_"a[136:139],v242 offset:5120\n"
M_"a[36:39],a[132:135],v[52:55],a[36:39]\n"
B_"a[132:135],v249 offset:61440\n"
M_"a[36:39],a[128:131],v[48:51],a[36:39]\n"
W_"7)\n"
B_"a[128:131],v249 offset:52224\n"
M_"a[40:43],a[144:147],v[32:35],a[40:43]\n"
L_"a[224:227],v255,s[36:39],s63 offen sc1\n"
M_"a[40:43],a[148:151],v[36:39],a[40:43]\n"
L_"a[228:231],v255,s[36:39],s63 offen offset:1024 sc1\n"
M_"a[40:43],a[152:155],v[40:43],a[40:43]\n"
M_"a[40:43],a[156:159],v[44:47],a[40:43]\n"
M_"a[44:47],a[156:159],v[60:63],a[44:47]\n"
B_"a[156:159],v242 offset:15360\n"
M_"a[44:47],a[152:155],v[56:59],a[44:47]\n"
B_"a[152:155],v242 offset:6144\n"
M_"a[44:47],a[148:151],v[52:55],a[44:47]\n"
B_"a[148:151],v249 offset:62464\n"
M_"a[44:47],a[144:147],v[48:51],a[44:47]\n"
W_"7)\n"
B_"a[144:147],v249 offset:53248\n"
M_"a[48:51],a[160:163],v[32:35],a[48:51]\n"
L_"a[232:235],v255,s[36:39],s63 offen offset:2048 sc1\n"
M_"a[48:51],a[164:167],v[36:39],a[48:51]\n"
L_"a[236:239],v255,s[36:39],s63 offen offset:3072 sc1\n"
M_"a[48:51],a[168:171],v[40:43],a[48:51]\n"
S_"s63,172032\n"
M_"a[48:51],a[172:175],v[44:47],a[48:51]\n"
M_"a[52:55],a[172:175],v[60:63],a[52:55]\n"
L_"a[240:243],v255,s[36:39],s63 offen sc1\n"
M_"a[52:55],a[168:171],v[56:59],a[52:55]\n"
M_"a[52:55],a[164:167],v[52:55],a[52:55]\n"
M_"a[52:55],a[160:163],v[48:51],a[52:55]\n"
W_"4)\n"
M_"a[56:59],a[128:131],v[32:35],a[56:59]\n"
L_"a[244:247],v255,s[36:39],s63 offen offset:1024 sc1\n"
M_"a[56:59],a[132:135],v[36:39],a[56:59]\n"
M_"a[56:59],a[136:139],v[40:43],a[56:59]\n"
W_"0)\n"
"s_barrier\n"
B_"a[160:163],v255 offset:0\n"
M_"a[56:59],a[140:143],v[44:47],a[56:59]\n"
B_"a[164:167],v255 offset:9216\n"
M_"a[60:63],a[140:143],v[60:63],a[60:63]\n"
B_"a[168:171],v255 offset:18432\n"
M_"a[60:63],a[136:139],v[56:59],a[60:63]\n"
B_"a[172:175],v255 offset:27648\n"
M_"a[60:63],a[132:135],v[52:55],a[60:63]\n"
B_"a[132:135],v255 offset:10240\n"
M_"a[60:63],a[128:131],v[48:51],a[60:63]\n"
B_"a[128:131],v255 offset:1024\n"
M_"a[64:67],a[144:147],v[32:35],a[64:67]\n"
B_"a[136:139],v255 offset:19456\n"
M_"a[64:67],a[148:151],v[36:39],a[64:67]\n"
B_"a[140:143],v255 offset:28672\n"
M_"a[64:67],a[152:155],v[40:43],a[64:67]\n"
"buffer_store_dwordx2 v[244:245],v243,s[56:59],s88 offen sc1\n"
M_"a[64:67],a[156:159],v[44:47],a[64:67]\n"
A_"s88,s70,1\n"
L_"a[248:251],v255,s[36:39],s63 offen offset:2048 sc1\n"
M_"a[68:71],a[156:159],v[60:63],a[68:71]\n"
"s_and_b32 s88,s88,3\n"
B_"a[156:159],v255 offset:29696\n"
M_"a[68:71],a[152:155],v[56:59],a[68:71]\n"
"s_mul_i32 s88,s88,9216\n"
B_"a[152:155],v255 offset:20480\n"
M_"a[68:71],a[148:151],v[52:55],a[68:71]\n"
A_"s90,s88,s61\n"
B_"a[148:151],v255 offset:11264\n"
M_"a[68:71],a[144:147],v[48:51],a[68:71]\n"
"s_waitcnt vmcnt(34) lgkmcnt(10)\n"
B_"a[144:147],v255 offset:2048\n"
M_"a[0:3],a[160:163],v[136:139],a[0:3]\n"
"s_waitcnt vmcnt(33) lgkmcnt(10)\n"
L_"a[252:255],v255,s[36:39],s63 offen offset:3072 sc1\n"
M_"a[0:3],a[164:167],v[140:143],a[0:3]\n"
"s_waitcnt vmcnt(33) lgkmcnt(9)\n"
L_"v255,s[24:27],s62 offen sc1 lds\n"
M_"a[0:3],a[168:171],v[144:147],a[0:3]\n"
"s_waitcnt vmcnt(33) lgkmcnt(8)\n"
S_"s63,28672\n"
A_"s88,s70,2\n"
M_"a[0:3],a[172:175],v[148:151],a[0:3]\n"
"s_waitcnt vmcnt(29)\n"
"s_and_b32 s88,s88,3\n"
A_"s93,s90,s60\n"
M_"a[4:7],a[172:175],v[164:167],a[4:7]\n"
"s_mul_i32 s88,s88,9216\n"
B_"a[172:175],v255 offset:30720\n"
M_"a[4:7],a[168:171],v[160:163],a[4:7]\n"
A_"s91,s88,s61\n"
B_"a[168:171],v255 offset:21504\n"
M_"a[4:7],a[164:167],v[156:159],a[4:7]\n"
A_"s88,s70,3\n"
B_"a[164:167],v255 offset:12288\n"
M_"a[4:7],a[160:163],v[152:155],a[4:7]\n"
W_"9)\n"
B_"a[160:163],v255 offset:3072\n"
M_"a[8:11],a[128:131],v[136:139],a[8:11]\n"
"s_and_b32 s88,s88,3\n"
L_"v255,s[24:27],s62 offen offset:1024 sc1 lds\n"
M_"a[8:11],a[132:135],v[140:143],a[8:11]\n"
W_"9)\n"
L_"v255,s[24:27],s62 offen offset:2048 sc1 lds\n"
M_"a[8:11],a[136:139],v[144:147],a[8:11]\n"
W_"8)\n"
"s_mul_i32 s88,s88,9216\n"
A_"s94,s91,s60\n"
M_"a[8:11],a[140:143],v[148:151],a[8:11]\n"
A_"s92,s88,s61\n"
M_"a[12:15],a[140:143],v[164:167],a[12:15]\n"
"s_mul_i32 s88,s70,147456\n"
B_"a[140:143],v255 offset:31744\n"
M_"a[12:15],a[136:139],v[160:163],a[12:15]\n"
A_"s95,s92,s60\n"
B_"a[136:139],v255 offset:22528\n"
M_"a[12:15],a[132:135],v[156:159],a[12:15]\n"
B_"a[132:135],v255 offset:13312\n"
M_"a[12:15],a[128:131],v[152:155],a[12:15]\n"
W_"7)\n"
B_"a[128:131],v255 offset:4096\n"
M_"a[16:19],a[144:147],v[136:139],a[16:19]\n"
L_"v255,s[24:27],s62 offen offset:3072 sc1 lds\n"
M_"a[16:19],a[148:151],v[140:143],a[16:19]\n"
A_"m0,s35,114688\n"
A_"s62,s34,1036288\n"
M_"a[16:19],a[152:155],v[144:147],a[16:19]\n"
L_"v255,s[24:27],s62 offen sc1 lds\n"
M_"a[16:19],a[156:159],v[148:151],a[16:19]\n"
M_"a[20:23],a[156:159],v[164:167],a[20:23]\n"
B_"a[156:159],v255 offset:32768\n"
M_"a[20:23],a[152:155],v[160:163],a[20:23]\n"
B_"a[152:155],v255 offset:23552\n"
M_"a[20:23],a[148:151],v[156:159],a[20:23]\n"
B_"a[148:151],v255 offset:14336\n"
M_"a[20:23],a[144:147],v[152:155],a[20:23]\n"
W_"7)\n"
B_"a[144:147],v255 offset:5120\n"
M_"a[24:27],a[160:163],v[136:139],a[24:27]\n"
L_"v255,s[24:27],s62 offen offset:1024 sc1 lds\n"
M_"a[24:27],a[164:167],v[140:143],a[24:27]\n"
L_"v255,s[24:27],s62 offen offset:2048 sc1 lds\n"
M_"a[24:27],a[168:171],v[144:147],a[24:27]\n"
M_"a[24:27],a[172:175],v[148:151],a[24:27]\n"
M_"a[28:31],a[172:175],v[164:167],a[28:31]\n"
B_"a[172:175],v255 offset:33792\n"
M_"a[28:31],a[168:171],v[160:163],a[28:31]\n"
B_"a[168:171],v255 offset:24576\n"
M_"a[28:31],a[164:167],v[156:159],a[28:31]\n"
B_"a[164:167],v255 offset:15360\n"
M_"a[28:31],a[160:163],v[152:155],a[28:31]\n"
W_"7)\n"
B_"a[160:163],v255 offset:6144\n"
M_"a[32:35],a[128:131],v[136:139],a[32:35]\n"
L_"v255,s[24:27],s62 offen offset:3072 sc1 lds\n"
M_"a[32:35],a[132:135],v[140:143],a[32:35]\n"
A_"m0,s35,118784\n"
A_"s62,s34,1040384\n"
M_"a[32:35],a[136:139],v[144:147],a[32:35]\n"
L_"v255,s[24:27],s62 offen sc1 lds\n"
M_"a[32:35],a[140:143],v[148:151],a[32:35]\n"
A_"m0,s35,0\n"
A_"s62,s34,1179648\n"
M_"a[36:39],a[140:143],v[164:167],a[36:39]\n"
B_"a[140:143],v255 offset:34816\n"
M_"a[36:39],a[136:139],v[160:163],a[36:39]\n"
B_"a[136:139],v255 offset:25600\n"
M_"a[36:39],a[132:135],v[156:159],a[36:39]\n"
B_"a[132:135],v255 offset:16384\n"
M_"a[36:39],a[128:131],v[152:155],a[36:39]\n"
W_"7)\n"
B_"a[128:131],v255 offset:7168\n"
M_"a[40:43],a[144:147],v[136:139],a[40:43]\n"
L_"v[0:3],v255,s[36:39],s63 offen sc1\n"
M_"a[40:43],a[148:151],v[140:143],a[40:43]\n"
L_"v[4:7],v255,s[36:39],s63 offen offset:1024 sc1\n"
M_"a[40:43],a[152:155],v[144:147],a[40:43]\n"
M_"a[40:43],a[156:159],v[148:151],a[40:43]\n"
M_"a[44:47],a[156:159],v[164:167],a[44:47]\n"
B_"a[156:159],v255 offset:35840\n"
M_"a[44:47],a[152:155],v[160:163],a[44:47]\n"
B_"a[152:155],v255 offset:26624\n"
M_"a[44:47],a[148:151],v[156:159],a[44:47]\n"
B_"a[148:151],v255 offset:17408\n"
M_"a[44:47],a[144:147],v[152:155],a[44:47]\n"
W_"7)\n"
B_"a[144:147],v255 offset:8192\n"
M_"a[48:51],a[160:163],v[136:139],a[48:51]\n"
L_"v[8:11],v255,s[36:39],s63 offen offset:2048 sc1\n"
M_"a[48:51],a[164:167],v[140:143],a[48:51]\n"
"buffer_load_dwordx2 v[12:13],v250,s[36:39],s63 offen offset:3072 sc1\n"
M_"a[48:51],a[168:171],v[144:147],a[48:51]\n"
M_"a[48:51],a[172:175],v[148:151],a[48:51]\n"
M_"a[52:55],a[172:175],v[164:167],a[52:55]\n"
"buffer_load_dwordx2 v[14:15],v250,s[36:39],s63 offen offset:3584 sc1\n"
M_"a[52:55],a[168:171],v[160:163],a[52:55]\n"
S_"s63,176128\n"
M_"a[52:55],a[164:167],v[156:159],a[52:55]\n"
M_"a[52:55],a[160:163],v[152:155],a[52:55]\n"
W_"4)\n"
M_"a[56:59],a[128:131],v[136:139],a[56:59]\n"
L_"v[16:19],v255,s[36:39],s63 offen sc1\n"
M_"a[56:59],a[132:135],v[140:143],a[56:59]\n"
M_"a[56:59],a[136:139],v[144:147],a[56:59]\n"
W_"0)\n"
"s_barrier\n"
B_"a[160:163],v255 offset:36864\n"
M_"a[56:59],a[140:143],v[148:151],a[56:59]\n"
B_"a[164:167],v255 offset:46080\n"
M_"a[60:63],a[140:143],v[164:167],a[60:63]\n"
B_"a[168:171],v255 offset:55296\n"
M_"a[60:63],a[136:139],v[160:163],a[60:63]\n"
B_"a[172:175],v255 offset:64512\n"
M_"a[60:63],a[132:135],v[156:159],a[60:63]\n"
B_"a[132:135],v255 offset:47104\n"
M_"a[60:63],a[128:131],v[152:155],a[60:63]\n"
B_"a[128:131],v255 offset:37888\n"
M_"a[64:67],a[144:147],v[136:139],a[64:67]\n"
B_"a[136:139],v255 offset:56320\n"
M_"a[64:67],a[148:151],v[140:143],a[64:67]\n"
B_"a[140:143],v249 offset:0\n"
M_"a[64:67],a[152:155],v[144:147],a[64:67]\n"
L_"v[20:23],v255,s[36:39],s63 offen offset:1024 sc1\n"
M_"a[64:67],a[156:159],v[148:151],a[64:67]\n"
L_"v[24:27],v255,s[36:39],s63 offen offset:2048 sc1\n"
M_"a[68:71],a[156:159],v[164:167],a[68:71]\n"
B_"a[156:159],v249 offset:1024\n"
M_"a[68:71],a[152:155],v[160:163],a[68:71]\n"
B_"a[152:155],v255 offset:57344\n"
M_"a[68:71],a[148:151],v[156:159],a[68:71]\n"
B_"a[148:151],v255 offset:48128\n"
M_"a[68:71],a[144:147],v[152:155],a[68:71]\n"
"s_waitcnt vmcnt(44) lgkmcnt(10)\n"
B_"a[144:147],v255 offset:38912\n"
M_"a[0:3],a[160:163],a[192:195],a[0:3]\n"
"s_waitcnt vmcnt(43) lgkmcnt(10)\n"
"buffer_load_dwordx2 v[28:29],v250,s[36:39],s63 offen offset:3072 sc1\n"
M_"a[0:3],a[164:167],a[196:199],a[0:3]\n"
"s_waitcnt vmcnt(42) lgkmcnt(9)\n"
"buffer_load_dwordx2 v[30:31],v250,s[36:39],s63 offen offset:3584 sc1\n"
M_"a[0:3],a[168:171],a[200:203],a[0:3]\n"
"s_waitcnt vmcnt(42) lgkmcnt(8)\n"
L_"v255,s[24:27],s62 offen sc1 lds\n"
M_"a[0:3],a[172:175],a[204:207],a[0:3]\n"
"s_waitcnt vmcnt(38)\n"
S_"s63,32768\n"
M_"a[4:7],a[172:175],a[220:223],a[4:7]\n"
B_"a[172:175],v249 offset:2048\n"
M_"a[4:7],a[168:171],a[216:219],a[4:7]\n"
B_"a[168:171],v255 offset:58368\n"
M_"a[4:7],a[164:167],a[212:215],a[4:7]\n"
B_"a[164:167],v255 offset:49152\n"
M_"a[4:7],a[160:163],a[208:211],a[4:7]\n"
W_"9)\n"
B_"a[160:163],v255 offset:39936\n"
M_"a[8:11],a[128:131],a[192:195],a[8:11]\n"
L_"v255,s[24:27],s62 offen offset:1024 sc1 lds\n"
M_"a[8:11],a[132:135],a[196:199],a[8:11]\n"
W_"9)\n"
L_"v255,s[24:27],s62 offen offset:2048 sc1 lds\n"
M_"a[8:11],a[136:139],a[200:203],a[8:11]\n"
W_"8)\n"
M_"a[8:11],a[140:143],a[204:207],a[8:11]\n"
M_"a[12:15],a[140:143],a[220:223],a[12:15]\n"
B_"a[140:143],v249 offset:3072\n"
M_"a[12:15],a[136:139],a[216:219],a[12:15]\n"
B_"a[136:139],v255 offset:59392\n"
M_"a[12:15],a[132:135],a[212:215],a[12:15]\n"
B_"a[132:135],v255 offset:50176\n"
M_"a[12:15],a[128:131],a[208:211],a[12:15]\n"
W_"7)\n"
B_"a[128:131],v255 offset:40960\n"
M_"a[16:19],a[144:147],a[192:195],a[16:19]\n"
L_"v255,s[24:27],s62 offen offset:3072 sc1 lds\n"
M_"a[16:19],a[148:151],a[196:199],a[16:19]\n"
A_"m0,s35,4096\n"
A_"s62,s34,1183744\n"
M_"a[16:19],a[152:155],a[200:203],a[16:19]\n"
L_"v255,s[24:27],s62 offen sc1 lds\n"
M_"a[16:19],a[156:159],a[204:207],a[16:19]\n"
M_"a[20:23],a[156:159],a[220:223],a[20:23]\n"
B_"a[156:159],v249 offset:4096\n"
M_"a[20:23],a[152:155],a[216:219],a[20:23]\n"
B_"a[152:155],v255 offset:60416\n"
M_"a[20:23],a[148:151],a[212:215],a[20:23]\n"
B_"a[148:151],v255 offset:51200\n"
M_"a[20:23],a[144:147],a[208:211],a[20:23]\n"
W_"7)\n"
B_"a[144:147],v255 offset:41984\n"
M_"a[24:27],a[160:163],a[192:195],a[24:27]\n"
L_"v255,s[24:27],s62 offen offset:1024 sc1 lds\n"
M_"a[24:27],a[164:167],a[196:199],a[24:27]\n"
L_"v255,s[24:27],s62 offen offset:2048 sc1 lds\n"
M_"a[24:27],a[168:171],a[200:203],a[24:27]\n"
M_"a[24:27],a[172:175],a[204:207],a[24:27]\n"
M_"a[28:31],a[172:175],a[220:223],a[28:31]\n"
B_"a[172:175],v249 offset:5120\n"
M_"a[28:31],a[168:171],a[216:219],a[28:31]\n"
B_"a[168:171],v255 offset:61440\n"
M_"a[28:31],a[164:167],a[212:215],a[28:31]\n"
B_"a[164:167],v255 offset:52224\n"
M_"a[28:31],a[160:163],a[208:211],a[28:31]\n"
W_"7)\n"
B_"a[160:163],v255 offset:43008\n"
M_"a[32:35],a[128:131],a[192:195],a[32:35]\n"
L_"v255,s[24:27],s62 offen offset:3072 sc1 lds\n"
M_"a[32:35],a[132:135],a[196:199],a[32:35]\n"
A_"m0,s35,8192\n"
A_"s62,s34,1187840\n"
M_"a[32:35],a[136:139],a[200:203],a[32:35]\n"
"s_sub_u32 s34,s34,s88\n"
L_"v255,s[24:27],s62 offen sc1 lds\n"
M_"a[32:35],a[140:143],a[204:207],a[32:35]\n"
A_"m0,s35,36864\n"
A_"s62,s34,5308416\n"
A_"s88,s74,24576\n"
M_"a[36:39],a[140:143],a[220:223],a[36:39]\n"
B_"a[140:143],v249 offset:6144\n"
M_"a[36:39],a[136:139],a[216:219],a[36:39]\n"
B_"a[136:139],v255 offset:62464\n"
M_"a[36:39],a[132:135],a[212:215],a[36:39]\n"
B_"a[132:135],v255 offset:53248\n"
M_"a[36:39],a[128:131],a[208:211],a[36:39]\n"
W_"7)\n"
B_"a[128:131],v255 offset:44032\n"
M_"a[40:43],a[144:147],a[192:195],a[40:43]\n"
L_"v[32:35],v255,s[36:39],s63 offen sc1\n"
M_"a[40:43],a[148:151],a[196:199],a[40:43]\n"
L_"v[36:39],v255,s[36:39],s63 offen offset:1024 sc1\n"
M_"a[40:43],a[152:155],a[200:203],a[40:43]\n"
M_"a[40:43],a[156:159],a[204:207],a[40:43]\n"
M_"a[44:47],a[156:159],a[220:223],a[44:47]\n"
B_"a[156:159],v249 offset:7168\n"
M_"a[44:47],a[152:155],a[216:219],a[44:47]\n"
B_"a[152:155],v255 offset:63488\n"
M_"a[44:47],a[148:151],a[212:215],a[44:47]\n"
B_"a[148:151],v255 offset:54272\n"
M_"a[44:47],a[144:147],a[208:211],a[44:47]\n"
W_"7)\n"
B_"a[144:147],v255 offset:45056\n"
M_"a[48:51],a[160:163],a[192:195],a[48:51]\n"
L_"v[40:43],v255,s[36:39],s63 offen offset:2048 sc1\n"
M_"a[48:51],a[164:167],a[196:199],a[48:51]\n"
L_"v[44:47],v255,s[36:39],s63 offen offset:3072 sc1\n"
M_"a[48:51],a[168:171],a[200:203],a[48:51]\n"
S_"s63,180224\n"
M_"a[48:51],a[172:175],a[204:207],a[48:51]\n"
M_"a[52:55],a[172:175],a[220:223],a[52:55]\n"
L_"v[48:51],v255,s[36:39],s63 offen sc1\n"
M_"a[52:55],a[168:171],a[216:219],a[52:55]\n"
M_"a[52:55],a[164:167],a[212:215],a[52:55]\n"
M_"a[52:55],a[160:163],a[208:211],a[52:55]\n"
W_"4)\n"
M_"a[56:59],a[128:131],a[192:195],a[56:59]\n"
L_"v[52:55],v255,s[36:39],s63 offen offset:1024 sc1\n"
M_"a[56:59],a[132:135],a[196:199],a[56:59]\n"
M_"a[56:59],a[136:139],a[200:203],a[56:59]\n"
"s_waitcnt vmcnt(43) lgkmcnt(0)\n"
"s_barrier\n"
B_"a[160:163],v249 offset:8192\n"
M_"a[56:59],a[140:143],a[204:207],a[56:59]\n"
B_"a[164:167],v249 offset:17408\n"
M_"a[60:63],a[140:143],a[220:223],a[60:63]\n"
B_"a[168:171],v249 offset:26624\n"
M_"a[60:63],a[136:139],a[216:219],a[60:63]\n"
B_"a[172:175],v249 offset:35840\n"
M_"a[60:63],a[132:135],a[212:215],a[60:63]\n"
B_"a[132:135],v249 offset:18432\n"
M_"a[60:63],a[128:131],a[208:211],a[60:63]\n"
B_"a[128:131],v249 offset:9216\n"
M_"a[64:67],a[144:147],a[192:195],a[64:67]\n"
B_"a[136:139],v249 offset:27648\n"
M_"a[64:67],a[148:151],a[196:199],a[64:67]\n"
B_"a[140:143],v249 offset:36864\n"
M_"a[64:67],a[152:155],a[200:203],a[64:67]\n"
L_"v[56:59],v255,s[36:39],s63 offen offset:2048 sc1\n"
M_"a[64:67],a[156:159],a[204:207],a[64:67]\n"
L_"v[60:63],v255,s[36:39],s63 offen offset:3072 sc1\n"
M_"a[68:71],a[156:159],a[220:223],a[68:71]\n"
S_"s63,0\n"
B_"a[156:159],v249 offset:37888\n"
M_"a[68:71],a[152:155],a[216:219],a[68:71]\n"
B_"a[152:155],v249 offset:28672\n"
M_"a[68:71],a[148:151],a[212:215],a[68:71]\n"
B_"a[148:151],v249 offset:19456\n"
M_"a[68:71],a[144:147],a[208:211],a[68:71]\n"
"s_waitcnt vmcnt(44) lgkmcnt(10)\n"
B_"a[144:147],v249 offset:10240\n"
M_"a[0:3],a[160:163],a[224:227],a[0:3]\n"
"s_waitcnt vmcnt(43) lgkmcnt(10)\n"
L_"v255,s[24:27],s62 offen sc1 lds\n"
M_"a[0:3],a[164:167],a[228:231],a[0:3]\n"
"s_waitcnt vmcnt(43) lgkmcnt(9)\n"
M_"a[0:3],a[168:171],a[232:235],a[0:3]\n"
"s_waitcnt vmcnt(42) lgkmcnt(8)\n"
M_"a[0:3],a[172:175],a[236:239],a[0:3]\n"
"s_waitcnt vmcnt(37)\n"
M_"a[4:7],a[172:175],a[252:255],a[4:7]\n"
B_"a[172:175],v249 offset:38912\n"
M_"a[4:7],a[168:171],a[248:251],a[4:7]\n"
B_"a[168:171],v249 offset:29696\n"
M_"a[4:7],a[164:167],a[244:247],a[4:7]\n"
B_"a[164:167],v249 offset:20480\n"
M_"a[4:7],a[160:163],a[240:243],a[4:7]\n"
W_"9)\n"
B_"a[160:163],v249 offset:11264\n"
M_"a[8:11],a[128:131],a[224:227],a[8:11]\n"
L_"v255,s[24:27],s62 offen offset:1024 sc1 lds\n"
M_"a[8:11],a[132:135],a[228:231],a[8:11]\n"
W_"9)\n"
L_"v255,s[24:27],s62 offen offset:2048 sc1 lds\n"
M_"a[8:11],a[136:139],a[232:235],a[8:11]\n"
W_"8)\n"
M_"a[8:11],a[140:143],a[236:239],a[8:11]\n"
M_"a[12:15],a[140:143],a[252:255],a[12:15]\n"
B_"a[140:143],v249 offset:39936\n"
M_"a[12:15],a[136:139],a[248:251],a[12:15]\n"
B_"a[136:139],v249 offset:30720\n"
M_"a[12:15],a[132:135],a[244:247],a[12:15]\n"
B_"a[132:135],v249 offset:21504\n"
M_"a[12:15],a[128:131],a[240:243],a[12:15]\n"
W_"7)\n"
B_"a[128:131],v249 offset:12288\n"
M_"a[16:19],a[144:147],a[224:227],a[16:19]\n"
L_"v255,s[24:27],s62 offen offset:3072 sc1 lds\n"
M_"a[16:19],a[148:151],a[228:231],a[16:19]\n"
A_"m0,s35,40960\n"
A_"s62,s34,5312512\n"
M_"a[16:19],a[152:155],a[232:235],a[16:19]\n"
L_"v255,s[24:27],s62 offen sc1 lds\n"
M_"a[16:19],a[156:159],a[236:239],a[16:19]\n"
M_"a[20:23],a[156:159],a[252:255],a[20:23]\n"
B_"a[156:159],v249 offset:40960\n"
M_"a[20:23],a[152:155],a[248:251],a[20:23]\n"
B_"a[152:155],v249 offset:31744\n"
M_"a[20:23],a[148:151],a[244:247],a[20:23]\n"
B_"a[148:151],v249 offset:22528\n"
M_"a[20:23],a[144:147],a[240:243],a[20:23]\n"
W_"7)\n"
B_"a[144:147],v249 offset:13312\n"
M_"a[24:27],a[160:163],a[224:227],a[24:27]\n"
L_"v255,s[24:27],s62 offen offset:1024 sc1 lds\n"
M_"a[24:27],a[164:167],a[228:231],a[24:27]\n"
L_"v255,s[24:27],s62 offen offset:2048 sc1 lds\n"
M_"a[24:27],a[168:171],a[232:235],a[24:27]\n"
M_"a[24:27],a[172:175],a[236:239],a[24:27]\n"
M_"a[28:31],a[172:175],a[252:255],a[28:31]\n"
B_"a[172:175],v249 offset:41984\n"
M_"a[28:31],a[168:171],a[248:251],a[28:31]\n"
B_"a[168:171],v249 offset:32768\n"
M_"a[28:31],a[164:167],a[244:247],a[28:31]\n"
B_"a[164:167],v249 offset:23552\n"
M_"a[28:31],a[160:163],a[240:243],a[28:31]\n"
W_"7)\n"
B_"a[160:163],v249 offset:14336\n"
M_"a[32:35],a[128:131],a[224:227],a[32:35]\n"
L_"v255,s[24:27],s62 offen offset:3072 sc1 lds\n"
M_"a[32:35],a[132:135],a[228:231],a[32:35]\n"
A_"m0,s35,45056\n"
A_"s62,s34,5316608\n"
M_"a[32:35],a[136:139],a[232:235],a[32:35]\n"
L_"v255,s[24:27],s62 offen sc1 lds\n"
M_"a[32:35],a[140:143],a[236:239],a[32:35]\n"
A_"m0,s35,73728\n"
A_"s62,s34,5455872\n"
M_"a[36:39],a[140:143],a[252:255],a[36:39]\n"
B_"a[140:143],v249 offset:43008\n"
M_"a[36:39],a[136:139],a[248:251],a[36:39]\n"
B_"a[136:139],v249 offset:33792\n"
M_"a[36:39],a[132:135],a[244:247],a[36:39]\n"
B_"a[132:135],v249 offset:24576\n"
M_"a[36:39],a[128:131],a[240:243],a[36:39]\n"
W_"7)\n"
B_"a[128:131],v249 offset:15360\n"
M_"a[40:43],a[144:147],a[224:227],a[40:43]\n"
"buffer_load_dwordx2 v[232:233],v251,s[56:59],s85 offen sc1\n"
M_"a[40:43],a[148:151],a[228:231],a[40:43]\n"
M_"a[40:43],a[152:155],a[232:235],a[40:43]\n"
M_"a[40:43],a[156:159],a[236:239],a[40:43]\n"
M_"a[44:47],a[156:159],a[252:255],a[44:47]\n"
B_"a[156:159],v249 offset:44032\n"
M_"a[44:47],a[152:155],a[248:251],a[44:47]\n"
B_"a[152:155],v249 offset:34816\n"
M_"a[44:47],a[148:151],a[244:247],a[44:47]\n"
B_"a[148:151],v249 offset:25600\n"
M_"a[44:47],a[144:147],a[240:243],a[44:47]\n"
W_"7)\n"
B_"a[144:147],v249 offset:16384\n"
M_"a[48:51],a[160:163],a[224:227],a[48:51]\n"
M_"a[48:51],a[164:167],a[228:231],a[48:51]\n"
M_"a[48:51],a[168:171],a[232:235],a[48:51]\n"
M_"a[48:51],a[172:175],a[236:239],a[48:51]\n"
M_"a[52:55],a[172:175],a[252:255],a[52:55]\n"
M_"a[52:55],a[168:171],a[248:251],a[52:55]\n"
M_"a[52:55],a[164:167],a[244:247],a[52:55]\n"
M_"a[52:55],a[160:163],a[240:243],a[52:55]\n"
W_"4)\n"
M_"a[56:59],a[128:131],a[224:227],a[56:59]\n"
M_"a[56:59],a[132:135],a[228:231],a[56:59]\n"
M_"a[56:59],a[136:139],a[232:235],a[56:59]\n"
"s_waitcnt vmcnt(37) lgkmcnt(0)\n"
"s_barrier\n"
B_"a[160:163],v249 offset:45056\n"
M_"a[56:59],a[140:143],a[236:239],a[56:59]\n"
B_"a[164:167],v249 offset:54272\n"
M_"a[60:63],a[140:143],a[252:255],a[60:63]\n"
B_"a[168:171],v249 offset:63488\n"
M_"a[60:63],a[136:139],a[248:251],a[60:63]\n"
B_"a[172:175],v242 offset:7168\n"
M_"a[60:63],a[132:135],a[244:247],a[60:63]\n"
B_"a[132:135],v249 offset:55296\n"
M_"a[60:63],a[128:131],a[240:243],a[60:63]\n"
B_"a[128:131],v249 offset:46080\n"
M_"a[64:67],a[144:147],a[224:227],a[64:67]\n"
B_"a[136:139],v249 offset:64512\n"
M_"a[64:67],a[148:151],a[228:231],a[64:67]\n"
B_"a[140:143],v242 offset:8192\n"
M_"a[64:67],a[152:155],a[232:235],a[64:67]\n"
M_"a[64:67],a[156:159],a[236:239],a[64:67]\n"
M_"a[68:71],a[156:159],a[252:255],a[68:71]\n"
B_"a[156:159],v242 offset:9216\n"
M_"a[68:71],a[152:155],a[248:251],a[68:71]\n"
B_"a[152:155],v242 offset:0\n"
M_"a[68:71],a[148:151],a[244:247],a[68:71]\n"
B_"a[148:151],v249 offset:56320\n"
M_"a[68:71],a[144:147],a[240:243],a[68:71]\n"
"s_waitcnt vmcnt(0)\n"
"v_cmp_eq_u64 vcc,s[66:67],v[232:233]\n"
"s_nop 3\n"
"s_cmp_eq_u64 vcc,-1\n"
"s_cbranch_scc0 SPINO_%=\n"
"BACKO_%=:\n"
W_"10)\n"
M_"a[0:3],a[160:163],v[0:3],a[0:3]\n"
W_"9)\n"
B_"a[144:147],v249 offset:47104\n"
M_"a[0:3],a[164:167],v[4:7],a[0:3]\n"
W_"9)\n"
L_"v255,s[24:27],s62 offen sc1 lds\n"
M_"a[0:3],a[168:171],v[8:11],a[0:3]\n"
W_"8)\n"
L_"v255,s[24:27],s62 offen offset:1024 sc1 lds\n"
M_"a[0:3],a[172:175],v[12:15],a[0:3]\n"
A_"s85,s71,24576\n"
M_"a[4:7],a[172:175],v[28:31],a[4:7]\n"
B_"a[172:175],v242 offset:10240\n"
M_"a[4:7],a[168:171],v[24:27],a[4:7]\n"
B_"a[168:171],v242 offset:1024\n"
M_"a[4:7],a[164:167],v[20:23],a[4:7]\n"
B_"a[164:167],v249 offset:57344\n"
M_"a[4:7],a[160:163],v[16:19],a[4:7]\n"
W_"9)\n"
B_"a[160:163],v249 offset:48128\n"
M_"a[8:11],a[128:131],v[0:3],a[8:11]\n"
L_"v255,s[24:27],s62 offen offset:2048 sc1 lds\n"
M_"a[8:11],a[132:135],v[4:7],a[8:11]\n"
W_"9)\n"
L_"v255,s[24:27],s62 offen offset:3072 sc1 lds\n"
M_"a[8:11],a[136:139],v[8:11],a[8:11]\n"
W_"8)\n"
A_"m0,s35,77824\n"
A_"s62,s34,5459968\n"
M_"a[8:11],a[140:143],v[12:15],a[8:11]\n"
L_"v255,s[24:27],s62 offen sc1 lds\n"
M_"a[12:15],a[140:143],v[28:31],a[12:15]\n"
B_"a[140:143],v242 offset:11264\n"
M_"a[12:15],a[136:139],v[24:27],a[12:15]\n"
B_"a[136:139],v242 offset:2048\n"
M_"a[12:15],a[132:135],v[20:23],a[12:15]\n"
B_"a[132:135],v249 offset:58368\n"
M_"a[12:15],a[128:131],v[16:19],a[12:15]\n"
W_"7)\n"
B_"a[128:131],v249 offset:49152\n"
M_"a[16:19],a[144:147],v[0:3],a[16:19]\n"
L_"v255,s[24:27],s62 offen offset:1024 sc1 lds\n"
M_"a[16:19],a[148:151],v[4:7],a[16:19]\n"
L_"v255,s[24:27],s62 offen offset:2048 sc1 lds\n"
M_"a[16:19],a[152:155],v[8:11],a[16:19]\n"
L_"v255,s[24:27],s62 offen offset:3072 sc1 lds\n"
M_"a[16:19],a[156:159],v[12:15],a[16:19]\n"
A_"m0,s35,81920\n"
A_"s62,s34,5464064\n"
M_"a[20:23],a[156:159],v[28:31],a[20:23]\n"
B_"a[156:159],v242 offset:12288\n"
M_"a[20:23],a[152:155],v[24:27],a[20:23]\n"
B_"a[152:155],v242 offset:3072\n"
M_"a[20:23],a[148:151],v[20:23],a[20:23]\n"
B_"a[148:151],v249 offset:59392\n"
M_"a[20:23],a[144:147],v[16:19],a[20:23]\n"
W_"7)\n"
B_"a[144:147],v249 offset:50176\n"
M_"a[24:27],a[160:163],v[0:3],a[24:27]\n"
L_"v255,s[24:27],s62 offen sc1 lds\n"
M_"a[24:27],a[164:167],v[4:7],a[24:27]\n"
A_"m0,s35,110592\n"
L_"v[136:139],v255,s[28:31],s63 offen sc1\n"
M_"a[24:27],a[168:171],v[8:11],a[24:27]\n"
A_"s62,s34,5603328\n"
L_"v[140:143],v255,s[28:31],s63 offen offset:1024 sc1\n"
M_"a[24:27],a[172:175],v[12:15],a[24:27]\n"
L_"v[144:147],v255,s[28:31],s63 offen offset:2048 sc1\n"
M_"a[28:31],a[172:175],v[28:31],a[28:31]\n"
B_"a[172:175],v242 offset:13312\n"
M_"a[28:31],a[168:171],v[24:27],a[28:31]\n"
B_"a[168:171],v242 offset:4096\n"
M_"a[28:31],a[164:167],v[20:23],a[28:31]\n"
B_"a[164:167],v249 offset:60416\n"
M_"a[28:31],a[160:163],v[16:19],a[28:31]\n"
W_"7)\n"
B_"a[160:163],v249 offset:51200\n"
M_"a[32:35],a[128:131],v[0:3],a[32:35]\n"
L_"v[148:151],v255,s[28:31],s63 offen offset:3072 sc1\n"
M_"a[32:35],a[132:135],v[4:7],a[32:35]\n"
S_"s63,131072\n"
M_"a[32:35],a[136:139],v[8:11],a[32:35]\n"
L_"v[152:155],v255,s[28:31],s63 offen sc1\n"
M_"a[32:35],a[140:143],v[12:15],a[32:35]\n"
L_"v[156:159],v255,s[28:31],s63 offen offset:1024 sc1\n"
M_"a[36:39],a[140:143],v[28:31],a[36:39]\n"
B_"a[140:143],v242 offset:14336\n"
M_"a[36:39],a[136:139],v[24:27],a[36:39]\n"
B_"a[136:139],v242 offset:5120\n"
M_"a[36:39],a[132:135],v[20:23],a[36:39]\n"
B_"a[132:135],v249 offset:61440\n"
M_"a[36:39],a[128:131],v[16:19],a[36:39]\n"
W_"7)\n"
B_"a[128:131],v249 offset:52224\n"
M_"a[40:43],a[144:147],v[0:3],a[40:43]\n"
L_"v[160:163],v255,s[28:31],s63 offen offset:2048 sc1\n"
M_"a[40:43],a[148:151],v[4:7],a[40:43]\n"
L_"v[164:167],v255,s[28:31],s63 offen offset:3072 sc1\n"
M_"a[40:43],a[152:155],v[8:11],a[40:43]\n"
S_"s63,4096\n"
M_"a[40:43],a[156:159],v[12:15],a[40:43]\n"
L_"a[192:195],v255,s[28:31],s63 offen sc1\n"
M_"a[44:47],a[156:159],v[28:31],a[44:47]\n"
B_"a[156:159],v242 offset:15360\n"
M_"a[44:47],a[152:155],v[24:27],a[44:47]\n"
B_"a[152:155],v242 offset:6144\n"
M_"a[44:47],a[148:151],v[20:23],a[44:47]\n"
B_"a[148:151],v249 offset:62464\n"
M_"a[44:47],a[144:147],v[16:19],a[44:47]\n"
W_"7)\n"
B_"a[144:147],v249 offset:53248\n"
M_"a[48:51],a[160:163],v[0:3],a[48:51]\n"
L_"a[196:199],v255,s[28:31],s63 offen offset:1024 sc1\n"
M_"a[48:51],a[164:167],v[4:7],a[48:51]\n"
L_"a[200:203],v255,s[28:31],s63 offen offset:2048 sc1\n"
M_"a[48:51],a[168:171],v[8:11],a[48:51]\n"
L_"a[204:207],v255,s[28:31],s63 offen offset:3072 sc1\n"
M_"a[48:51],a[172:175],v[12:15],a[48:51]\n"
S_"s63,135168\n"
M_"a[52:55],a[172:175],v[28:31],a[52:55]\n"
L_"a[208:211],v255,s[28:31],s63 offen sc1\n"
M_"a[52:55],a[168:171],v[24:27],a[52:55]\n"
L_"a[212:215],v255,s[28:31],s63 offen offset:1024 sc1\n"
M_"a[52:55],a[164:167],v[20:23],a[52:55]\n"
L_"a[216:219],v255,s[28:31],s63 offen offset:2048 sc1\n"
M_"a[52:55],a[160:163],v[16:19],a[52:55]\n"
W_"4)\n"
L_"a[220:223],v255,s[28:31],s63 offen offset:3072 sc1\n"
M_"a[56:59],a[128:131],v[0:3],a[56:59]\n"
S_"s63,8192\n"
M_"a[56:59],a[132:135],v[4:7],a[56:59]\n"
M_"a[56:59],a[136:139],v[8:11],a[56:59]\n"
W_"0)\n"
"s_barrier\n"
B_"a[160:163],v255 offset:0\n"
M_"a[56:59],a[140:143],v[12:15],a[56:59]\n"
B_"a[164:167],v255 offset:9216\n"
M_"a[60:63],a[140:143],v[28:31],a[60:63]\n"
B_"a[168:171],v255 offset:18432\n"
M_"a[60:63],a[136:139],v[24:27],a[60:63]\n"
B_"a[172:175],v255 offset:27648\n"
M_"a[60:63],a[132:135],v[20:23],a[60:63]\n"
B_"a[132:135],v255 offset:10240\n"
M_"a[60:63],a[128:131],v[16:19],a[60:63]\n"
B_"a[128:131],v255 offset:1024\n"
M_"a[64:67],a[144:147],v[0:3],a[64:67]\n"
B_"a[136:139],v255 offset:19456\n"
M_"a[64:67],a[148:151],v[4:7],a[64:67]\n"
B_"a[140:143],v255 offset:28672\n"
M_"a[64:67],a[152:155],v[8:11],a[64:67]\n"
M_"a[64:67],a[156:159],v[12:15],a[64:67]\n"
M_"a[68:71],a[156:159],v[28:31],a[68:71]\n"
B_"a[156:159],v255 offset:29696\n"
M_"a[68:71],a[152:155],v[24:27],a[68:71]\n"
B_"a[152:155],v255 offset:20480\n"
M_"a[68:71],a[148:151],v[20:23],a[68:71]\n"
B_"a[148:151],v255 offset:11264\n"
M_"a[68:71],a[144:147],v[16:19],a[68:71]\n"
W_"10)\n"
B_"a[144:147],v255 offset:2048\n"
M_"a[0:3],a[160:163],v[32:35],a[0:3]\n"
W_"10)\n"
L_"v255,s[24:27],s62 offen sc1 lds\n"
M_"a[0:3],a[164:167],v[36:39],a[0:3]\n"
W_"9)\n"
M_"a[0:3],a[168:171],v[40:43],a[0:3]\n"
W_"8)\n"
M_"a[0:3],a[172:175],v[44:47],a[0:3]\n"
M_"a[4:7],a[172:175],v[60:63],a[4:7]\n"
B_"a[172:175],v255 offset:30720\n"
M_"a[4:7],a[168:171],v[56:59],a[4:7]\n"
B_"a[168:171],v255 offset:21504\n"
M_"a[4:7],a[164:167],v[52:55],a[4:7]\n"
B_"a[164:167],v255 offset:12288\n"
M_"a[4:7],a[160:163],v[48:51],a[4:7]\n"
W_"9)\n"
B_"a[160:163],v255 offset:3072\n"
M_"a[8:11],a[128:131],v[32:35],a[8:11]\n"
L_"v255,s[24:27],s62 offen offset:1024 sc1 lds\n"
M_"a[8:11],a[132:135],v[36:39],a[8:11]\n"
W_"9)\n"
L_"v255,s[24:27],s62 offen offset:2048 sc1 lds\n"
M_"a[8:11],a[136:139],v[40:43],a[8:11]\n"
W_"8)\n"
M_"a[8:11],a[140:143],v[44:47],a[8:11]\n"
M_"a[12:15],a[140:143],v[60:63],a[12:15]\n"
B_"a[140:143],v255 offset:31744\n"
M_"a[12:15],a[136:139],v[56:59],a[12:15]\n"
B_"a[136:139],v255 offset:22528\n"
M_"a[12:15],a[132:135],v[52:55],a[12:15]\n"
B_"a[132:135],v255 offset:13312\n"
M_"a[12:15],a[128:131],v[48:51],a[12:15]\n"
W_"7)\n"
B_"a[128:131],v255 offset:4096\n"
M_"a[16:19],a[144:147],v[32:35],a[16:19]\n"
L_"v255,s[24:27],s62 offen offset:3072 sc1 lds\n"
M_"a[16:19],a[148:151],v[36:39],a[16:19]\n"
A_"m0,s35,114688\n"
A_"s62,s34,5607424\n"
M_"a[16:19],a[152:155],v[40:43],a[16:19]\n"
L_"v255,s[24:27],s62 offen sc1 lds\n"
M_"a[16:19],a[156:159],v[44:47],a[16:19]\n"
M_"a[20:23],a[156:159],v[60:63],a[20:23]\n"
B_"a[156:159],v255 offset:32768\n"
M_"a[20:23],a[152:155],v[56:59],a[20:23]\n"
B_"a[152:155],v255 offset:23552\n"
M_"a[20:23],a[148:151],v[52:55],a[20:23]\n"
B_"a[148:151],v255 offset:14336\n"
M_"a[20:23],a[144:147],v[48:51],a[20:23]\n"
W_"7)\n"
B_"a[144:147],v255 offset:5120\n"
R_"v224,a16\n"
M_"a[24:27],a[160:163],v[32:35],a[24:27]\n"
L_"v255,s[24:27],s62 offen offset:1024 sc1 lds\n"
M_"a[24:27],a[164:167],v[36:39],a[24:27]\n"
L_"v255,s[24:27],s62 offen offset:2048 sc1 lds\n"
M_"a[24:27],a[168:171],v[40:43],a[24:27]\n"
R_"v225,a17\n"
R_"v226,a18\n"
M_"a[24:27],a[172:175],v[44:47],a[24:27]\n"
R_"v227,a19\n"
C_"v224,v224,v225\n"
M_"a[28:31],a[172:175],v[60:63],a[28:31]\n"
B_"a[172:175],v255 offset:33792\n"
R_"v228,a20\n"
M_"a[28:31],a[168:171],v[56:59],a[28:31]\n"
B_"a[168:171],v255 offset:24576\n"
R_"v229,a21\n"
M_"a[28:31],a[164:167],v[52:55],a[28:31]\n"
B_"a[164:167],v255 offset:15360\n"
R_"v230,a22\n"
M_"a[28:31],a[160:163],v[48:51],a[28:31]\n"
W_"7)\n"
B_"a[160:163],v255 offset:6144\n"
R_"v231,a23\n"
M_"a[32:35],a[128:131],v[32:35],a[32:35]\n"
L_"v255,s[24:27],s62 offen offset:3072 sc1 lds\n"
M_"a[32:35],a[132:135],v[36:39],a[32:35]\n"
A_"m0,s35,118784\n"
A_"s62,s34,5611520\n"
C_"v225,v226,v227\n"
M_"a[32:35],a[136:139],v[40:43],a[32:35]\n"
C_"v226,v228,v229\n"
C_"v227,v230,v231\n"
M_"a[32:35],a[140:143],v[44:47],a[32:35]\n"
O_"v[224:227],v255,s[48:51],s87 offen sc1\n"
M_"a[36:39],a[140:143],v[60:63],a[36:39]\n"
A_"s87,s61,3072\n"
B_"a[140:143],v255 offset:34816\n"
R_"v208,a24\n"
M_"a[36:39],a[136:139],v[56:59],a[36:39]\n"
"s_sub_u32 s86,s87,9216\n"
B_"a[136:139],v255 offset:25600\n"
R_"v209,a25\n"
M_"a[36:39],a[132:135],v[52:55],a[36:39]\n"
"s_min_u32 s87,s87,s86\n"
B_"a[132:135],v255 offset:16384\n"
R_"v210,a26\n"
M_"a[36:39],a[128:131],v[48:51],a[36:39]\n"
W_"7)\n"
B_"a[128:131],v255 offset:7168\n"
R_"v211,a27\n"
M_"a[40:43],a[144:147],v[32:35],a[40:43]\n"
A_"s87,s87,s84\n"
L_"v255,s[24:27],s62 offen sc1 lds\n"
M_"a[40:43],a[148:151],v[36:39],a[40:43]\n"
A_"m0,s35,0\n"
L_"a[224:227],v255,s[28:31],s63 offen sc1\n"
M_"a[40:43],a[152:155],v[40:43],a[40:43]\n"
A_"s62,s34,5750784\n"
L_"a[228:231],v255,s[28:31],s63 offen offset:1024 sc1\n"
M_"a[40:43],a[156:159],v[44:47],a[40:43]\n"
C_"v208,v208,v209\n"
R_"v212,a28\n"
M_"a[44:47],a[156:159],v[60:63],a[44:47]\n"
B_"a[156:159],v255 offset:35840\n"
R_"v213,a29\n"
M_"a[44:47],a[152:155],v[56:59],a[44:47]\n"
B_"a[152:155],v255 offset:26624\n"
R_"v214,a30\n"
M_"a[44:47],a[148:151],v[52:55],a[44:47]\n"
B_"a[148:151],v255 offset:17408\n"
R_"v215,a31\n"
M_"a[44:47],a[144:147],v[48:51],a[44:47]\n"
W_"7)\n"
B_"a[144:147],v255 offset:8192\n"
C_"v209,v210,v211\n"
M_"a[48:51],a[160:163],v[32:35],a[48:51]\n"
L_"a[232:235],v255,s[28:31],s63 offen offset:2048 sc1\n"
M_"a[48:51],a[164:167],v[36:39],a[48:51]\n"
L_"a[236:239],v255,s[28:31],s63 offen offset:3072 sc1\n"
M_"a[48:51],a[168:171],v[40:43],a[48:51]\n"
S_"s63,139264\n"
C_"v210,v212,v213\n"
C_"v211,v214,v215\n"
M_"a[48:51],a[172:175],v[44:47],a[48:51]\n"
O_"v[208:211],v255,s[48:51],s87 offen sc1\n"
M_"a[52:55],a[172:175],v[60:63],a[52:55]\n"
A_"s87,s61,4096\n"
L_"a[240:243],v255,s[28:31],s63 offen sc1\n"
M_"a[52:55],a[168:171],v[56:59],a[52:55]\n"
"s_sub_u32 s86,s87,9216\n"
R_"v216,a32\n"
R_"v217,a33\n"
M_"a[52:55],a[164:167],v[52:55],a[52:55]\n"
"s_min_u32 s87,s87,s86\n"
R_"v218,a34\n"
R_"v219,a35\n"
M_"a[52:55],a[160:163],v[48:51],a[52:55]\n"
W_"4)\n"
C_"v216,v216,v217\n"
R_"v220,a36\n"
M_"a[56:59],a[128:131],v[32:35],a[56:59]\n"
A_"s87,s87,s84\n"
L_"a[244:247],v255,s[28:31],s63 offen offset:1024 sc1\n"
M_"a[56:59],a[132:135],v[36:39],a[56:59]\n"
R_"v221,a37\n"
R_"v222,a38\n"
M_"a[56:59],a[136:139],v[40:43],a[56:59]\n"
W_"0)\n"
"s_barrier\n"
B_"a[160:163],v255 offset:36864\n"
M_"a[56:59],a[140:143],v[44:47],a[56:59]\n"
B_"a[164:167],v255 offset:46080\n"
R_"v223,a39\n"
M_"a[60:63],a[140:143],v[60:63],a[60:63]\n"
B_"a[168:171],v255 offset:55296\n"
C_"v217,v218,v219\n"
M_"a[60:63],a[136:139],v[56:59],a[60:63]\n"
B_"a[172:175],v255 offset:64512\n"
C_"v218,v220,v221\n"
M_"a[60:63],a[132:135],v[52:55],a[60:63]\n"
B_"a[132:135],v255 offset:47104\n"
C_"v219,v222,v223\n"
M_"a[60:63],a[128:131],v[48:51],a[60:63]\n"
B_"a[128:131],v255 offset:37888\n"
R_"v224,a40\n"
M_"a[64:67],a[144:147],v[32:35],a[64:67]\n"
B_"a[136:139],v255 offset:56320\n"
R_"v225,a41\n"
M_"a[64:67],a[148:151],v[36:39],a[64:67]\n"
B_"a[140:143],v249 offset:0\n"
R_"v226,a42\n"
M_"a[64:67],a[152:155],v[40:43],a[64:67]\n"
L_"a[248:251],v255,s[28:31],s63 offen offset:2048 sc1\n"
M_"a[64:67],a[156:159],v[44:47],a[64:67]\n"
L_"a[252:255],v255,s[28:31],s63 offen offset:3072 sc1\n"
M_"a[68:71],a[156:159],v[60:63],a[68:71]\n"
S_"s63,12288\n"
B_"a[156:159],v249 offset:1024\n"
R_"v227,a43\n"
M_"a[68:71],a[152:155],v[56:59],a[68:71]\n"
B_"a[152:155],v255 offset:57344\n"
C_"v224,v224,v225\n"
M_"a[68:71],a[148:151],v[52:55],a[68:71]\n"
B_"a[148:151],v255 offset:48128\n"
R_"v228,a44\n"
M_"a[68:71],a[144:147],v[48:51],a[68:71]\n"
"s_waitcnt vmcnt(34) lgkmcnt(10)\n"
B_"a[144:147],v255 offset:38912\n"
R_"v229,a45\n"
M_"v[64:67],a[160:163],v[136:139],0\n"
"s_waitcnt vmcnt(33) lgkmcnt(10)\n"
O_"v[216:219],v255,s[48:51],s87 offen sc1\n"
M_"v[64:67],a[164:167],v[140:143],v[64:67]\n"
"s_waitcnt vmcnt(33) lgkmcnt(9)\n"
L_"v255,s[24:27],s62 offen sc1 lds\n"
M_"v[64:67],a[168:171],v[144:147],v[64:67]\n"
"s_waitcnt vmcnt(33) lgkmcnt(8)\n"
R_"v230,a46\n"
R_"v231,a47\n"
M_"v[64:67],a[172:175],v[148:151],v[64:67]\n"
"s_waitcnt vmcnt(29)\n"
C_"v225,v226,v227\n"
R_"v208,a48\n"
M_"v[68:71],a[172:175],v[164:167],0\n"
A_"s87,s61,5120\n"
B_"a[172:175],v249 offset:2048\n"
C_"v226,v228,v229\n"
M_"v[68:71],a[168:171],v[160:163],v[68:71]\n"
"s_sub_u32 s86,s87,9216\n"
B_"a[168:171],v255 offset:58368\n"
C_"v227,v230,v231\n"
M_"v[68:71],a[164:167],v[156:159],v[68:71]\n"
"s_min_u32 s87,s87,s86\n"
B_"a[164:167],v255 offset:49152\n"
R_"v209,a49\n"
M_"v[68:71],a[160:163],v[152:155],v[68:71]\n"
W_"9)\n"
B_"a[160:163],v255 offset:39936\n"
R_"v210,a50\n"
M_"v[72:75],a[128:131],v[136:139],0\n"
A_"s87,s87,s84\n"
L_"v255,s[24:27],s62 offen offset:1024 sc1 lds\n"
M_"v[72:75],a[132:135],v[140:143],v[72:75]\n"
W_"9)\n"
L_"v255,s[24:27],s62 offen offset:2048 sc1 lds\n"
M_"v[72:75],a[136:139],v[144:147],v[72:75]\n"
W_"8)\n"
R_"v211,a51\n"
C_"v208,v208,v209\n"
M_"v[72:75],a[140:143],v[148:151],v[72:75]\n"
R_"v212,a52\n"
R_"v213,a53\n"
M_"v[76:79],a[140:143],v[164:167],0\n"
B_"a[140:143],v249 offset:3072\n"
R_"v214,a54\n"
M_"v[76:79],a[136:139],v[160:163],v[76:79]\n"
B_"a[136:139],v255 offset:59392\n"
R_"v215,a55\n"
M_"v[76:79],a[132:135],v[156:159],v[76:79]\n"
B_"a[132:135],v255 offset:50176\n"
C_"v209,v210,v211\n"
M_"v[76:79],a[128:131],v[152:155],v[76:79]\n"
W_"7)\n"
B_"a[128:131],v255 offset:40960\n"
C_"v210,v212,v213\n"
M_"v[80:83],a[144:147],v[136:139],0\n"
L_"v255,s[24:27],s62 offen offset:3072 sc1 lds\n"
M_"v[80:83],a[148:151],v[140:143],v[80:83]\n"
A_"m0,s35,4096\n"
O_"v[224:227],v255,s[48:51],s87 offen sc1\n"
M_"v[80:83],a[152:155],v[144:147],v[80:83]\n"
A_"s62,s34,5754880\n"
C_"v211,v214,v215\n"
R_"v216,a56\n"
M_"v[80:83],a[156:159],v[148:151],v[80:83]\n"
A_"s87,s61,6144\n"
L_"v255,s[24:27],s62 offen sc1 lds\n"
M_"v[84:87],a[156:159],v[164:167],0\n"
"s_sub_u32 s86,s87,9216\n"
B_"a[156:159],v249 offset:4096\n"
R_"v217,a57\n"
M_"v[84:87],a[152:155],v[160:163],v[84:87]\n"
"s_min_u32 s87,s87,s86\n"
B_"a[152:155],v255 offset:60416\n"
R_"v218,a58\n"
M_"v[84:87],a[148:151],v[156:159],v[84:87]\n"
A_"s87,s87,s84\n"
B_"a[148:151],v255 offset:51200\n"
R_"v219,a59\n"
M_"v[84:87],a[144:147],v[152:155],v[84:87]\n"
W_"7)\n"
B_"a[144:147],v255 offset:41984\n"
C_"v216,v216,v217\n"
M_"v[88:91],a[160:163],v[136:139],0\n"
L_"v255,s[24:27],s62 offen offset:1024 sc1 lds\n"
M_"v[88:91],a[164:167],v[140:143],v[88:91]\n"
L_"v255,s[24:27],s62 offen offset:2048 sc1 lds\n"
M_"v[88:91],a[168:171],v[144:147],v[88:91]\n"
R_"v220,a60\n"
R_"v221,a61\n"
M_"v[88:91],a[172:175],v[148:151],v[88:91]\n"
R_"v222,a62\n"
R_"v223,a63\n"
M_"v[92:95],a[172:175],v[164:167],0\n"
B_"a[172:175],v249 offset:5120\n"
C_"v217,v218,v219\n"
M_"v[92:95],a[168:171],v[160:163],v[92:95]\n"
B_"a[168:171],v255 offset:61440\n"
C_"v218,v220,v221\n"
M_"v[92:95],a[164:167],v[156:159],v[92:95]\n"
B_"a[164:167],v255 offset:52224\n"
C_"v219,v222,v223\n"
M_"v[92:95],a[160:163],v[152:155],v[92:95]\n"
W_"7)\n"
B_"a[160:163],v255 offset:43008\n"
R_"v224,a64\n"
M_"v[96:99],a[128:131],v[136:139],0\n"
L_"v255,s[24:27],s62 offen offset:3072 sc1 lds\n"
M_"v[96:99],a[132:135],v[140:143],v[96:99]\n"
A_"m0,s35,8192\n"
O_"v[208:211],v255,s[48:51],s87 offen sc1\n"
M_"v[96:99],a[136:139],v[144:147],v[96:99]\n"
A_"s62,s34,5758976\n"
R_"v225,a65\n"
R_"v226,a66\n"
M_"v[96:99],a[140:143],v[148:151],v[96:99]\n"
A_"s87,s61,7168\n"
L_"v255,s[24:27],s62 offen sc1 lds\n"
M_"v[100:103],a[140:143],v[164:167],0\n"
"s_sub_u32 s86,s87,9216\n"
B_"a[140:143],v249 offset:6144\n"
R_"v227,a67\n"
M_"v[100:103],a[136:139],v[160:163],v[100:103]\n"
"s_min_u32 s87,s87,s86\n"
B_"a[136:139],v255 offset:62464\n"
C_"v224,v224,v225\n"
M_"v[100:103],a[132:135],v[156:159],v[100:103]\n"
A_"s87,s87,s84\n"
B_"a[132:135],v255 offset:53248\n"
R_"v228,a68\n"
M_"v[100:103],a[128:131],v[152:155],v[100:103]\n"
W_"7)\n"
B_"a[128:131],v255 offset:44032\n"
R_"v229,a69\n"
M_"v[104:107],a[144:147],v[136:139],0\n"
A_"m0,s35,36864\n"
L_"v[0:3],v255,s[28:31],s63 offen sc1\n"
M_"v[104:107],a[148:151],v[140:143],v[104:107]\n"
A_"s62,s34,5898240\n"
L_"v[4:7],v255,s[28:31],s63 offen offset:1024 sc1\n"
M_"v[104:107],a[152:155],v[144:147],v[104:107]\n"
R_"v230,a70\n"
R_"v231,a71\n"
M_"v[104:107],a[156:159],v[148:151],v[104:107]\n"
C_"v225,v226,v227\n"
C_"v226,v228,v229\n"
M_"v[108:111],a[156:159],v[164:167],0\n"
B_"a[156:159],v249 offset:7168\n"
C_"v227,v230,v231\n"
M_"v[108:111],a[152:155],v[160:163],v[108:111]\n"
B_"a[152:155],v255 offset:63488\n"
M_"v[108:111],a[148:151],v[156:159],v[108:111]\n"
B_"a[148:151],v255 offset:54272\n"
M_"v[108:111],a[144:147],v[152:155],v[108:111]\n"
W_"7)\n"
B_"a[144:147],v255 offset:45056\n"
M_"v[112:115],a[160:163],v[136:139],0\n"
L_"v[8:11],v255,s[28:31],s63 offen offset:2048 sc1\n"
M_"v[112:115],a[164:167],v[140:143],v[112:115]\n"
L_"v[12:15],v255,s[28:31],s63 offen offset:3072 sc1\n"
M_"v[112:115],a[168:171],v[144:147],v[112:115]\n"
S_"s63,143360\n"
O_"v[216:219],v255,s[48:51],s87 offen sc1\n"
M_"v[112:115],a[172:175],v[148:151],v[112:115]\n"
A_"s87,s61,8192\n"
M_"v[116:119],a[172:175],v[164:167],0\n"
"s_sub_u32 s86,s87,9216\n"
L_"v[16:19],v255,s[28:31],s63 offen sc1\n"
M_"v[116:119],a[168:171],v[160:163],v[116:119]\n"
"s_min_u32 s87,s87,s86\n"
M_"v[116:119],a[164:167],v[156:159],v[116:119]\n"
A_"s87,s87,s84\n"
M_"v[116:119],a[160:163],v[152:155],v[116:119]\n"
W_"4)\n"
M_"v[120:123],a[128:131],v[136:139],0\n"
L_"v[20:23],v255,s[28:31],s63 offen offset:1024 sc1\n"
M_"v[120:123],a[132:135],v[140:143],v[120:123]\n"
M_"v[120:123],a[136:139],v[144:147],v[120:123]\n"
W_"0)\n"
"s_barrier\n"
B_"a[160:163],v249 offset:8192\n"
M_"v[120:123],a[140:143],v[148:151],v[120:123]\n"
B_"a[164:167],v249 offset:17408\n"
M_"v[124:127],a[140:143],v[164:167],0\n"
B_"a[168:171],v249 offset:26624\n"
M_"v[124:127],a[136:139],v[160:163],v[124:127]\n"
B_"a[172:175],v249 offset:35840\n"
M_"v[124:127],a[132:135],v[156:159],v[124:127]\n"
B_"a[132:135],v249 offset:18432\n"
M_"v[124:127],a[128:131],v[152:155],v[124:127]\n"
B_"a[128:131],v249 offset:9216\n"
M_"v[128:131],a[144:147],v[136:139],0\n"
B_"a[136:139],v249 offset:27648\n"
M_"v[128:131],a[148:151],v[140:143],v[128:131]\n"
B_"a[140:143],v249 offset:36864\n"
M_"v[128:131],a[152:155],v[144:147],v[128:131]\n"
L_"v[24:27],v255,s[28:31],s63 offen offset:2048 sc1\n"
M_"v[128:131],a[156:159],v[148:151],v[128:131]\n"
L_"v[28:31],v255,s[28:31],s63 offen offset:3072 sc1\n"
M_"v[132:135],a[156:159],v[164:167],0\n"
S_"s63,16384\n"
B_"a[156:159],v249 offset:37888\n"
M_"v[132:135],a[152:155],v[160:163],v[132:135]\n"
B_"a[152:155],v249 offset:28672\n"
M_"v[132:135],a[148:151],v[156:159],v[132:135]\n"
B_"a[148:151],v249 offset:19456\n"
M_"v[132:135],a[144:147],v[152:155],v[132:135]\n"
"s_waitcnt vmcnt(47) lgkmcnt(10)\n"
B_"a[144:147],v249 offset:10240\n"
M_"v[64:67],a[160:163],a[192:195],v[64:67]\n"
"s_waitcnt vmcnt(46) lgkmcnt(10)\n"
O_"v[224:227],v255,s[48:51],s87 offen sc1\n"
M_"v[64:67],a[164:167],a[196:199],v[64:67]\n"
"s_waitcnt vmcnt(46) lgkmcnt(9)\n"
L_"v255,s[24:27],s62 offen sc1 lds\n"
M_"v[64:67],a[168:171],a[200:203],v[64:67]\n"
"s_waitcnt vmcnt(46) lgkmcnt(8)\n"
A_"s87,s61,2048\n"
M_"v[64:67],a[172:175],a[204:207],v[64:67]\n"
"s_waitcnt vmcnt(42)\n"
"s_sub_u32 s86,s87,9216\n"
M_"v[68:71],a[172:175],a[220:223],v[68:71]\n"
"s_min_u32 s87,s87,s86\n"
B_"a[172:175],v249 offset:38912\n"
M_"v[68:71],a[168:171],a[216:219],v[68:71]\n"
A_"s87,s87,s84\n"
B_"a[168:171],v249 offset:29696\n"
M_"v[68:71],a[164:167],a[212:215],v[68:71]\n"
"s_xor_b32 s86,s60,1073741824\n"
B_"a[164:167],v249 offset:20480\n"
M_"v[68:71],a[160:163],a[208:211],v[68:71]\n"
W_"9)\n"
B_"a[160:163],v249 offset:11264\n"
M_"v[72:75],a[128:131],a[192:195],v[72:75]\n"
A_"s87,s87,s86\n"
L_"v255,s[24:27],s62 offen offset:1024 sc1 lds\n"
M_"v[72:75],a[132:135],a[196:199],v[72:75]\n"
W_"9)\n"
L_"v255,s[24:27],s62 offen offset:2048 sc1 lds\n"
M_"v[72:75],a[136:139],a[200:203],v[72:75]\n"
W_"8)\n"
A_"s87,s87,36864\n"
M_"v[72:75],a[140:143],a[204:207],v[72:75]\n"
M_"v[76:79],a[140:143],a[220:223],v[76:79]\n"
B_"a[140:143],v249 offset:39936\n"
M_"v[76:79],a[136:139],a[216:219],v[76:79]\n"
B_"a[136:139],v249 offset:30720\n"
M_"v[76:79],a[132:135],a[212:215],v[76:79]\n"
B_"a[132:135],v249 offset:21504\n"
M_"v[76:79],a[128:131],a[208:211],v[76:79]\n"
W_"7)\n"
B_"a[128:131],v249 offset:12288\n"
M_"v[80:83],a[144:147],a[192:195],v[80:83]\n"
L_"v255,s[24:27],s62 offen offset:3072 sc1 lds\n"
M_"v[80:83],a[148:151],a[196:199],v[80:83]\n"
A_"m0,s35,40960\n"
A_"s62,s34,5902336\n"
M_"v[80:83],a[152:155],a[200:203],v[80:83]\n"
L_"v255,s[24:27],s62 offen sc1 lds\n"
M_"v[80:83],a[156:159],a[204:207],v[80:83]\n"
M_"v[84:87],a[156:159],a[220:223],v[84:87]\n"
B_"a[156:159],v249 offset:40960\n"
M_"v[84:87],a[152:155],a[216:219],v[84:87]\n"
B_"a[152:155],v249 offset:31744\n"
M_"v[84:87],a[148:151],a[212:215],v[84:87]\n"
B_"a[148:151],v249 offset:22528\n"
M_"v[84:87],a[144:147],a[208:211],v[84:87]\n"
W_"7)\n"
B_"a[144:147],v249 offset:13312\n"
M_"v[88:91],a[160:163],a[192:195],v[88:91]\n"
L_"v255,s[24:27],s62 offen offset:1024 sc1 lds\n"
M_"v[88:91],a[164:167],a[196:199],v[88:91]\n"
L_"v255,s[24:27],s62 offen offset:2048 sc1 lds\n"
M_"v[88:91],a[168:171],a[200:203],v[88:91]\n"
M_"v[88:91],a[172:175],a[204:207],v[88:91]\n"
M_"v[92:95],a[172:175],a[220:223],v[92:95]\n"
B_"a[172:175],v249 offset:41984\n"
M_"v[92:95],a[168:171],a[216:219],v[92:95]\n"
B_"a[168:171],v249 offset:32768\n"
M_"v[92:95],a[164:167],a[212:215],v[92:95]\n"
B_"a[164:167],v249 offset:23552\n"
M_"v[92:95],a[160:163],a[208:211],v[92:95]\n"
W_"7)\n"
B_"a[160:163],v249 offset:14336\n"
M_"v[96:99],a[128:131],a[192:195],v[96:99]\n"
L_"v255,s[24:27],s62 offen offset:3072 sc1 lds\n"
M_"v[96:99],a[132:135],a[196:199],v[96:99]\n"
A_"m0,s35,45056\n"
A_"s62,s34,5906432\n"
M_"v[96:99],a[136:139],a[200:203],v[96:99]\n"
L_"v255,s[24:27],s62 offen sc1 lds\n"
M_"v[96:99],a[140:143],a[204:207],v[96:99]\n"
A_"m0,s35,73728\n"
A_"s62,s34,6045696\n"
M_"v[100:103],a[140:143],a[220:223],v[100:103]\n"
B_"a[140:143],v249 offset:43008\n"
M_"v[100:103],a[136:139],a[216:219],v[100:103]\n"
B_"a[136:139],v249 offset:33792\n"
M_"v[100:103],a[132:135],a[212:215],v[100:103]\n"
B_"a[132:135],v249 offset:24576\n"
M_"v[100:103],a[128:131],a[208:211],v[100:103]\n"
W_"7)\n"
B_"a[128:131],v249 offset:15360\n"
M_"v[104:107],a[144:147],a[192:195],v[104:107]\n"
L_"v[32:35],v255,s[28:31],s63 offen sc1\n"
M_"v[104:107],a[148:151],a[196:199],v[104:107]\n"
L_"v[36:39],v255,s[28:31],s63 offen offset:1024 sc1\n"
M_"v[104:107],a[152:155],a[200:203],v[104:107]\n"
M_"v[104:107],a[156:159],a[204:207],v[104:107]\n"
M_"v[108:111],a[156:159],a[220:223],v[108:111]\n"
B_"a[156:159],v249 offset:44032\n"
M_"v[108:111],a[152:155],a[216:219],v[108:111]\n"
B_"a[152:155],v249 offset:34816\n"
M_"v[108:111],a[148:151],a[212:215],v[108:111]\n"
B_"a[148:151],v249 offset:25600\n"
M_"v[108:111],a[144:147],a[208:211],v[108:111]\n"
W_"7)\n"
B_"a[144:147],v249 offset:16384\n"
M_"v[112:115],a[160:163],a[192:195],v[112:115]\n"
L_"v[40:43],v255,s[28:31],s63 offen offset:2048 sc1\n"
M_"v[112:115],a[164:167],a[196:199],v[112:115]\n"
L_"v[44:47],v255,s[28:31],s63 offen offset:3072 sc1\n"
M_"v[112:115],a[168:171],a[200:203],v[112:115]\n"
S_"s63,147456\n"
M_"v[112:115],a[172:175],a[204:207],v[112:115]\n"
M_"v[116:119],a[172:175],a[220:223],v[116:119]\n"
L_"v[48:51],v255,s[28:31],s63 offen sc1\n"
M_"v[116:119],a[168:171],a[216:219],v[116:119]\n"
M_"v[116:119],a[164:167],a[212:215],v[116:119]\n"
M_"v[116:119],a[160:163],a[208:211],v[116:119]\n"
W_"4)\n"
M_"v[120:123],a[128:131],a[192:195],v[120:123]\n"
L_"v[52:55],v255,s[28:31],s63 offen offset:1024 sc1\n"
M_"v[120:123],a[132:135],a[196:199],v[120:123]\n"
M_"v[120:123],a[136:139],a[200:203],v[120:123]\n"
"s_waitcnt vmcnt(46) lgkmcnt(0)\n"
"s_barrier\n"
B_"a[160:163],v249 offset:45056\n"
M_"v[120:123],a[140:143],a[204:207],v[120:123]\n"
B_"a[164:167],v249 offset:54272\n"
M_"v[124:127],a[140:143],a[220:223],v[124:127]\n"
B_"a[168:171],v249 offset:63488\n"
M_"v[124:127],a[136:139],a[216:219],v[124:127]\n"
B_"a[172:175],v242 offset:7168\n"
M_"v[124:127],a[132:135],a[212:215],v[124:127]\n"
B_"a[132:135],v249 offset:55296\n"
M_"v[124:127],a[128:131],a[208:211],v[124:127]\n"
B_"a[128:131],v249 offset:46080\n"
M_"v[128:131],a[144:147],a[192:195],v[128:131]\n"
B_"a[136:139],v249 offset:64512\n"
M_"v[128:131],a[148:151],a[196:199],v[128:131]\n"
B_"a[140:143],v242 offset:8192\n"
M_"v[128:131],a[152:155],a[200:203],v[128:131]\n"
L_"v[56:59],v255,s[28:31],s63 offen offset:2048 sc1\n"
M_"v[128:131],a[156:159],a[204:207],v[128:131]\n"
L_"v[60:63],v255,s[28:31],s63 offen offset:3072 sc1\n"
M_"v[132:135],a[156:159],a[220:223],v[132:135]\n"
S_"s63,20480\n"
B_"a[156:159],v242 offset:9216\n"
M_"v[132:135],a[152:155],a[216:219],v[132:135]\n"
B_"a[152:155],v242 offset:0\n"
M_"v[132:135],a[148:151],a[212:215],v[132:135]\n"
B_"a[148:151],v249 offset:56320\n"
M_"v[132:135],a[144:147],a[208:211],v[132:135]\n"
"s_waitcnt vmcnt(47) lgkmcnt(10)\n"
B_"a[144:147],v249 offset:47104\n"
M_"v[64:67],a[160:163],a[224:227],v[64:67]\n"
"s_waitcnt vmcnt(46) lgkmcnt(10)\n"
L_"v255,s[24:27],s62 offen sc1 lds\n"
M_"v[64:67],a[164:167],a[228:231],v[64:67]\n"
"s_waitcnt vmcnt(46) lgkmcnt(9)\n"
M_"v[64:67],a[168:171],a[232:235],v[64:67]\n"
"s_waitcnt vmcnt(45) lgkmcnt(8)\n"
M_"v[64:67],a[172:175],a[236:239],v[64:67]\n"
"s_waitcnt vmcnt(40)\n"
M_"v[68:71],a[172:175],a[252:255],v[68:71]\n"
B_"a[172:175],v242 offset:10240\n"
M_"v[68:71],a[168:171],a[248:251],v[68:71]\n"
B_"a[168:171],v242 offset:1024\n"
M_"v[68:71],a[164:167],a[244:247],v[68:71]\n"
B_"a[164:167],v249 offset:57344\n"
M_"v[68:71],a[160:163],a[240:243],v[68:71]\n"
W_"9)\n"
B_"a[160:163],v249 offset:48128\n"
M_"v[72:75],a[128:131],a[224:227],v[72:75]\n"
L_"v255,s[24:27],s62 offen offset:1024 sc1 lds\n"
M_"v[72:75],a[132:135],a[228:231],v[72:75]\n"
W_"9)\n"
L_"v255,s[24:27],s62 offen offset:2048 sc1 lds\n"
M_"v[72:75],a[136:139],a[232:235],v[72:75]\n"
W_"8)\n"
M_"v[72:75],a[140:143],a[236:239],v[72:75]\n"
M_"v[76:79],a[140:143],a[252:255],v[76:79]\n"
B_"a[140:143],v242 offset:11264\n"
M_"v[76:79],a[136:139],a[248:251],v[76:79]\n"
B_"a[136:139],v242 offset:2048\n"
M_"v[76:79],a[132:135],a[244:247],v[76:79]\n"
B_"a[132:135],v249 offset:58368\n"
M_"v[76:79],a[128:131],a[240:243],v[76:79]\n"
W_"7)\n"
B_"a[128:131],v249 offset:49152\n"
M_"v[80:83],a[144:147],a[224:227],v[80:83]\n"
L_"v255,s[24:27],s62 offen offset:3072 sc1 lds\n"
M_"v[80:83],a[148:151],a[228:231],v[80:83]\n"
A_"m0,s35,77824\n"
A_"s62,s34,6049792\n"
M_"v[80:83],a[152:155],a[232:235],v[80:83]\n"
L_"v255,s[24:27],s62 offen sc1 lds\n"
M_"v[80:83],a[156:159],a[236:239],v[80:83]\n"
M_"v[84:87],a[156:159],a[252:255],v[84:87]\n"
B_"a[156:159],v242 offset:12288\n"
M_"v[84:87],a[152:155],a[248:251],v[84:87]\n"
B_"a[152:155],v242 offset:3072\n"
M_"v[84:87],a[148:151],a[244:247],v[84:87]\n"
B_"a[148:151],v249 offset:59392\n"
M_"v[84:87],a[144:147],a[240:243],v[84:87]\n"
W_"7)\n"
B_"a[144:147],v249 offset:50176\n"
M_"v[88:91],a[160:163],a[224:227],v[88:91]\n"
L_"v255,s[24:27],s62 offen offset:1024 sc1 lds\n"
M_"v[88:91],a[164:167],a[228:231],v[88:91]\n"
L_"v255,s[24:27],s62 offen offset:2048 sc1 lds\n"
M_"v[88:91],a[168:171],a[232:235],v[88:91]\n"
M_"v[88:91],a[172:175],a[236:239],v[88:91]\n"
M_"v[92:95],a[172:175],a[252:255],v[92:95]\n"
B_"a[172:175],v242 offset:13312\n"
M_"v[92:95],a[168:171],a[248:251],v[92:95]\n"
B_"a[168:171],v242 offset:4096\n"
M_"v[92:95],a[164:167],a[244:247],v[92:95]\n"
B_"a[164:167],v249 offset:60416\n"
M_"v[92:95],a[160:163],a[240:243],v[92:95]\n"
W_"7)\n"
B_"a[160:163],v249 offset:51200\n"
M_"v[96:99],a[128:131],a[224:227],v[96:99]\n"
L_"v255,s[24:27],s62 offen offset:3072 sc1 lds\n"
M_"v[96:99],a[132:135],a[228:231],v[96:99]\n"
A_"m0,s35,81920\n"
A_"s62,s34,6053888\n"
M_"v[96:99],a[136:139],a[232:235],v[96:99]\n"
L_"v255,s[24:27],s62 offen sc1 lds\n"
M_"v[96:99],a[140:143],a[236:239],v[96:99]\n"
A_"m0,s35,110592\n"
A_"s62,s34,6193152\n"
M_"v[100:103],a[140:143],a[252:255],v[100:103]\n"
B_"a[140:143],v242 offset:14336\n"
M_"v[100:103],a[136:139],a[248:251],v[100:103]\n"
B_"a[136:139],v242 offset:5120\n"
M_"v[100:103],a[132:135],a[244:247],v[100:103]\n"
B_"a[132:135],v249 offset:61440\n"
M_"v[100:103],a[128:131],a[240:243],v[100:103]\n"
W_"7)\n"
B_"a[128:131],v249 offset:52224\n"
M_"v[104:107],a[144:147],a[224:227],v[104:107]\n"
L_"v[136:139],v255,s[28:31],s63 offen sc1\n"
M_"v[104:107],a[148:151],a[228:231],v[104:107]\n"
L_"v[140:143],v255,s[28:31],s63 offen offset:1024 sc1\n"
M_"v[104:107],a[152:155],a[232:235],v[104:107]\n"
M_"v[104:107],a[156:159],a[236:239],v[104:107]\n"
M_"v[108:111],a[156:159],a[252:255],v[108:111]\n"
B_"a[156:159],v242 offset:15360\n"
M_"v[108:111],a[152:155],a[248:251],v[108:111]\n"
B_"a[152:155],v242 offset:6144\n"
M_"v[108:111],a[148:151],a[244:247],v[108:111]\n"
B_"a[148:151],v249 offset:62464\n"
M_"v[108:111],a[144:147],a[240:243],v[108:111]\n"
W_"7)\n"
B_"a[144:147],v249 offset:53248\n"
M_"v[112:115],a[160:163],a[224:227],v[112:115]\n"
L_"v[144:147],v255,s[28:31],s63 offen offset:2048 sc1\n"
M_"v[112:115],a[164:167],a[228:231],v[112:115]\n"
L_"v[148:151],v255,s[28:31],s63 offen offset:3072 sc1\n"
M_"v[112:115],a[168:171],a[232:235],v[112:115]\n"
S_"s63,151552\n"
M_"v[112:115],a[172:175],a[236:239],v[112:115]\n"
M_"v[116:119],a[172:175],a[252:255],v[116:119]\n"
L_"v[152:155],v255,s[28:31],s63 offen sc1\n"
M_"v[116:119],a[168:171],a[248:251],v[116:119]\n"
M_"v[116:119],a[164:167],a[244:247],v[116:119]\n"
M_"v[116:119],a[160:163],a[240:243],v[116:119]\n"
W_"4)\n"
M_"v[120:123],a[128:131],a[224:227],v[120:123]\n"
L_"v[156:159],v255,s[28:31],s63 offen offset:1024 sc1\n"
M_"v[120:123],a[132:135],a[228:231],v[120:123]\n"
M_"v[120:123],a[136:139],a[232:235],v[120:123]\n"
"s_waitcnt vmcnt(32) lgkmcnt(0)\n"
"s_barrier\n"
B_"a[160:163],v255 offset:0\n"
M_"v[120:123],a[140:143],a[236:239],v[120:123]\n"
B_"a[164:167],v255 offset:9216\n"
M_"v[124:127],a[140:143],a[252:255],v[124:127]\n"
B_"a[168:171],v255 offset:18432\n"
M_"v[124:127],a[136:139],a[248:251],v[124:127]\n"
B_"a[172:175],v255 offset:27648\n"
M_"v[124:127],a[132:135],a[244:247],v[124:127]\n"
B_"a[132:135],v255 offset:10240\n"
M_"v[124:127],a[128:131],a[240:243],v[124:127]\n"
B_"a[128:131],v255 offset:1024\n"
M_"v[128:131],a[144:147],a[224:227],v[128:131]\n"
B_"a[136:139],v255 offset:19456\n"
M_"v[128:131],a[148:151],a[228:231],v[128:131]\n"
B_"a[140:143],v255 offset:28672\n"
M_"v[128:131],a[152:155],a[232:235],v[128:131]\n"
"buffer_store_dwordx2 v[244:245],v243,s[56:59],s88 offen sc1\n"
M_"v[128:131],a[156:159],a[236:239],v[128:131]\n"
L_"v[160:163],v255,s[28:31],s63 offen offset:2048 sc1\n"
M_"v[132:135],a[156:159],a[252:255],v[132:135]\n"
B_"a[156:159],v255 offset:29696\n"
M_"v[132:135],a[152:155],a[248:251],v[132:135]\n"
B_"a[152:155],v255 offset:20480\n"
M_"v[132:135],a[148:151],a[244:247],v[132:135]\n"
B_"a[148:151],v255 offset:11264\n"
M_"v[132:135],a[144:147],a[240:243],v[132:135]\n"
W_"10)\n"
B_"a[144:147],v255 offset:2048\n"
M_"v[64:67],a[160:163],v[0:3],v[64:67]\n"
W_"10)\n"
L_"v[164:167],v255,s[28:31],s63 offen offset:3072 sc1\n"
M_"v[64:67],a[164:167],v[4:7],v[64:67]\n"
W_"9)\n"
L_"v255,s[24:27],s62 offen sc1 lds\n"
M_"v[64:67],a[168:171],v[8:11],v[64:67]\n"
W_"8)\n"
S_"s63,24576\n"
M_"v[64:67],a[172:175],v[12:15],v[64:67]\n"
M_"v[68:71],a[172:175],v[28:31],v[68:71]\n"
B_"a[172:175],v255 offset:30720\n"
M_"v[68:71],a[168:171],v[24:27],v[68:71]\n"
B_"a[168:171],v255 offset:21504\n"
M_"v[68:71],a[164:167],v[20:23],v[68:71]\n"
B_"a[164:167],v255 offset:12288\n"
M_"v[68:71],a[160:163],v[16:19],v[68:71]\n"
W_"9)\n"
B_"a[160:163],v255 offset:3072\n"
M_"v[72:75],a[128:131],v[0:3],v[72:75]\n"
L_"v255,s[24:27],s62 offen offset:1024 sc1 lds\n"
M_"v[72:75],a[132:135],v[4:7],v[72:75]\n"
W_"9)\n"
L_"v255,s[24:27],s62 offen offset:2048 sc1 lds\n"
M_"v[72:75],a[136:139],v[8:11],v[72:75]\n"
W_"8)\n"
M_"v[72:75],a[140:143],v[12:15],v[72:75]\n"
M_"v[76:79],a[140:143],v[28:31],v[76:79]\n"
B_"a[140:143],v255 offset:31744\n"
M_"v[76:79],a[136:139],v[24:27],v[76:79]\n"
B_"a[136:139],v255 offset:22528\n"
M_"v[76:79],a[132:135],v[20:23],v[76:79]\n"
B_"a[132:135],v255 offset:13312\n"
M_"v[76:79],a[128:131],v[16:19],v[76:79]\n"
W_"7)\n"
B_"a[128:131],v255 offset:4096\n"
M_"v[80:83],a[144:147],v[0:3],v[80:83]\n"
L_"v255,s[24:27],s62 offen offset:3072 sc1 lds\n"
M_"v[80:83],a[148:151],v[4:7],v[80:83]\n"
A_"m0,s35,114688\n"
A_"s62,s34,6197248\n"
M_"v[80:83],a[152:155],v[8:11],v[80:83]\n"
L_"v255,s[24:27],s62 offen sc1 lds\n"
M_"v[80:83],a[156:159],v[12:15],v[80:83]\n"
M_"v[84:87],a[156:159],v[28:31],v[84:87]\n"
B_"a[156:159],v255 offset:32768\n"
M_"v[84:87],a[152:155],v[24:27],v[84:87]\n"
B_"a[152:155],v255 offset:23552\n"
M_"v[84:87],a[148:151],v[20:23],v[84:87]\n"
B_"a[148:151],v255 offset:14336\n"
M_"v[84:87],a[144:147],v[16:19],v[84:87]\n"
W_"7)\n"
B_"a[144:147],v255 offset:5120\n"
M_"v[88:91],a[160:163],v[0:3],v[88:91]\n"
L_"v255,s[24:27],s62 offen offset:1024 sc1 lds\n"
M_"v[88:91],a[164:167],v[4:7],v[88:91]\n"
L_"v255,s[24:27],s62 offen offset:2048 sc1 lds\n"
M_"v[88:91],a[168:171],v[8:11],v[88:91]\n"
M_"v[88:91],a[172:175],v[12:15],v[88:91]\n"
M_"v[92:95],a[172:175],v[28:31],v[92:95]\n"
B_"a[172:175],v255 offset:33792\n"
M_"v[92:95],a[168:171],v[24:27],v[92:95]\n"
B_"a[168:171],v255 offset:24576\n"
M_"v[92:95],a[164:167],v[20:23],v[92:95]\n"
B_"a[164:167],v255 offset:15360\n"
M_"v[92:95],a[160:163],v[16:19],v[92:95]\n"
W_"7)\n"
B_"a[160:163],v255 offset:6144\n"
M_"v[96:99],a[128:131],v[0:3],v[96:99]\n"
L_"v255,s[24:27],s62 offen offset:3072 sc1 lds\n"
M_"v[96:99],a[132:135],v[4:7],v[96:99]\n"
A_"m0,s35,118784\n"
A_"s62,s34,6201344\n"
M_"v[96:99],a[136:139],v[8:11],v[96:99]\n"
L_"v255,s[24:27],s62 offen sc1 lds\n"
M_"v[96:99],a[140:143],v[12:15],v[96:99]\n"
A_"m0,s35,0\n"
A_"s62,s34,6340608\n"
M_"v[100:103],a[140:143],v[28:31],v[100:103]\n"
B_"a[140:143],v255 offset:34816\n"
M_"v[100:103],a[136:139],v[24:27],v[100:103]\n"
B_"a[136:139],v255 offset:25600\n"
M_"v[100:103],a[132:135],v[20:23],v[100:103]\n"
B_"a[132:135],v255 offset:16384\n"
M_"v[100:103],a[128:131],v[16:19],v[100:103]\n"
W_"7)\n"
B_"a[128:131],v255 offset:7168\n"
M_"v[104:107],a[144:147],v[0:3],v[104:107]\n"
L_"a[192:195],v255,s[28:31],s63 offen sc1\n"
M_"v[104:107],a[148:151],v[4:7],v[104:107]\n"
L_"a[196:199],v255,s[28:31],s63 offen offset:1024 sc1\n"
M_"v[104:107],a[152:155],v[8:11],v[104:107]\n"
M_"v[104:107],a[156:159],v[12:15],v[104:107]\n"
M_"v[108:111],a[156:159],v[28:31],v[108:111]\n"
B_"a[156:159],v255 offset:35840\n"
M_"v[108:111],a[152:155],v[24:27],v[108:111]\n"
B_"a[152:155],v255 offset:26624\n"
M_"v[108:111],a[148:151],v[20:23],v[108:111]\n"
B_"a[148:151],v255 offset:17408\n"
M_"v[108:111],a[144:147],v[16:19],v[108:111]\n"
W_"7)\n"
B_"a[144:147],v255 offset:8192\n"
M_"v[112:115],a[160:163],v[0:3],v[112:115]\n"
L_"a[200:203],v255,s[28:31],s63 offen offset:2048 sc1\n"
M_"v[112:115],a[164:167],v[4:7],v[112:115]\n"
L_"a[204:207],v255,s[28:31],s63 offen offset:3072 sc1\n"
M_"v[112:115],a[168:171],v[8:11],v[112:115]\n"
S_"s63,155648\n"
M_"v[112:115],a[172:175],v[12:15],v[112:115]\n"
M_"v[116:119],a[172:175],v[28:31],v[116:119]\n"
L_"a[208:211],v255,s[28:31],s63 offen sc1\n"
M_"v[116:119],a[168:171],v[24:27],v[116:119]\n"
M_"v[116:119],a[164:167],v[20:23],v[116:119]\n"
M_"v[116:119],a[160:163],v[16:19],v[116:119]\n"
W_"4)\n"
M_"v[120:123],a[128:131],v[0:3],v[120:123]\n"
L_"a[212:215],v255,s[28:31],s63 offen offset:1024 sc1\n"
M_"v[120:123],a[132:135],v[4:7],v[120:123]\n"
M_"v[120:123],a[136:139],v[8:11],v[120:123]\n"
"s_waitcnt vmcnt(41) lgkmcnt(0)\n"
"s_barrier\n"
B_"a[160:163],v255 offset:36864\n"
M_"v[120:123],a[140:143],v[12:15],v[120:123]\n"
B_"a[164:167],v255 offset:46080\n"
M_"v[124:127],a[140:143],v[28:31],v[124:127]\n"
B_"a[168:171],v255 offset:55296\n"
M_"v[124:127],a[136:139],v[24:27],v[124:127]\n"
B_"a[172:175],v255 offset:64512\n"
M_"v[124:127],a[132:135],v[20:23],v[124:127]\n"
B_"a[132:135],v255 offset:47104\n"
M_"v[124:127],a[128:131],v[16:19],v[124:127]\n"
B_"a[128:131],v255 offset:37888\n"
M_"v[128:131],a[144:147],v[0:3],v[128:131]\n"
B_"a[136:139],v255 offset:56320\n"
M_"v[128:131],a[148:151],v[4:7],v[128:131]\n"
B_"a[140:143],v249 offset:0\n"
M_"v[128:131],a[152:155],v[8:11],v[128:131]\n"
L_"a[216:219],v255,s[28:31],s63 offen offset:2048 sc1\n"
M_"v[128:131],a[156:159],v[12:15],v[128:131]\n"
L_"a[220:223],v255,s[28:31],s63 offen offset:3072 sc1\n"
M_"v[132:135],a[156:159],v[28:31],v[132:135]\n"
S_"s63,28672\n"
B_"a[156:159],v249 offset:1024\n"
M_"v[132:135],a[152:155],v[24:27],v[132:135]\n"
B_"a[152:155],v255 offset:57344\n"
M_"v[132:135],a[148:151],v[20:23],v[132:135]\n"
B_"a[148:151],v255 offset:48128\n"
M_"v[132:135],a[144:147],v[16:19],v[132:135]\n"
"s_waitcnt vmcnt(42) lgkmcnt(10)\n"
B_"a[144:147],v255 offset:38912\n"
M_"v[64:67],a[160:163],v[32:35],v[64:67]\n"
"s_waitcnt vmcnt(41) lgkmcnt(10)\n"
L_"v255,s[24:27],s62 offen sc1 lds\n"
M_"v[64:67],a[164:167],v[36:39],v[64:67]\n"
"s_waitcnt vmcnt(41) lgkmcnt(9)\n"
M_"v[64:67],a[168:171],v[40:43],v[64:67]\n"
"s_waitcnt vmcnt(40) lgkmcnt(8)\n"
M_"v[64:67],a[172:175],v[44:47],v[64:67]\n"
"s_waitcnt vmcnt(36)\n"
M_"v[68:71],a[172:175],v[60:63],v[68:71]\n"
B_"a[172:175],v249 offset:2048\n"
M_"v[68:71],a[168:171],v[56:59],v[68:71]\n"
B_"a[168:171],v255 offset:58368\n"
M_"v[68:71],a[164:167],v[52:55],v[68:71]\n"
B_"a[164:167],v255 offset:49152\n"
M_"v[68:71],a[160:163],v[48:51],v[68:71]\n"
W_"9)\n"
B_"a[160:163],v255 offset:39936\n"
M_"v[72:75],a[128:131],v[32:35],v[72:75]\n"
L_"v255,s[24:27],s62 offen offset:1024 sc1 lds\n"
M_"v[72:75],a[132:135],v[36:39],v[72:75]\n"
W_"9)\n"
L_"v255,s[24:27],s62 offen offset:2048 sc1 lds\n"
M_"v[72:75],a[136:139],v[40:43],v[72:75]\n"
W_"8)\n"
M_"v[72:75],a[140:143],v[44:47],v[72:75]\n"
M_"v[76:79],a[140:143],v[60:63],v[76:79]\n"
B_"a[140:143],v249 offset:3072\n"
M_"v[76:79],a[136:139],v[56:59],v[76:79]\n"
B_"a[136:139],v255 offset:59392\n"
M_"v[76:79],a[132:135],v[52:55],v[76:79]\n"
B_"a[132:135],v255 offset:50176\n"
M_"v[76:79],a[128:131],v[48:51],v[76:79]\n"
W_"7)\n"
B_"a[128:131],v255 offset:40960\n"
M_"v[80:83],a[144:147],v[32:35],v[80:83]\n"
L_"v255,s[24:27],s62 offen offset:3072 sc1 lds\n"
M_"v[80:83],a[148:151],v[36:39],v[80:83]\n"
A_"m0,s35,4096\n"
A_"s62,s34,6344704\n"
M_"v[80:83],a[152:155],v[40:43],v[80:83]\n"
L_"v255,s[24:27],s62 offen sc1 lds\n"
M_"v[80:83],a[156:159],v[44:47],v[80:83]\n"
M_"v[84:87],a[156:159],v[60:63],v[84:87]\n"
B_"a[156:159],v249 offset:4096\n"
M_"v[84:87],a[152:155],v[56:59],v[84:87]\n"
B_"a[152:155],v255 offset:60416\n"
M_"v[84:87],a[148:151],v[52:55],v[84:87]\n"
B_"a[148:151],v255 offset:51200\n"
M_"v[84:87],a[144:147],v[48:51],v[84:87]\n"
W_"7)\n"
B_"a[144:147],v255 offset:41984\n"
M_"v[88:91],a[160:163],v[32:35],v[88:91]\n"
L_"v255,s[24:27],s62 offen offset:1024 sc1 lds\n"
M_"v[88:91],a[164:167],v[36:39],v[88:91]\n"
L_"v255,s[24:27],s62 offen offset:2048 sc1 lds\n"
M_"v[88:91],a[168:171],v[40:43],v[88:91]\n"
M_"v[88:91],a[172:175],v[44:47],v[88:91]\n"
M_"v[92:95],a[172:175],v[60:63],v[92:95]\n"
B_"a[172:175],v249 offset:5120\n"
M_"v[92:95],a[168:171],v[56:59],v[92:95]\n"
B_"a[168:171],v255 offset:61440\n"
M_"v[92:95],a[164:167],v[52:55],v[92:95]\n"
B_"a[164:167],v255 offset:52224\n"
M_"v[92:95],a[160:163],v[48:51],v[92:95]\n"
W_"7)\n"
B_"a[160:163],v255 offset:43008\n"
M_"v[96:99],a[128:131],v[32:35],v[96:99]\n"
L_"v255,s[24:27],s62 offen offset:3072 sc1 lds\n"
M_"v[96:99],a[132:135],v[36:39],v[96:99]\n"
A_"m0,s35,8192\n"
A_"s62,s34,6348800\n"
M_"v[96:99],a[136:139],v[40:43],v[96:99]\n"
L_"v255,s[24:27],s62 offen sc1 lds\n"
M_"v[96:99],a[140:143],v[44:47],v[96:99]\n"
M_"v[100:103],a[140:143],v[60:63],v[100:103]\n"
B_"a[140:143],v249 offset:6144\n"
M_"v[100:103],a[136:139],v[56:59],v[100:103]\n"
B_"a[136:139],v255 offset:62464\n"
M_"v[100:103],a[132:135],v[52:55],v[100:103]\n"
B_"a[132:135],v255 offset:53248\n"
M_"v[100:103],a[128:131],v[48:51],v[100:103]\n"
W_"7)\n"
B_"a[128:131],v255 offset:44032\n"
M_"v[104:107],a[144:147],v[32:35],v[104:107]\n"
L_"a[224:227],v255,s[28:31],s63 offen sc1\n"
M_"v[104:107],a[148:151],v[36:39],v[104:107]\n"
"buffer_load_dwordx2 v[232:233],v252,s[56:59],s85 offen sc1\n"
M_"v[104:107],a[152:155],v[40:43],v[104:107]\n"
L_"a[228:231],v255,s[28:31],s63 offen offset:1024 sc1\n"
M_"v[104:107],a[156:159],v[44:47],v[104:107]\n"
M_"v[108:111],a[156:159],v[60:63],v[108:111]\n"
B_"a[156:159],v249 offset:7168\n"
M_"v[108:111],a[152:155],v[56:59],v[108:111]\n"
B_"a[152:155],v255 offset:63488\n"
M_"v[108:111],a[148:151],v[52:55],v[108:111]\n"
B_"a[148:151],v255 offset:54272\n"
M_"v[108:111],a[144:147],v[48:51],v[108:111]\n"
W_"7)\n"
B_"a[144:147],v255 offset:45056\n"
M_"v[112:115],a[160:163],v[32:35],v[112:115]\n"
L_"a[232:235],v255,s[28:31],s63 offen offset:2048 sc1\n"
M_"v[112:115],a[164:167],v[36:39],v[112:115]\n"
L_"a[236:239],v255,s[28:31],s63 offen offset:3072 sc1\n"
M_"v[112:115],a[168:171],v[40:43],v[112:115]\n"
S_"s63,159744\n"
M_"v[112:115],a[172:175],v[44:47],v[112:115]\n"
M_"v[116:119],a[172:175],v[60:63],v[116:119]\n"
L_"a[240:243],v255,s[28:31],s63 offen sc1\n"
M_"v[116:119],a[168:171],v[56:59],v[116:119]\n"
M_"v[116:119],a[164:167],v[52:55],v[116:119]\n"
M_"v[116:119],a[160:163],v[48:51],v[116:119]\n"
W_"4)\n"
M_"v[120:123],a[128:131],v[32:35],v[120:123]\n"
L_"a[244:247],v255,s[28:31],s63 offen offset:1024 sc1\n"
M_"v[120:123],a[132:135],v[36:39],v[120:123]\n"
M_"v[120:123],a[136:139],v[40:43],v[120:123]\n"
"s_waitcnt vmcnt(42) lgkmcnt(0)\n"
"s_barrier\n"
B_"a[160:163],v249 offset:8192\n"
M_"v[120:123],a[140:143],v[44:47],v[120:123]\n"
B_"a[164:167],v249 offset:17408\n"
M_"v[124:127],a[140:143],v[60:63],v[124:127]\n"
B_"a[168:171],v249 offset:26624\n"
M_"v[124:127],a[136:139],v[56:59],v[124:127]\n"
B_"a[172:175],v249 offset:35840\n"
M_"v[124:127],a[132:135],v[52:55],v[124:127]\n"
B_"a[132:135],v249 offset:18432\n"
M_"v[124:127],a[128:131],v[48:51],v[124:127]\n"
B_"a[128:131],v249 offset:9216\n"
M_"v[128:131],a[144:147],v[32:35],v[128:131]\n"
B_"a[136:139],v249 offset:27648\n"
M_"v[128:131],a[148:151],v[36:39],v[128:131]\n"
B_"a[140:143],v249 offset:36864\n"
M_"v[128:131],a[152:155],v[40:43],v[128:131]\n"
L_"a[248:251],v255,s[28:31],s63 offen offset:2048 sc1\n"
M_"v[128:131],a[156:159],v[44:47],v[128:131]\n"
L_"a[252:255],v255,s[28:31],s63 offen offset:3072 sc1\n"
M_"v[132:135],a[156:159],v[60:63],v[132:135]\n"
B_"a[156:159],v249 offset:37888\n"
M_"v[132:135],a[152:155],v[56:59],v[132:135]\n"
B_"a[152:155],v249 offset:28672\n"
M_"v[132:135],a[148:151],v[52:55],v[132:135]\n"
B_"a[148:151],v249 offset:19456\n"
M_"v[132:135],a[144:147],v[48:51],v[132:135]\n"
"s_waitcnt vmcnt(7)\n"
"v_cmp_eq_u64 vcc,s[66:67],v[232:233]\n"
"s_nop 3\n"
"s_cmp_eq_u64 vcc,-1\n"
"s_cbranch_scc0 SPING_%=\n"
"BACKG_%=:\n"
W_"10)\n"
M_"v[64:67],a[160:163],v[136:139],v[64:67]\n"
W_"9)\n"
B_"a[144:147],v249 offset:10240\n"
M_"v[64:67],a[164:167],v[140:143],v[64:67]\n"
W_"9)\n"
"buffer_load_dwordx2 v[0:1],v248,s[40:43],0 offen\n"
M_"v[64:67],a[168:171],v[144:147],v[64:67]\n"
W_"8)\n"
"buffer_load_dwordx2 v[2:3],v247,s[40:43],0 offen\n"
M_"v[64:67],a[172:175],v[148:151],v[64:67]\n"
L_"v[168:171],v255,s[48:51],s90 offen sc1\n"
M_"v[68:71],a[172:175],v[164:167],v[68:71]\n"
B_"a[172:175],v249 offset:38912\n"
M_"v[68:71],a[168:171],v[160:163],v[68:71]\n"
B_"a[168:171],v249 offset:29696\n"
M_"v[68:71],a[164:167],v[156:159],v[68:71]\n"
B_"a[164:167],v249 offset:20480\n"
M_"v[68:71],a[160:163],v[152:155],v[68:71]\n"
W_"9)\n"
B_"a[160:163],v249 offset:11264\n"
M_"v[72:75],a[128:131],v[136:139],v[72:75]\n"
"buffer_load_dwordx2 v[4:5],v248,s[40:43],0 offen offset:32\n"
M_"v[72:75],a[132:135],v[140:143],v[72:75]\n"
W_"9)\n"
L_"v[180:183],v255,s[48:51],s90 offen offset:1024 sc1\n"
M_"v[72:75],a[136:139],v[144:147],v[72:75]\n"
W_"8)\n"
"buffer_load_dwordx2 v[6:7],v247,s[40:43],0 offen offset:32\n"
M_"v[72:75],a[140:143],v[148:151],v[72:75]\n"
A_"s90,s90,36864\n"
L_"v[192:195],v255,s[48:51],s93 offen offset:2048 sc1\n"
M_"v[76:79],a[140:143],v[164:167],v[76:79]\n"
A_"s93,s93,36864\n"
B_"a[140:143],v249 offset:39936\n"
M_"v[76:79],a[136:139],v[160:163],v[76:79]\n"
B_"a[136:139],v249 offset:30720\n"
M_"v[76:79],a[132:135],v[156:159],v[76:79]\n"
B_"a[132:135],v249 offset:21504\n"
M_"v[76:79],a[128:131],v[152:155],v[76:79]\n"
W_"7)\n"
B_"a[128:131],v249 offset:12288\n"
M_"v[80:83],a[144:147],v[136:139],v[80:83]\n"
"buffer_load_dwordx2 v[8:9],v248,s[40:43],s60 offen offset:64\n"
M_"v[80:83],a[148:151],v[140:143],v[80:83]\n"
L_"v[172:175],v255,s[48:51],s91 offen sc1\n"
M_"v[80:83],a[152:155],v[144:147],v[80:83]\n"
"buffer_load_dwordx2 v[10:11],v247,s[40:43],s60 offen offset:64\n"
M_"v[80:83],a[156:159],v[148:151],v[80:83]\n"
L_"v[184:187],v255,s[48:51],s91 offen offset:1024 sc1\n"
M_"v[84:87],a[156:159],v[164:167],v[84:87]\n"
A_"s91,s91,36864\n"
B_"a[156:159],v249 offset:40960\n"
M_"v[84:87],a[152:155],v[160:163],v[84:87]\n"
B_"a[152:155],v249 offset:31744\n"
M_"v[84:87],a[148:151],v[156:159],v[84:87]\n"
B_"a[148:151],v249 offset:22528\n"
M_"v[84:87],a[144:147],v[152:155],v[84:87]\n"
W_"7)\n"
B_"a[144:147],v249 offset:13312\n"
M_"v[88:91],a[160:163],v[136:139],v[88:91]\n"
L_"v[196:199],v255,s[48:51],s94 offen offset:2048 sc1\n"
M_"v[88:91],a[164:167],v[140:143],v[88:91]\n"
A_"s94,s94,36864\n"
L_"v[176:179],v255,s[48:51],s92 offen sc1\n"
M_"v[88:91],a[168:171],v[144:147],v[88:91]\n"
M_"v[88:91],a[172:175],v[148:151],v[88:91]\n"
M_"v[92:95],a[172:175],v[164:167],v[92:95]\n"
B_"a[172:175],v249 offset:41984\n"
M_"v[92:95],a[168:171],v[160:163],v[92:95]\n"
B_"a[168:171],v249 offset:32768\n"
M_"v[92:95],a[164:167],v[156:159],v[92:95]\n"
B_"a[164:167],v249 offset:23552\n"
M_"v[92:95],a[160:163],v[152:155],v[92:95]\n"
W_"7)\n"
B_"a[160:163],v249 offset:14336\n"
M_"v[96:99],a[128:131],v[136:139],v[96:99]\n"
L_"v[188:191],v255,s[48:51],s92 offen offset:1024 sc1\n"
M_"v[96:99],a[132:135],v[140:143],v[96:99]\n"
A_"s92,s92,36864\n"
L_"v[200:203],v255,s[48:51],s95 offen offset:2048 sc1\n"
M_"v[96:99],a[136:139],v[144:147],v[96:99]\n"
A_"s95,s95,36864\n"
M_"v[96:99],a[140:143],v[148:151],v[96:99]\n"
M_"v[100:103],a[140:143],v[164:167],v[100:103]\n"
B_"a[140:143],v249 offset:43008\n"
M_"v[100:103],a[136:139],v[160:163],v[100:103]\n"
B_"a[136:139],v249 offset:33792\n"
M_"v[100:103],a[132:135],v[156:159],v[100:103]\n"
B_"a[132:135],v249 offset:24576\n"
M_"v[100:103],a[128:131],v[152:155],v[100:103]\n"
W_"7)\n"
B_"a[128:131],v249 offset:15360\n"
M_"v[104:107],a[144:147],v[136:139],v[104:107]\n"
M_"v[104:107],a[148:151],v[140:143],v[104:107]\n"
M_"v[104:107],a[152:155],v[144:147],v[104:107]\n"
M_"v[104:107],a[156:159],v[148:151],v[104:107]\n"
M_"v[108:111],a[156:159],v[164:167],v[108:111]\n"
B_"a[156:159],v249 offset:44032\n"
M_"v[108:111],a[152:155],v[160:163],v[108:111]\n"
B_"a[152:155],v249 offset:34816\n"
M_"v[108:111],a[148:151],v[156:159],v[108:111]\n"
B_"a[148:151],v249 offset:25600\n"
M_"v[108:111],a[144:147],v[152:155],v[108:111]\n"
W_"7)\n"
B_"a[144:147],v249 offset:16384\n"
M_"v[112:115],a[160:163],v[136:139],v[112:115]\n"
M_"v[112:115],a[164:167],v[140:143],v[112:115]\n"
M_"v[112:115],a[168:171],v[144:147],v[112:115]\n"
M_"v[112:115],a[172:175],v[148:151],v[112:115]\n"
M_"v[116:119],a[172:175],v[164:167],v[116:119]\n"
M_"v[116:119],a[168:171],v[160:163],v[116:119]\n"
M_"v[116:119],a[164:167],v[156:159],v[116:119]\n"
M_"v[116:119],a[160:163],v[152:155],v[116:119]\n"
W_"4)\n"
M_"v[120:123],a[128:131],v[136:139],v[120:123]\n"
M_"v[120:123],a[132:135],v[140:143],v[120:123]\n"
M_"v[120:123],a[136:139],v[144:147],v[120:123]\n"
W_"0)\n"
"s_barrier\n"
B_"a[160:163],v249 offset:47104\n"
M_"v[120:123],a[140:143],v[148:151],v[120:123]\n"
B_"a[164:167],v249 offset:56320\n"
M_"v[124:127],a[140:143],v[164:167],v[124:127]\n"
B_"a[168:171],v242 offset:0\n"
M_"v[124:127],a[136:139],v[160:163],v[124:127]\n"
B_"a[172:175],v242 offset:9216\n"
M_"v[124:127],a[132:135],v[156:159],v[124:127]\n"
B_"a[132:135],v255 offset:11264\n"
M_"v[124:127],a[128:131],v[152:155],v[124:127]\n"
B_"a[128:131],v255 offset:2048\n"
M_"v[128:131],a[144:147],v[136:139],v[128:131]\n"
B_"a[136:139],v255 offset:20480\n"
M_"v[128:131],a[148:151],v[140:143],v[128:131]\n"
B_"a[140:143],v255 offset:29696\n"
M_"v[128:131],a[152:155],v[144:147],v[128:131]\n"
M_"v[128:131],a[156:159],v[148:151],v[128:131]\n"
M_"v[132:135],a[156:159],v[164:167],v[132:135]\n"
B_"a[156:159],v242 offset:10240\n"
"s_waitcnt vmcnt(14)\n"
"v_lshlrev_b32 v208,16,v0\n"
M_"v[132:135],a[152:155],v[160:163],v[132:135]\n"
B_"a[152:155],v242 offset:1024\n"
"v_and_b32 v209,0xffff0000,v0\n"
M_"v[132:135],a[148:151],v[156:159],v[132:135]\n"
B_"a[148:151],v249 offset:57344\n"
"v_lshlrev_b32 v210,16,v1\n"
M_"v[132:135],a[144:147],v[152:155],v[132:135]\n"
W_"10)\n"
B_"a[144:147],v249 offset:48128\n"
"v_and_b32 v211,0xffff0000,v1\n"
M_"v[80:83],a[160:163],a[192:195],v[80:83]\n"
W_"10)\n"
"s_waitcnt vmcnt(13)\n"
"v_lshlrev_b32 v212,16,v2\n"
"v_and_b32 v213,0xffff0000,v2\n"
M_"v[80:83],a[164:167],a[196:199],v[80:83]\n"
W_"9)\n"
"v_lshlrev_b32 v214,16,v3\n"
"v_and_b32 v215,0xffff0000,v3\n"
M_"v[80:83],a[168:171],a[200:203],v[80:83]\n"
W_"8)\n"
"s_waitcnt vmcnt(11)\n"
"v_lshlrev_b32 v216,16,v4\n"
"v_and_b32 v217,0xffff0000,v4\n"
M_"v[80:83],a[172:175],a[204:207],v[80:83]\n"
"v_lshlrev_b32 v218,16,v5\n"
"v_and_b32 v219,0xffff0000,v5\n"
M_"v[84:87],a[172:175],a[220:223],v[84:87]\n"
B_"a[172:175],v255 offset:30720\n"
"s_waitcnt vmcnt(9)\n"
"v_lshlrev_b32 v220,16,v6\n"
M_"v[84:87],a[168:171],a[216:219],v[84:87]\n"
B_"a[168:171],v255 offset:21504\n"
"v_and_b32 v221,0xffff0000,v6\n"
M_"v[84:87],a[164:167],a[212:215],v[84:87]\n"
B_"a[164:167],v255 offset:12288\n"
"v_lshlrev_b32 v222,16,v7\n"
M_"v[84:87],a[160:163],a[208:211],v[84:87]\n"
W_"9)\n"
B_"a[160:163],v255 offset:3072\n"
"v_and_b32 v223,0xffff0000,v7\n"
M_"v[80:83],a[128:131],a[224:227],v[80:83]\n"
"s_waitcnt vmcnt(7)\n"
"v_lshlrev_b32 v224,16,v8\n"
"v_and_b32 v225,0xffff0000,v8\n"
M_"v[80:83],a[132:135],a[228:231],v[80:83]\n"
W_"9)\n"
"v_lshlrev_b32 v226,16,v9\n"
"v_and_b32 v227,0xffff0000,v9\n"
M_"v[80:83],a[136:139],a[232:235],v[80:83]\n"
W_"8)\n"
"s_waitcnt vmcnt(5)\n"
"v_lshlrev_b32 v228,16,v10\n"
"v_and_b32 v229,0xffff0000,v10\n"
M_"v[80:83],a[140:143],a[236:239],v[80:83]\n"
"v_lshlrev_b32 v230,16,v11\n"
"v_and_b32 v231,0xffff0000,v11\n"
M_"v[84:87],a[140:143],a[252:255],v[84:87]\n"
B_"a[140:143],v242 offset:11264\n"
M_"v[84:87],a[136:139],a[248:251],v[84:87]\n"
B_"a[136:139],v242 offset:2048\n"
M_"v[84:87],a[132:135],a[244:247],v[84:87]\n"
B_"a[132:135],v249 offset:58368\n"
M_"v[84:87],a[128:131],a[240:243],v[84:87]\n"
B_"a[128:131],v249 offset:49152\n"
C_"v204,v80,v81\n"
M_"a[0:3],v[236:239],v[168:171],a[0:3]\n"
C_"v205,v82,v83\n"
"v_min_u32 v204,-2,v204\n"
M_"a[0:3],v[236:239],v[172:175],a[0:3]\n"
"s_waitcnt vmcnt(2)\n"
"v_min_u32 v205,-2,v205\n"
C_"v206,v84,v85\n"
M_"a[0:3],v[236:239],v[176:179],a[0:3]\n"
C_"v207,v86,v87\n"
"v_min_u32 v206,-2,v206\n"
M_"a[4:7],v[234:237],v[168:171],a[4:7]\n"
"v_min_u32 v207,-2,v207\n"
M_"a[4:7],v[234:237],v[172:175],a[4:7]\n"
M_"a[4:7],v[234:237],v[176:179],a[4:7]\n"
W_"8)\n"
M_"v[88:91],a[144:147],a[192:195],v[88:91]\n"
O_"v[204:207],v255,s[48:51],s87 offen sc1\n"
M_"v[88:91],a[148:151],a[196:199],v[88:91]\n"
A_"s87,s61,3072\n"
R_"v168,a0\n"
R_"v169,a1\n"
M_"v[88:91],a[152:155],a[200:203],v[88:91]\n"
"s_sub_u32 s86,s87,9216\n"
R_"v170,a2\n"
R_"v171,a3\n"
M_"v[88:91],a[156:159],a[204:207],v[88:91]\n"
"s_min_u32 s87,s87,s86\n"
R_"v172,a4\n"
R_"v173,a5\n"
M_"v[92:95],a[156:159],a[220:223],v[92:95]\n"
A_"s87,s87,s84\n"
B_"a[156:159],v255 offset:31744\n"
R_"v174,a6\n"
M_"v[92:95],a[152:155],a[216:219],v[92:95]\n"
A_"s87,s87,36864\n"
B_"a[152:155],v255 offset:22528\n"
R_"v175,a7\n"
M_"v[92:95],a[148:151],a[212:215],v[92:95]\n"
B_"a[148:151],v255 offset:13312\n"
"v_fmamk_f32 v32,v168,0xc2b17218,v253\n"
M_"v[92:95],a[144:147],a[208:211],v[92:95]\n"
W_"7)\n"
B_"a[144:147],v255 offset:4096\n"
"v_fmamk_f32 v33,v169,0xc2b17218,v253\n"
M_"v[88:91],a[160:163],a[224:227],v[88:91]\n"
E_"v32,v32\n"
M_"v[88:91],a[164:167],a[228:231],v[88:91]\n"
E_"v33,v33\n"
M_"v[88:91],a[168:171],a[232:235],v[88:91]\n"
"v_fmamk_f32 v34,v170,0xc2b17218,v253\n"
"v_fmamk_f32 v35,v171,0xc2b17218,v253\n"
M_"v[88:91],a[172:175],a[236:239],v[88:91]\n"
E_"v34,v34\n"
M_"v[92:95],a[172:175],a[252:255],v[92:95]\n"
B_"a[172:175],v242 offset:12288\n"
"v_fmamk_f32 v36,v172,0xc2b17218,v253\n"
M_"v[92:95],a[168:171],a[248:251],v[92:95]\n"
B_"a[168:171],v242 offset:3072\n"
"v_fmamk_f32 v37,v173,0xc2b17218,v253\n"
M_"v[92:95],a[164:167],a[244:247],v[92:95]\n"
B_"a[164:167],v249 offset:59392\n"
C_"v88,v88,v89\n"
M_"v[92:95],a[160:163],a[240:243],v[92:95]\n"
B_"a[160:163],v249 offset:50176\n"
C_"v89,v90,v91\n"
M_"a[8:11],v[236:239],v[180:183],a[8:11]\n"
"v_min_u32 v88,-2,v88\n"
"v_min_u32 v89,-2,v89\n"
M_"a[8:11],v[236:239],v[184:187],a[8:11]\n"
"s_waitcnt vmcnt(2)\n"
E_"v35,v35\n"
M_"a[8:11],v[236:239],v[188:191],a[8:11]\n"
C_"v90,v92,v93\n"
C_"v91,v94,v95\n"
M_"a[12:15],v[234:237],v[180:183],a[12:15]\n"
"v_min_u32 v90,-2,v90\n"
"v_min_u32 v91,-2,v91\n"
M_"a[12:15],v[234:237],v[184:187],a[12:15]\n"
E_"v36,v36\n"
M_"a[12:15],v[234:237],v[188:191],a[12:15]\n"
W_"8)\n"
E_"v37,v37\n"
M_"v[96:99],a[128:131],a[192:195],v[96:99]\n"
O_"v[88:91],v255,s[48:51],s87 offen sc1\n"
M_"v[96:99],a[132:135],a[196:199],v[96:99]\n"
A_"s87,s61,4096\n"
"v_fmamk_f32 v38,v174,0xc2b17218,v253\n"
"v_fmamk_f32 v39,v175,0xc2b17218,v253\n"
M_"v[96:99],a[136:139],a[200:203],v[96:99]\n"
"s_sub_u32 s86,s87,9216\n"
E_"v38,v38\n"
M_"v[96:99],a[140:143],a[204:207],v[96:99]\n"
"s_min_u32 s87,s87,s86\n"
E_"v39,v39\n"
M_"v[100:103],a[140:143],a[220:223],v[100:103]\n"
A_"s87,s87,s84\n"
B_"a[140:143],v255 offset:32768\n"
"v_add_f32 v32,0x3c853526,v32\n"
M_"v[100:103],a[136:139],a[216:219],v[100:103]\n"
A_"s87,s87,36864\n"
B_"a[136:139],v255 offset:23552\n"
"v_add_f32 v33,0x3c853526,v33\n"
M_"v[100:103],a[132:135],a[212:215],v[100:103]\n"
B_"a[132:135],v255 offset:14336\n"
"v_add_f32 v34,0x3c853526,v34\n"
M_"v[100:103],a[128:131],a[208:211],v[100:103]\n"
W_"7)\n"
B_"a[128:131],v255 offset:5120\n"
"v_add_f32 v35,0x3c853526,v35\n"
M_"v[96:99],a[144:147],a[224:227],v[96:99]\n"
"v_rcp_f32 v32,v32\n"
M_"v[96:99],a[148:151],a[228:231],v[96:99]\n"
"v_rcp_f32 v33,v33\n"
M_"v[96:99],a[152:155],a[232:235],v[96:99]\n"
"v_rcp_f32 v34,v34\n"
M_"v[96:99],a[156:159],a[236:239],v[96:99]\n"
"v_rcp_f32 v35,v35\n"
M_"v[100:103],a[156:159],a[252:255],v[100:103]\n"
B_"a[156:159],v242 offset:13312\n"
"v_add_f32 v36,0x3c853526,v36\n"
M_"v[100:103],a[152:155],a[248:251],v[100:103]\n"
B_"a[152:155],v242 offset:4096\n"
"v_add_f32 v37,0x3c853526,v37\n"
M_"v[100:103],a[148:151],a[244:247],v[100:103]\n"
B_"a[148:151],v249 offset:60416\n"
C_"v96,v96,v97\n"
M_"v[100:103],a[144:147],a[240:243],v[100:103]\n"
B_"a[144:147],v249 offset:51200\n"
C_"v97,v98,v99\n"
M_"a[16:19],v[236:239],v[192:195],a[16:19]\n"
"v_min_u32 v96,-2,v96\n"
"v_min_u32 v97,-2,v97\n"
M_"a[16:19],v[236:239],v[196:199],a[16:19]\n"
"s_waitcnt vmcnt(2)\n"
"v_rcp_f32 v36,v36\n"
M_"a[16:19],v[236:239],v[200:203],a[16:19]\n"
C_"v98,v100,v101\n"
C_"v99,v102,v103\n"
M_"a[20:23],v[234:237],v[192:195],a[20:23]\n"
"v_min_u32 v98,-2,v98\n"
"v_min_u32 v99,-2,v99\n"
M_"a[20:23],v[234:237],v[196:199],a[20:23]\n"
"v_rcp_f32 v37,v37\n"
M_"a[20:23],v[234:237],v[200:203],a[20:23]\n"
W_"8)\n"
"v_add_f32 v38,0x3c853526,v38\n"
"v_add_f32 v39,0x3c853526,v39\n"
M_"v[104:107],a[160:163],a[192:195],v[104:107]\n"
O_"v[96:99],v255,s[48:51],s87 offen sc1\n"
M_"v[104:107],a[164:167],a[196:199],v[104:107]\n"
A_"s87,s61,5120\n"
"v_rcp_f32 v38,v38\n"
M_"v[104:107],a[168:171],a[200:203],v[104:107]\n"
"s_sub_u32 s86,s87,9216\n"
"v_rcp_f32 v39,v39\n"
M_"v[104:107],a[172:175],a[204:207],v[104:107]\n"
"s_min_u32 s87,s87,s86\n"
U_"v168,v168,v32\n"
U_"v169,v169,v33\n"
M_"v[108:111],a[172:175],a[220:223],v[108:111]\n"
A_"s87,s87,s84\n"
B_"a[172:175],v255 offset:33792\n"
U_"v170,v170,v34\n"
M_"v[108:111],a[168:171],a[216:219],v[108:111]\n"
A_"s87,s87,36864\n"
B_"a[168:171],v255 offset:24576\n"
U_"v171,v171,v35\n"
M_"v[108:111],a[164:167],a[212:215],v[108:111]\n"
B_"a[164:167],v255 offset:15360\n"
U_"v172,v172,v36\n"
M_"v[108:111],a[160:163],a[208:211],v[108:111]\n"
W_"7)\n"
B_"a[160:163],v255 offset:6144\n"
U_"v173,v173,v37\n"
M_"v[104:107],a[128:131],a[224:227],v[104:107]\n"
U_"v174,v174,v38\n"
U_"v175,v175,v39\n"
M_"v[104:107],a[132:135],a[228:231],v[104:107]\n"
R_"v180,a8\n"
R_"v181,a9\n"
M_"v[104:107],a[136:139],a[232:235],v[104:107]\n"
R_"v182,a10\n"
R_"v183,a11\n"
M_"v[104:107],a[140:143],a[236:239],v[104:107]\n"
R_"v184,a12\n"
R_"v185,a13\n"
M_"v[108:111],a[140:143],a[252:255],v[108:111]\n"
B_"a[140:143],v242 offset:14336\n"
R_"v186,a14\n"
M_"v[108:111],a[136:139],a[248:251],v[108:111]\n"
B_"a[136:139],v242 offset:5120\n"
R_"v187,a15\n"
M_"v[108:111],a[132:135],a[244:247],v[108:111]\n"
B_"a[132:135],v249 offset:61440\n"
C_"v104,v104,v105\n"
M_"v[108:111],a[128:131],a[240:243],v[108:111]\n"
W_"7)\n"
B_"a[128:131],v249 offset:52224\n"
C_"v105,v106,v107\n"
M_"v[112:115],a[144:147],a[192:195],v[112:115]\n"
"v_min_u32 v104,-2,v104\n"
"v_min_u32 v105,-2,v105\n"
M_"v[112:115],a[148:151],a[196:199],v[112:115]\n"
"v_fmamk_f32 v40,v180,0xc2b17218,v253\n"
"v_fmamk_f32 v41,v181,0xc2b17218,v253\n"
M_"v[112:115],a[152:155],a[200:203],v[112:115]\n"
C_"v106,v108,v109\n"
C_"v107,v110,v111\n"
M_"v[112:115],a[156:159],a[204:207],v[112:115]\n"
"v_min_u32 v106,-2,v106\n"
"v_min_u32 v107,-2,v107\n"
M_"v[116:119],a[156:159],a[220:223],v[116:119]\n"
B_"a[156:159],v255 offset:34816\n"
"v_fmamk_f32 v42,v182,0xc2b17218,v253\n"
M_"v[116:119],a[152:155],a[216:219],v[116:119]\n"
B_"a[152:155],v255 offset:25600\n"
"v_fmamk_f32 v43,v183,0xc2b17218,v253\n"
M_"v[116:119],a[148:151],a[212:215],v[116:119]\n"
B_"a[148:151],v255 offset:16384\n"
"v_fmamk_f32 v44,v184,0xc2b17218,v253\n"
M_"v[116:119],a[144:147],a[208:211],v[116:119]\n"
W_"7)\n"
B_"a[144:147],v255 offset:7168\n"
"v_fmamk_f32 v45,v185,0xc2b17218,v253\n"
M_"v[112:115],a[160:163],a[224:227],v[112:115]\n"
O_"v[104:107],v255,s[48:51],s87 offen sc1\n"
M_"v[112:115],a[164:167],a[228:231],v[112:115]\n"
A_"s87,s61,6144\n"
E_"v40,v40\n"
M_"v[112:115],a[168:171],a[232:235],v[112:115]\n"
"s_sub_u32 s86,s87,9216\n"
E_"v41,v41\n"
M_"v[112:115],a[172:175],a[236:239],v[112:115]\n"
"s_min_u32 s87,s87,s86\n"
E_"v42,v42\n"
M_"v[116:119],a[172:175],a[252:255],v[116:119]\n"
A_"s87,s87,s84\n"
B_"a[172:175],v242 offset:15360\n"
"v_fmamk_f32 v46,v186,0xc2b17218,v253\n"
M_"v[116:119],a[168:171],a[248:251],v[116:119]\n"
A_"s87,s87,36864\n"
B_"a[168:171],v242 offset:6144\n"
"v_fmamk_f32 v47,v187,0xc2b17218,v253\n"
M_"v[116:119],a[164:167],a[244:247],v[116:119]\n"
B_"a[164:167],v249 offset:62464\n"
C_"v112,v112,v113\n"
M_"v[116:119],a[160:163],a[240:243],v[116:119]\n"
W_"7)\n"
B_"a[160:163],v249 offset:53248\n"
C_"v113,v114,v115\n"
M_"v[120:123],a[128:131],a[192:195],v[120:123]\n"
"v_min_u32 v112,-2,v112\n"
"v_min_u32 v113,-2,v113\n"
M_"v[120:123],a[132:135],a[196:199],v[120:123]\n"
E_"v43,v43\n"
M_"v[120:123],a[136:139],a[200:203],v[120:123]\n"
C_"v114,v116,v117\n"
C_"v115,v118,v119\n"
M_"v[120:123],a[140:143],a[204:207],v[120:123]\n"
"v_min_u32 v114,-2,v114\n"
"v_min_u32 v115,-2,v115\n"
M_"v[124:127],a[140:143],a[220:223],v[124:127]\n"
B_"a[140:143],v255 offset:35840\n"
"v_add_f32 v40,0x3c853526,v40\n"
M_"v[124:127],a[136:139],a[216:219],v[124:127]\n"
B_"a[136:139],v255 offset:26624\n"
"v_add_f32 v41,0x3c853526,v41\n"
M_"v[124:127],a[132:135],a[212:215],v[124:127]\n"
B_"a[132:135],v255 offset:17408\n"
"v_add_f32 v42,0x3c853526,v42\n"
M_"v[124:127],a[128:131],a[208:211],v[124:127]\n"
W_"7)\n"
B_"a[128:131],v255 offset:8192\n"
"v_add_f32 v43,0x3c853526,v43\n"
M_"v[120:123],a[144:147],a[224:227],v[120:123]\n"
O_"v[112:115],v255,s[48:51],s87 offen sc1\n"
M_"v[120:123],a[148:151],a[228:231],v[120:123]\n"
A_"s87,s61,7168\n"
E_"v44,v44\n"
M_"v[120:123],a[152:155],a[232:235],v[120:123]\n"
"s_sub_u32 s86,s87,9216\n"
E_"v45,v45\n"
M_"v[120:123],a[156:159],a[236:239],v[120:123]\n"
"s_min_u32 s87,s87,s86\n"
E_"v46,v46\n"
M_"v[124:127],a[156:159],a[252:255],v[124:127]\n"
A_"s87,s87,s84\n"
B_"a[156:159],v242 offset:7168\n"
"v_add_f32 v44,0x3c853526,v44\n"
M_"v[124:127],a[152:155],a[248:251],v[124:127]\n"
A_"s87,s87,36864\n"
B_"a[152:155],v249 offset:63488\n"
"v_add_f32 v45,0x3c853526,v45\n"
M_"v[124:127],a[148:151],a[244:247],v[124:127]\n"
B_"a[148:151],v249 offset:54272\n"
C_"v120,v120,v121\n"
M_"v[124:127],a[144:147],a[240:243],v[124:127]\n"
W_"7)\n"
B_"a[144:147],v249 offset:45056\n"
C_"v121,v122,v123\n"
M_"v[128:131],a[160:163],a[192:195],v[128:131]\n"
"v_min_u32 v120,-2,v120\n"
"v_min_u32 v121,-2,v121\n"
M_"v[128:131],a[164:167],a[196:199],v[128:131]\n"
E_"v47,v47\n"
M_"v[128:131],a[168:171],a[200:203],v[128:131]\n"
C_"v122,v124,v125\n"
C_"v123,v126,v127\n"
M_"v[128:131],a[172:175],a[204:207],v[128:131]\n"
"v_min_u32 v122,-2,v122\n"
"v_min_u32 v123,-2,v123\n"
M_"v[132:135],a[172:175],a[220:223],v[132:135]\n"
B_"a[172:175],v255 offset:27648\n"
"v_add_f32 v46,0x3c853526,v46\n"
M_"v[132:135],a[168:171],a[216:219],v[132:135]\n"
B_"a[168:171],v255 offset:18432\n"
"v_add_f32 v47,0x3c853526,v47\n"
M_"v[132:135],a[164:167],a[212:215],v[132:135]\n"
B_"a[164:167],v255 offset:9216\n"
R_"v192,a16\n"
M_"v[132:135],a[160:163],a[208:211],v[132:135]\n"
W_"7)\n"
B_"a[160:163],v255 offset:0\n"
R_"v193,a17\n"
M_"v[128:131],a[128:131],a[224:227],v[128:131]\n"
O_"v[120:123],v255,s[48:51],s87 offen sc1\n"
M_"v[128:131],a[132:135],a[228:231],v[128:131]\n"
A_"s87,s61,8192\n"
"v_rcp_f32 v40,v40\n"
M_"v[128:131],a[136:139],a[232:235],v[128:131]\n"
"s_sub_u32 s86,s87,9216\n"
"v_rcp_f32 v41,v41\n"
M_"v[128:131],a[140:143],a[236:239],v[128:131]\n"
"s_min_u32 s87,s87,s86\n"
"v_rcp_f32 v42,v42\n"
M_"v[132:135],a[140:143],a[252:255],v[132:135]\n"
A_"s87,s87,s84\n"
B_"a[140:143],v242 offset:8192\n"
U_"v180,v180,v40\n"
M_"v[132:135],a[136:139],a[248:251],v[132:135]\n"
A_"s87,s87,36864\n"
B_"a[136:139],v249 offset:64512\n"
U_"v181,v181,v41\n"
M_"v[132:135],a[132:135],a[244:247],v[132:135]\n"
B_"a[132:135],v249 offset:55296\n"
C_"v128,v128,v129\n"
M_"v[132:135],a[128:131],a[240:243],v[132:135]\n"
W_"7)\n"
B_"a[128:131],v249 offset:46080\n"
C_"v129,v130,v131\n"
M_"v[64:67],a[144:147],a[192:195],v[64:67]\n"
"v_rcp_f32 v43,v43\n"
M_"v[64:67],a[148:151],a[196:199],v[64:67]\n"
"v_rcp_f32 v44,v44\n"
M_"v[64:67],a[152:155],a[200:203],v[64:67]\n"
"v_rcp_f32 v45,v45\n"
M_"v[64:67],a[156:159],a[204:207],v[64:67]\n"
"v_rcp_f32 v46,v46\n"
M_"v[68:71],a[156:159],a[220:223],v[68:71]\n"
B_"a[156:159],v255 offset:28672\n"
U_"v182,v182,v42\n"
M_"v[68:71],a[152:155],a[216:219],v[68:71]\n"
B_"a[152:155],v255 offset:19456\n"
U_"v183,v183,v43\n"
M_"v[68:71],a[148:151],a[212:215],v[68:71]\n"
B_"a[148:151],v255 offset:10240\n"
U_"v184,v184,v44\n"
M_"v[68:71],a[144:147],a[208:211],v[68:71]\n"
W_"7)\n"
B_"a[144:147],v255 offset:1024\n"
U_"v185,v185,v45\n"
M_"v[64:67],a[160:163],a[224:227],v[64:67]\n"
"v_rcp_f32 v47,v47\n"
M_"v[64:67],a[164:167],a[228:231],v[64:67]\n"
U_"v186,v186,v46\n"
U_"v187,v187,v47\n"
M_"v[64:67],a[168:171],a[232:235],v[64:67]\n"
C_"v130,v132,v133\n"
C_"v131,v134,v135\n"
M_"v[64:67],a[172:175],a[236:239],v[64:67]\n"
"v_min_u32 v128,-2,v128\n"
"v_min_u32 v129,-2,v129\n"
M_"v[68:71],a[172:175],a[252:255],v[68:71]\n"
"v_min_u32 v130,-2,v130\n"
"v_min_u32 v131,-2,v131\n"
M_"v[68:71],a[168:171],a[248:251],v[68:71]\n"
O_"v[128:131],v255,s[48:51],s87 offen sc1\n"
M_"v[68:71],a[164:167],a[244:247],v[68:71]\n"
R_"v194,a18\n"
R_"v195,a19\n"
M_"v[68:71],a[160:163],a[240:243],v[68:71]\n"
W_"4)\n"
R_"v196,a20\n"
R_"v197,a21\n"
M_"v[72:75],a[128:131],a[192:195],v[72:75]\n"
R_"v198,a22\n"
R_"v199,a23\n"
M_"v[72:75],a[132:135],a[196:199],v[72:75]\n"
"v_fmamk_f32 v48,v192,0xc2b17218,v253\n"
"v_fmamk_f32 v49,v193,0xc2b17218,v253\n"
M_"v[72:75],a[136:139],a[200:203],v[72:75]\n"
"v_fmamk_f32 v50,v194,0xc2b17218,v253\n"
"v_fmamk_f32 v51,v195,0xc2b17218,v253\n"
M_"v[72:75],a[140:143],a[204:207],v[72:75]\n"
"v_fmamk_f32 v52,v196,0xc2b17218,v253\n"
"v_fmamk_f32 v53,v197,0xc2b17218,v253\n"
M_"v[76:79],a[140:143],a[220:223],v[76:79]\n"
"v_fmamk_f32 v54,v198,0xc2b17218,v253\n"
"v_fmamk_f32 v55,v199,0xc2b17218,v253\n"
M_"v[76:79],a[136:139],a[216:219],v[76:79]\n"
E_"v48,v48\n"
M_"v[76:79],a[132:135],a[212:215],v[76:79]\n"
E_"v49,v49\n"
M_"v[76:79],a[128:131],a[208:211],v[76:79]\n"
W_"0)\n"
E_"v50,v50\n"
M_"v[72:75],a[144:147],a[224:227],v[72:75]\n"
E_"v51,v51\n"
M_"v[72:75],a[148:151],a[228:231],v[72:75]\n"
E_"v52,v52\n"
M_"v[72:75],a[152:155],a[232:235],v[72:75]\n"
E_"v53,v53\n"
M_"v[72:75],a[156:159],a[236:239],v[72:75]\n"
E_"v54,v54\n"
M_"v[76:79],a[156:159],a[252:255],v[76:79]\n"
E_"v55,v55\n"
M_"v[76:79],a[152:155],a[248:251],v[76:79]\n"
"v_add_f32 v48,0x3c853526,v48\n"
"v_add_f32 v49,0x3c853526,v49\n"
M_"v[76:79],a[148:151],a[244:247],v[76:79]\n"
"v_add_f32 v50,0x3c853526,v50\n"
"v_add_f32 v51,0x3c853526,v51\n"
M_"v[76:79],a[144:147],a[240:243],v[76:79]\n"
"s_nop 0\n"
L_"v[0:3],v255,s[48:51],s90 offen sc1\n"
L_"v[12:15],v255,s[48:51],s90 offen offset:1024 sc1\n"
L_"v[24:27],v255,s[48:51],s93 offen offset:2048 sc1\n"
L_"v[4:7],v255,s[48:51],s91 offen sc1\n"
L_"v[16:19],v255,s[48:51],s91 offen offset:1024 sc1\n"
L_"v[28:31],v255,s[48:51],s94 offen offset:2048 sc1\n"
L_"v[8:11],v255,s[48:51],s92 offen sc1\n"
L_"v[20:23],v255,s[48:51],s92 offen offset:1024 sc1\n"
L_"v[32:35],v255,s[48:51],s95 offen offset:2048 sc1\n"
"s_waitcnt vmcnt(8)\n"
"v_max3_u32 v36,v0,v1,v2\n"
"s_waitcnt vmcnt(5)\n"
"v_max3_u32 v37,v3,v4,v5\n"
"s_waitcnt vmcnt(2)\n"
"v_max3_u32 v38,v6,v7,v8\n"
"v_max3_u32 v39,v9,v10,v11\n"
"v_max3_u32 v40,v12,v13,v14\n"
"v_max3_u32 v41,v15,v16,v17\n"
"s_waitcnt vmcnt(1)\n"
"v_max3_u32 v42,v18,v19,v20\n"
"v_max3_u32 v43,v21,v22,v23\n"
"v_max3_u32 v44,v24,v25,v26\n"
"v_max3_u32 v45,v27,v28,v29\n"
"s_waitcnt vmcnt(0)\n"
"v_max3_u32 v46,v30,v31,v32\n"
"v_max3_u32 v47,v33,v34,v35\n"
"v_max3_u32 v36,v36,v37,v38\n"
"v_max3_u32 v37,v39,v40,v41\n"
"v_max3_u32 v38,v42,v43,v44\n"
"v_max3_u32 v39,v45,v46,v47\n"
"v_max3_u32 v36,v36,v37,v38\n"
"v_max_u32 v36,v36,v39\n"
"v_cmp_eq_u32 vcc,-1,v36\n"
"s_nop 3\n"
"s_cbranch_vccnz SPINS_%=\n"
"BACKS_%=:\n"
"s_or_b32 s88,%[bid],s33\n"
"s_cselect_b32 s88,1073741824,0\n"
"buffer_store_dwordx2 v[244:245],v243,s[56:59],s88 offen sc1\n"
M_"v[64:67],v[236:239],v[0:3],v[64:67]\n"
"v_add_f32 v52,0x3c853526,v52\n"
"v_add_f32 v53,0x3c853526,v53\n"
M_"v[64:67],v[236:239],v[4:7],v[64:67]\n"
"v_add_f32 v54,0x3c853526,v54\n"
"v_add_f32 v55,0x3c853526,v55\n"
M_"v[64:67],v[236:239],v[8:11],v[64:67]\n"
"v_rcp_f32 v48,v48\n"
M_"v[68:71],v[234:237],v[0:3],v[68:71]\n"
"v_rcp_f32 v49,v49\n"
M_"v[68:71],v[234:237],v[4:7],v[68:71]\n"
"v_rcp_f32 v50,v50\n"
M_"v[68:71],v[234:237],v[8:11],v[68:71]\n"
"v_rcp_f32 v51,v51\n"
M_"v[72:75],v[236:239],v[12:15],v[72:75]\n"
"v_rcp_f32 v52,v52\n"
M_"v[72:75],v[236:239],v[16:19],v[72:75]\n"
"v_rcp_f32 v53,v53\n"
M_"v[72:75],v[236:239],v[20:23],v[72:75]\n"
"v_rcp_f32 v54,v54\n"
M_"v[76:79],v[234:237],v[12:15],v[76:79]\n"
"v_rcp_f32 v55,v55\n"
M_"v[76:79],v[234:237],v[16:19],v[76:79]\n"
U_"v192,v192,v48\n"
U_"v193,v193,v49\n"
M_"v[76:79],v[234:237],v[20:23],v[76:79]\n"
U_"v194,v194,v50\n"
U_"v195,v195,v51\n"
M_"v[80:83],v[236:239],v[24:27],v[80:83]\n"
U_"v196,v196,v52\n"
U_"v197,v197,v53\n"
M_"v[80:83],v[236:239],v[28:31],v[80:83]\n"
U_"v198,v198,v54\n"
U_"v199,v199,v55\n"
M_"v[80:83],v[236:239],v[32:35],v[80:83]\n"
"v_fmac_f32 v208,v64,v168\n"
"v_fmac_f32 v209,v65,v169\n"
M_"v[84:87],v[234:237],v[24:27],v[84:87]\n"
"v_fmac_f32 v212,v68,v172\n"
"v_fmac_f32 v213,v69,v173\n"
M_"v[84:87],v[234:237],v[28:31],v[84:87]\n"
"v_fmac_f32 v216,v72,v180\n"
"v_fmac_f32 v217,v73,v181\n"
M_"v[84:87],v[234:237],v[32:35],v[84:87]\n"
"v_fmac_f32 v220,v76,v184\n"
"v_fmac_f32 v221,v77,v185\n"
"v_fmac_f32 v224,v80,v192\n"
"v_fmac_f32 v225,v81,v193\n"
"v_fmac_f32 v210,v66,v170\n"
"v_fmac_f32 v211,v67,v171\n"
C_"v208,v208,v209\n"
"v_fmac_f32 v214,v70,v174\n"
"v_fmac_f32 v228,v84,v196\n"
"v_fmac_f32 v229,v85,v197\n"
"v_fmac_f32 v215,v71,v175\n"
C_"v212,v212,v213\n"
"v_fmac_f32 v218,v74,v182\n"
"v_fmac_f32 v219,v75,v183\n"
C_"v216,v216,v217\n"
"v_fmac_f32 v222,v78,v186\n"
"v_fmac_f32 v223,v79,v187\n"
C_"v220,v220,v221\n"
"v_fmac_f32 v226,v82,v194\n"
"v_fmac_f32 v227,v83,v195\n"
C_"v224,v224,v225\n"
"v_fmac_f32 v230,v86,v198\n"
"v_fmac_f32 v231,v87,v199\n"
C_"v228,v228,v229\n"
C_"v209,v210,v211\n"
C_"v213,v214,v215\n"
C_"v217,v218,v219\n"
C_"v221,v222,v223\n"
C_"v225,v226,v227\n"
C_"v229,v230,v231\n"
"buffer_store_dwordx2 v[208:209],v248,s[44:47],0 offen\n"
"buffer_store_dwordx2 v[212:213],v247,s[44:47],0 offen\n"
"buffer_store_dwordx2 v[216:217],v248,s[44:47],0 offen offset:32\n"
"buffer_store_dwordx2 v[220:221],v247,s[44:47],0 offen offset:32\n"
"buffer_store_dwordx2 v[224:225],v248,s[44:47],s60 offen offset:64\n"
"buffer_store_dwordx2 v[228:229],v247,s[44:47],s60 offen offset:64\n"
"s_nop 0\n"
"s_branch THEEND_%=\n"
"SPINH_%=:\n"
S_"s89,0x40000\n"
"SPINLH_%=:\n"
"s_sleep 2\n"
S_"s88,8192\n"
L_"v[228:231],v224,s[56:59],s88 offen offset:0 sc1\n"
L_"v[232:235],v224,s[56:59],s88 offen offset:16 sc1\n"
"s_waitcnt vmcnt(0)\n"
"v_cmp_eq_u64 vcc,s[66:67],v[228:229]\n"
"v_cmp_eq_u64 s[68:69],s[66:67],v[230:231]\n"
"s_nop 3\n"
"s_and_b64 vcc,vcc,s[68:69]\n"
"v_cmp_eq_u64 s[68:69],s[66:67],v[232:233]\n"
"s_nop 3\n"
"s_and_b64 vcc,vcc,s[68:69]\n"
"v_cmp_eq_u64 s[68:69],s[66:67],v[234:235]\n"
"s_nop 3\n"
"s_and_b64 vcc,vcc,s[68:69]\n"
"s_nop 3\n"
"s_cmp_eq_u64 vcc,-1\n"
"s_cbranch_scc1 BACKH_%=\n"
"s_sub_u32 s89,s89,1\n"
"s_cbranch_scc0 SPINLH_%=\n"
"s_branch BACKH_%=\n"
"SPINO_%=:\n"
S_"s89,0x40000\n"
"SPINLO_%=:\n"
"s_sleep 2\n"
"buffer_load_dwordx2 v[232:233],v251,s[56:59],s85 offen sc1\n"
"s_waitcnt vmcnt(0)\n"
"v_cmp_eq_u64 vcc,s[66:67],v[232:233]\n"
"s_nop 3\n"
"s_cmp_eq_u64 vcc,-1\n"
"s_cbranch_scc1 BACKO_%=\n"
"s_sub_u32 s89,s89,1\n"
"s_cbranch_scc0 SPINLO_%=\n"
"s_branch BACKO_%=\n"
"SPING_%=:\n"
S_"s89,0x40000\n"
"SPINLG_%=:\n"
"s_sleep 2\n"
"buffer_load_dwordx2 v[232:233],v252,s[56:59],s85 offen sc1\n"
"s_waitcnt vmcnt(0)\n"
"v_cmp_eq_u64 vcc,s[66:67],v[232:233]\n"
"s_nop 3\n"
"s_cmp_eq_u64 vcc,-1\n"
"s_cbranch_scc1 BACKG_%=\n"
"s_sub_u32 s89,s89,1\n"
"s_cbranch_scc0 SPINLG_%=\n"
"s_branch BACKG_%=\n"
"SPINS_%=:\n"
S_"s89,0x40000\n"
"SPINLS_%=:\n"
L_"v[0:3],v255,s[48:51],s90 offen sc1\n"
L_"v[12:15],v255,s[48:51],s90 offen offset:1024 sc1\n"
L_"v[24:27],v255,s[48:51],s93 offen offset:2048 sc1\n"
L_"v[4:7],v255,s[48:51],s91 offen sc1\n"
L_"v[16:19],v255,s[48:51],s91 offen offset:1024 sc1\n"
L_"v[28:31],v255,s[48:51],s94 offen offset:2048 sc1\n"
L_"v[8:11],v255,s[48:51],s92 offen sc1\n"
L_"v[20:23],v255,s[48:51],s92 offen offset:1024 sc1\n"
L_"v[32:35],v255,s[48:51],s95 offen offset:2048 sc1\n"
"s_waitcnt vmcnt(0)\n"
"v_max3_u32 v36,v0,v1,v2\n"
"v_max3_u32 v37,v3,v4,v5\n"
"v_max3_u32 v38,v6,v7,v8\n"
"v_max3_u32 v39,v9,v10,v11\n"
"v_max3_u32 v40,v12,v13,v14\n"
"v_max3_u32 v41,v15,v16,v17\n"
"v_max3_u32 v42,v18,v19,v20\n"
"v_max3_u32 v43,v21,v22,v23\n"
"v_max3_u32 v44,v24,v25,v26\n"
"v_max3_u32 v45,v27,v28,v29\n"
"v_max3_u32 v46,v30,v31,v32\n"
"v_max3_u32 v47,v33,v34,v35\n"
"v_max3_u32 v36,v36,v37,v38\n"
"v_max3_u32 v37,v39,v40,v41\n"
"v_max3_u32 v38,v42,v43,v44\n"
"v_max3_u32 v39,v45,v46,v47\n"
"v_max3_u32 v36,v36,v37,v38\n"
"v_max_u32 v36,v36,v39\n"
"v_cmp_eq_u32 vcc,-1,v36\n"
"s_nop 3\n"
"s_cbranch_vccz BACKS_%=\n"
"s_sub_u32 s89,s89,1\n"
"s_cbranch_scc0 SPINLS_%=\n"
"s_branch BACKS_%=\n"
"SPINBB2_%=:\n"
S_"s99,0x100000\n"
"SPINCB2_%=:\n"
"ds_read_b32 v241,v240\n"
W_"0)\n"
"v_cmp_lt_u32 vcc,v241,v226\n"
"s_nop 3\n"
"s_cbranch_vccz BACKBB2_%=\n"
"s_sub_u32 s99,s99,1\n"
"s_cbranch_scc0 SPINCB2_%=\n"
"s_branch BACKBB2_%=\n"
"SPINBB3_%=:\n"
S_"s99,0x100000\n"
"SPINCB3_%=:\n"
"ds_read_b32 v241,v240\n"
W_"0)\n"
"v_cmp_lt_u32 vcc,v241,v226\n"
"s_nop 3\n"
"s_cbranch_vccz BACKBB3_%=\n"
"s_sub_u32 s99,s99,1\n"
"s_cbranch_scc0 SPINCB3_%=\n"
"s_branch BACKBB3_%=\n"
"SPINBC0_%=:\n"
S_"s99,0x100000\n"
"SPINCC0_%=:\n"
"ds_read_b32 v241,v240\n"
W_"0)\n"
"v_cmp_lt_u32 vcc,v241,v226\n"
"s_nop 3\n"
"s_cbranch_vccz BACKBC0_%=\n"
"s_sub_u32 s99,s99,1\n"
"s_cbranch_scc0 SPINCC0_%=\n"
"s_branch BACKBC0_%=\n"
"SPINBC1_%=:\n"
S_"s99,0x100000\n"
"SPINCC1_%=:\n"
"ds_read_b32 v241,v240\n"
W_"0)\n"
"v_cmp_lt_u32 vcc,v241,v226\n"
"s_nop 3\n"
"s_cbranch_vccz BACKBC1_%=\n"
"s_sub_u32 s99,s99,1\n"
"s_cbranch_scc0 SPINCC1_%=\n"
"s_branch BACKBC1_%=\n"
"SPINBC2_%=:\n"
S_"s99,0x100000\n"
"SPINCC2_%=:\n"
"ds_read_b32 v241,v240\n"
W_"0)\n"
"v_cmp_lt_u32 vcc,v241,v226\n"
"s_nop 3\n"
"s_cbranch_vccz BACKBC2_%=\n"
"s_sub_u32 s99,s99,1\n"
"s_cbranch_scc0 SPINCC2_%=\n"
"s_branch BACKBC2_%=\n"
"SPINBC3_%=:\n"
S_"s99,0x100000\n"
"SPINCC3_%=:\n"
"ds_read_b32 v241,v240\n"
W_"0)\n"
"v_cmp_lt_u32 vcc,v241,v226\n"
"s_nop 3\n"
"s_cbranch_vccz BACKBC3_%=\n"
"s_sub_u32 s99,s99,1\n"
"s_cbranch_scc0 SPINCC3_%=\n"
"s_branch BACKBC3_%=\n"
"SPINBD0_%=:\n"
S_"s99,0x100000\n"
"SPINCD0_%=:\n"
"ds_read_b32 v241,v240\n"
W_"0)\n"
"v_cmp_lt_u32 vcc,v241,v226\n"
"s_nop 3\n"
"s_cbranch_vccz BACKBD0_%=\n"
"s_sub_u32 s99,s99,1\n"
"s_cbranch_scc0 SPINCD0_%=\n"
"s_branch BACKBD0_%=\n"
"SPINBD1_%=:\n"
S_"s99,0x100000\n"
"SPINCD1_%=:\n"
"ds_read_b32 v241,v240\n"
W_"0)\n"
"v_cmp_lt_u32 vcc,v241,v226\n"
"s_nop 3\n"
"s_cbranch_vccz BACKBD1_%=\n"
"s_sub_u32 s99,s99,1\n"
"s_cbranch_scc0 SPINCD1_%=\n"
"s_branch BACKBD1_%=\n"
"SPINBD2_%=:\n"
S_"s99,0x100000\n"
"SPINCD2_%=:\n"
"ds_read_b32 v241,v240\n"
W_"0)\n"
"v_cmp_lt_u32 vcc,v241,v226\n"
"s_nop 3\n"
"s_cbranch_vccz BACKBD2_%=\n"
"s_sub_u32 s99,s99,1\n"
"s_cbranch_scc0 SPINCD2_%=\n"
"s_branch BACKBD2_%=\n"
"SPINBD3_%=:\n"
S_"s99,0x100000\n"
"SPINCD3_%=:\n"
"ds_read_b32 v241,v240\n"
W_"0)\n"
"v_cmp_lt_u32 vcc,v241,v226\n"
"s_nop 3\n"
"s_cbranch_vccz BACKBD3_%=\n"
"s_sub_u32 s99,s99,1\n"
"s_cbranch_scc0 SPINCD3_%=\n"
"s_branch BACKBD3_%=\n"
"SPINBI0_%=:\n"
S_"s99,0x100000\n"
"SPINCI0_%=:\n"
"ds_read_b32 v241,v240\n"
W_"0)\n"
"v_cmp_lt_u32 vcc,v241,v226\n"
"s_nop 3\n"
"s_cbranch_vccz BACKBI0_%=\n"
"s_sub_u32 s99,s99,1\n"
"s_cbranch_scc0 SPINCI0_%=\n"
"s_branch BACKBI0_%=\n"
"SPINBI1_%=:\n"
S_"s99,0x100000\n"
"SPINCI1_%=:\n"
"ds_read_b32 v241,v240\n"
W_"0)\n"
"v_cmp_lt_u32 vcc,v241,v226\n"
"s_nop 3\n"
"s_cbranch_vccz BACKBI1_%=\n"
"s_sub_u32 s99,s99,1\n"
"s_cbranch_scc0 SPINCI1_%=\n"
"s_branch BACKBI1_%=\n"
"SPINBI2_%=:\n"
S_"s99,0x100000\n"
"SPINCI2_%=:\n"
"ds_read_b32 v241,v240\n"
W_"0)\n"
"v_cmp_lt_u32 vcc,v241,v226\n"
"s_nop 3\n"
"s_cbranch_vccz BACKBI2_%=\n"
"s_sub_u32 s99,s99,1\n"
"s_cbranch_scc0 SPINCI2_%=\n"
"s_branch BACKBI2_%=\n"
"SPINBI3_%=:\n"
S_"s99,0x100000\n"
"SPINCI3_%=:\n"
"ds_read_b32 v241,v240\n"
W_"0)\n"
"v_cmp_lt_u32 vcc,v241,v226\n"
"s_nop 3\n"
"s_cbranch_vccz BACKBI3_%=\n"
"s_sub_u32 s99,s99,1\n"
"s_cbranch_scc0 SPINCI3_%=\n"
"s_branch BACKBI3_%=\n"
"CODE1_%=:\n"
"FALLBACK_%=:\n"
"s_waitcnt vmcnt(0) lgkmcnt(0)\n"
"v_bfe_u32 v238,v255,4,4\n"
"v_lshrrev_b32 v236,8,v255\n"
"v_lshlrev_b32 v239,1,v238\n"
"v_lshl_add_u32 v239,v236,10,v239\n"
"v_lshlrev_b32 v238,8,v238\n"
"v_lshl_add_u32 v238,v236,4,v238\n"
"v_mov_b32 v64,0\n"
"v_mov_b32 v65,0\n"
"v_mov_b32 v66,0\n"
"v_mov_b32 v67,0\n"
"v_mov_b32 v68,0\n"
"v_mov_b32 v69,0\n"
"v_mov_b32 v70,0\n"
"v_mov_b32 v71,0\n"
"v_mov_b32 v72,0\n"
"v_mov_b32 v73,0\n"
"v_mov_b32 v74,0\n"
"v_mov_b32 v75,0\n"
"v_mov_b32 v76,0\n"
"v_mov_b32 v77,0\n"
"v_mov_b32 v78,0\n"
"v_mov_b32 v79,0\n"
"v_mov_b32 v80,0\n"
"v_mov_b32 v81,0\n"
"v_mov_b32 v82,0\n"
"v_mov_b32 v83,0\n"
"v_mov_b32 v84,0\n"
"v_mov_b32 v85,0\n"
"v_mov_b32 v86,0\n"
"v_mov_b32 v87,0\n"
"v_mov_b32 v88,0\n"
"v_mov_b32 v89,0\n"
"v_mov_b32 v90,0\n"
"v_mov_b32 v91,0\n"
"v_mov_b32 v92,0\n"
"v_mov_b32 v93,0\n"
"v_mov_b32 v94,0\n"
"v_mov_b32 v95,0\n"
"v_mov_b32 v96,0\n"
"v_mov_b32 v97,0\n"
"v_mov_b32 v98,0\n"
"v_mov_b32 v99,0\n"
"v_mov_b32 v100,0\n"
"v_mov_b32 v101,0\n"
"v_mov_b32 v102,0\n"
"v_mov_b32 v103,0\n"
"v_mov_b32 v104,0\n"
"v_mov_b32 v105,0\n"
"v_mov_b32 v106,0\n"
"v_mov_b32 v107,0\n"
"v_mov_b32 v108,0\n"
"v_mov_b32 v109,0\n"
"v_mov_b32 v110,0\n"
"v_mov_b32 v111,0\n"
"v_mov_b32 v112,0\n"
"v_mov_b32 v113,0\n"
"v_mov_b32 v114,0\n"
"v_mov_b32 v115,0\n"
"v_mov_b32 v116,0\n"
"v_mov_b32 v117,0\n"
"v_mov_b32 v118,0\n"
"v_mov_b32 v119,0\n"
"v_mov_b32 v120,0\n"
"v_mov_b32 v121,0\n"
"v_mov_b32 v122,0\n"
"v_mov_b32 v123,0\n"
"v_mov_b32 v124,0\n"
"v_mov_b32 v125,0\n"
"v_mov_b32 v126,0\n"
"v_mov_b32 v127,0\n"
"v_mov_b32 v128,0\n"
"v_mov_b32 v129,0\n"
"v_mov_b32 v130,0\n"
"v_mov_b32 v131,0\n"
"v_mov_b32 v132,0\n"
"v_mov_b32 v133,0\n"
"v_mov_b32 v134,0\n"
"v_mov_b32 v135,0\n"
"v_mov_b32 v136,0\n"
"v_mov_b32 v137,0\n"
"v_mov_b32 v138,0\n"
"v_mov_b32 v139,0\n"
"v_mov_b32 v140,0\n"
"v_mov_b32 v141,0\n"
"v_mov_b32 v142,0\n"
"v_mov_b32 v143,0\n"
"v_mov_b32 v144,0\n"
"v_mov_b32 v145,0\n"
"v_mov_b32 v146,0\n"
"v_mov_b32 v147,0\n"
"v_mov_b32 v148,0\n"
"v_mov_b32 v149,0\n"
"v_mov_b32 v150,0\n"
"v_mov_b32 v151,0\n"
"v_mov_b32 v152,0\n"
"v_mov_b32 v153,0\n"
"v_mov_b32 v154,0\n"
"v_mov_b32 v155,0\n"
"v_mov_b32 v156,0\n"
"v_mov_b32 v157,0\n"
"v_mov_b32 v158,0\n"
"v_mov_b32 v159,0\n"
"v_mov_b32 v160,0\n"
"v_mov_b32 v161,0\n"
"v_mov_b32 v162,0\n"
"v_mov_b32 v163,0\n"
"v_mov_b32 v164,0\n"
"v_mov_b32 v165,0\n"
"v_mov_b32 v166,0\n"
"v_mov_b32 v167,0\n"
"v_mov_b32 v168,0\n"
"v_mov_b32 v169,0\n"
"v_mov_b32 v170,0\n"
"v_mov_b32 v171,0\n"
"v_mov_b32 v172,0\n"
"v_mov_b32 v173,0\n"
"v_mov_b32 v174,0\n"
"v_mov_b32 v175,0\n"
"v_mov_b32 v176,0\n"
"v_mov_b32 v177,0\n"
"v_mov_b32 v178,0\n"
"v_mov_b32 v179,0\n"
"v_mov_b32 v180,0\n"
"v_mov_b32 v181,0\n"
"v_mov_b32 v182,0\n"
"v_mov_b32 v183,0\n"
"v_mov_b32 v184,0\n"
"v_mov_b32 v185,0\n"
"v_mov_b32 v186,0\n"
"v_mov_b32 v187,0\n"
"v_mov_b32 v188,0\n"
"v_mov_b32 v189,0\n"
"v_mov_b32 v190,0\n"
"v_mov_b32 v191,0\n"
"v_mov_b32 v224,0xff800000\n"
"v_mov_b32 v228,0\n"
"v_mov_b32 v225,0xff800000\n"
"v_mov_b32 v229,0\n"
"v_mov_b32 v226,0xff800000\n"
"v_mov_b32 v230,0\n"
"v_mov_b32 v227,0xff800000\n"
"v_mov_b32 v231,0\n"
S_"s84,0\n"
"FLOOP_%=:\n"
A_"s62,s84,4096\n"
L_"a[96:99],v238,s[40:43],s84 offen\n"
L_"a[100:103],v238,s[40:43],s84 offen offset:64\n"
L_"a[104:107],v238,s[40:43],s84 offen offset:128\n"
L_"a[108:111],v238,s[40:43],s84 offen offset:192\n"
L_"a[112:115],v238,s[40:43],s62 offen\n"
L_"a[116:119],v238,s[40:43],s62 offen offset:64\n"
L_"a[120:123],v238,s[40:43],s62 offen offset:128\n"
L_"a[124:127],v238,s[40:43],s62 offen offset:192\n"
"s_waitcnt vmcnt(7)\n"
M_"v[192:195],a[96:99],v[0:3],0\n"
"s_waitcnt vmcnt(6)\n"
M_"v[192:195],a[100:103],v[16:19],v[192:195]\n"
"s_waitcnt vmcnt(5)\n"
M_"v[192:195],a[104:107],v[32:35],v[192:195]\n"
"s_waitcnt vmcnt(4)\n"
M_"v[192:195],a[108:111],v[48:51],v[192:195]\n"
M_"v[196:199],a[96:99],v[4:7],0\n"
M_"v[196:199],a[100:103],v[20:23],v[196:199]\n"
M_"v[196:199],a[104:107],v[36:39],v[196:199]\n"
M_"v[196:199],a[108:111],v[52:55],v[196:199]\n"
M_"v[200:203],a[96:99],v[8:11],0\n"
M_"v[200:203],a[100:103],v[24:27],v[200:203]\n"
M_"v[200:203],a[104:107],v[40:43],v[200:203]\n"
M_"v[200:203],a[108:111],v[56:59],v[200:203]\n"
M_"v[204:207],a[96:99],v[12:15],0\n"
M_"v[204:207],a[100:103],v[28:31],v[204:207]\n"
M_"v[204:207],a[104:107],v[44:47],v[204:207]\n"
M_"v[204:207],a[108:111],v[60:63],v[204:207]\n"
"s_waitcnt vmcnt(3)\n"
M_"v[208:211],a[112:115],v[0:3],0\n"
"s_waitcnt vmcnt(2)\n"
M_"v[208:211],a[116:119],v[16:19],v[208:211]\n"
"s_waitcnt vmcnt(1)\n"
M_"v[208:211],a[120:123],v[32:35],v[208:211]\n"
"s_waitcnt vmcnt(0)\n"
M_"v[208:211],a[124:127],v[48:51],v[208:211]\n"
M_"v[212:215],a[112:115],v[4:7],0\n"
M_"v[212:215],a[116:119],v[20:23],v[212:215]\n"
M_"v[212:215],a[120:123],v[36:39],v[212:215]\n"
M_"v[212:215],a[124:127],v[52:55],v[212:215]\n"
M_"v[216:219],a[112:115],v[8:11],0\n"
M_"v[216:219],a[116:119],v[24:27],v[216:219]\n"
M_"v[216:219],a[120:123],v[40:43],v[216:219]\n"
M_"v[216:219],a[124:127],v[56:59],v[216:219]\n"
M_"v[220:223],a[112:115],v[12:15],0\n"
M_"v[220:223],a[116:119],v[28:31],v[220:223]\n"
M_"v[220:223],a[120:123],v[44:47],v[220:223]\n"
M_"v[220:223],a[124:127],v[60:63],v[220:223]\n"
"v_max3_f32 v236,v192,v193,v194\n"
"v_max3_f32 v236,v236,v195,v208\n"
"v_max3_f32 v236,v236,v209,v210\n"
"v_max3_f32 v236,v236,v211,v224\n"
"v_mov_b32 v237,v236\n"
"s_nop 1\n"
"v_permlane16_swap_b32 v236,v237\n"
"v_max_f32 v236,v236,v237\n"
"v_mov_b32 v237,v236\n"
"s_nop 1\n"
"v_permlane32_swap_b32 v236,v237\n"
"v_max_f32 v236,v236,v237\n"
"v_sub_f32 v232,v224,v236\n"
E_"v232,v232\n"
"v_mov_b32 v224,v236\n"
U_"v228,v228,v232\n"
"v_sub_f32 v192,v192,v236\n"
E_"v192,v192\n"
"s_nop 0\n"
"v_add_f32 v228,v228,v192\n"
"v_sub_f32 v193,v193,v236\n"
E_"v193,v193\n"
"s_nop 0\n"
"v_add_f32 v228,v228,v193\n"
"v_sub_f32 v194,v194,v236\n"
E_"v194,v194\n"
"s_nop 0\n"
"v_add_f32 v228,v228,v194\n"
"v_sub_f32 v195,v195,v236\n"
E_"v195,v195\n"
"s_nop 0\n"
"v_add_f32 v228,v228,v195\n"
"v_sub_f32 v208,v208,v236\n"
E_"v208,v208\n"
"s_nop 0\n"
"v_add_f32 v228,v228,v208\n"
"v_sub_f32 v209,v209,v236\n"
E_"v209,v209\n"
"s_nop 0\n"
"v_add_f32 v228,v228,v209\n"
"v_sub_f32 v210,v210,v236\n"
E_"v210,v210\n"
"s_nop 0\n"
"v_add_f32 v228,v228,v210\n"
"v_sub_f32 v211,v211,v236\n"
E_"v211,v211\n"
"s_nop 0\n"
"v_add_f32 v228,v228,v211\n"
C_"v192,v192,v193\n"
C_"v193,v194,v195\n"
C_"v194,v208,v209\n"
C_"v195,v210,v211\n"
U_"v64,v64,v232\n"
U_"v65,v65,v232\n"
U_"v66,v66,v232\n"
U_"v67,v67,v232\n"
U_"v80,v80,v232\n"
U_"v81,v81,v232\n"
U_"v82,v82,v232\n"
U_"v83,v83,v232\n"
U_"v96,v96,v232\n"
U_"v97,v97,v232\n"
U_"v98,v98,v232\n"
U_"v99,v99,v232\n"
U_"v112,v112,v232\n"
U_"v113,v113,v232\n"
U_"v114,v114,v232\n"
U_"v115,v115,v232\n"
U_"v128,v128,v232\n"
U_"v129,v129,v232\n"
U_"v130,v130,v232\n"
U_"v131,v131,v232\n"
U_"v144,v144,v232\n"
U_"v145,v145,v232\n"
U_"v146,v146,v232\n"
U_"v147,v147,v232\n"
U_"v160,v160,v232\n"
U_"v161,v161,v232\n"
U_"v162,v162,v232\n"
U_"v163,v163,v232\n"
U_"v176,v176,v232\n"
U_"v177,v177,v232\n"
U_"v178,v178,v232\n"
U_"v179,v179,v232\n"
"v_max3_f32 v236,v196,v197,v198\n"
"v_max3_f32 v236,v236,v199,v212\n"
"v_max3_f32 v236,v236,v213,v214\n"
"v_max3_f32 v236,v236,v215,v225\n"
"v_mov_b32 v237,v236\n"
"s_nop 1\n"
"v_permlane16_swap_b32 v236,v237\n"
"v_max_f32 v236,v236,v237\n"
"v_mov_b32 v237,v236\n"
"s_nop 1\n"
"v_permlane32_swap_b32 v236,v237\n"
"v_max_f32 v236,v236,v237\n"
"v_sub_f32 v233,v225,v236\n"
E_"v233,v233\n"
"v_mov_b32 v225,v236\n"
U_"v229,v229,v233\n"
"v_sub_f32 v196,v196,v236\n"
E_"v196,v196\n"
"s_nop 0\n"
"v_add_f32 v229,v229,v196\n"
"v_sub_f32 v197,v197,v236\n"
E_"v197,v197\n"
"s_nop 0\n"
"v_add_f32 v229,v229,v197\n"
"v_sub_f32 v198,v198,v236\n"
E_"v198,v198\n"
"s_nop 0\n"
"v_add_f32 v229,v229,v198\n"
"v_sub_f32 v199,v199,v236\n"
E_"v199,v199\n"
"s_nop 0\n"
"v_add_f32 v229,v229,v199\n"
"v_sub_f32 v212,v212,v236\n"
E_"v212,v212\n"
"s_nop 0\n"
"v_add_f32 v229,v229,v212\n"
"v_sub_f32 v213,v213,v236\n"
E_"v213,v213\n"
"s_nop 0\n"
"v_add_f32 v229,v229,v213\n"
"v_sub_f32 v214,v214,v236\n"
E_"v214,v214\n"
"s_nop 0\n"
"v_add_f32 v229,v229,v214\n"
"v_sub_f32 v215,v215,v236\n"
E_"v215,v215\n"
"s_nop 0\n"
"v_add_f32 v229,v229,v215\n"
C_"v196,v196,v197\n"
C_"v197,v198,v199\n"
C_"v198,v212,v213\n"
C_"v199,v214,v215\n"
U_"v68,v68,v233\n"
U_"v69,v69,v233\n"
U_"v70,v70,v233\n"
U_"v71,v71,v233\n"
U_"v84,v84,v233\n"
U_"v85,v85,v233\n"
U_"v86,v86,v233\n"
U_"v87,v87,v233\n"
U_"v100,v100,v233\n"
U_"v101,v101,v233\n"
U_"v102,v102,v233\n"
U_"v103,v103,v233\n"
U_"v116,v116,v233\n"
U_"v117,v117,v233\n"
U_"v118,v118,v233\n"
U_"v119,v119,v233\n"
U_"v132,v132,v233\n"
U_"v133,v133,v233\n"
U_"v134,v134,v233\n"
U_"v135,v135,v233\n"
U_"v148,v148,v233\n"
U_"v149,v149,v233\n"
U_"v150,v150,v233\n"
U_"v151,v151,v233\n"
U_"v164,v164,v233\n"
U_"v165,v165,v233\n"
U_"v166,v166,v233\n"
U_"v167,v167,v233\n"
U_"v180,v180,v233\n"
U_"v181,v181,v233\n"
U_"v182,v182,v233\n"
U_"v183,v183,v233\n"
"v_max3_f32 v236,v200,v201,v202\n"
"v_max3_f32 v236,v236,v203,v216\n"
"v_max3_f32 v236,v236,v217,v218\n"
"v_max3_f32 v236,v236,v219,v226\n"
"v_mov_b32 v237,v236\n"
"s_nop 1\n"
"v_permlane16_swap_b32 v236,v237\n"
"v_max_f32 v236,v236,v237\n"
"v_mov_b32 v237,v236\n"
"s_nop 1\n"
"v_permlane32_swap_b32 v236,v237\n"
"v_max_f32 v236,v236,v237\n"
"v_sub_f32 v234,v226,v236\n"
E_"v234,v234\n"
"v_mov_b32 v226,v236\n"
U_"v230,v230,v234\n"
"v_sub_f32 v200,v200,v236\n"
E_"v200,v200\n"
"s_nop 0\n"
"v_add_f32 v230,v230,v200\n"
"v_sub_f32 v201,v201,v236\n"
E_"v201,v201\n"
"s_nop 0\n"
"v_add_f32 v230,v230,v201\n"
"v_sub_f32 v202,v202,v236\n"
E_"v202,v202\n"
"s_nop 0\n"
"v_add_f32 v230,v230,v202\n"
"v_sub_f32 v203,v203,v236\n"
E_"v203,v203\n"
"s_nop 0\n"
"v_add_f32 v230,v230,v203\n"
"v_sub_f32 v216,v216,v236\n"
E_"v216,v216\n"
"s_nop 0\n"
"v_add_f32 v230,v230,v216\n"
"v_sub_f32 v217,v217,v236\n"
E_"v217,v217\n"
"s_nop 0\n"
"v_add_f32 v230,v230,v217\n"
"v_sub_f32 v218,v218,v236\n"
E_"v218,v218\n"
"s_nop 0\n"
"v_add_f32 v230,v230,v218\n"
"v_sub_f32 v219,v219,v236\n"
E_"v219,v219\n"
"s_nop 0\n"
"v_add_f32 v230,v230,v219\n"
C_"v200,v200,v201\n"
C_"v201,v202,v203\n"
C_"v202,v216,v217\n"
C_"v203,v218,v219\n"
U_"v72,v72,v234\n"
U_"v73,v73,v234\n"
U_"v74,v74,v234\n"
U_"v75,v75,v234\n"
U_"v88,v88,v234\n"
U_"v89,v89,v234\n"
U_"v90,v90,v234\n"
U_"v91,v91,v234\n"
U_"v104,v104,v234\n"
U_"v105,v105,v234\n"
U_"v106,v106,v234\n"
U_"v107,v107,v234\n"
U_"v120,v120,v234\n"
U_"v121,v121,v234\n"
U_"v122,v122,v234\n"
U_"v123,v123,v234\n"
U_"v136,v136,v234\n"
U_"v137,v137,v234\n"
U_"v138,v138,v234\n"
U_"v139,v139,v234\n"
U_"v152,v152,v234\n"
U_"v153,v153,v234\n"
U_"v154,v154,v234\n"
U_"v155,v155,v234\n"
U_"v168,v168,v234\n"
U_"v169,v169,v234\n"
U_"v170,v170,v234\n"
U_"v171,v171,v234\n"
U_"v184,v184,v234\n"
U_"v185,v185,v234\n"
U_"v186,v186,v234\n"
U_"v187,v187,v234\n"
"v_max3_f32 v236,v204,v205,v206\n"
"v_max3_f32 v236,v236,v207,v220\n"
"v_max3_f32 v236,v236,v221,v222\n"
"v_max3_f32 v236,v236,v223,v227\n"
"v_mov_b32 v237,v236\n"
"s_nop 1\n"
"v_permlane16_swap_b32 v236,v237\n"
"v_max_f32 v236,v236,v237\n"
"v_mov_b32 v237,v236\n"
"s_nop 1\n"
"v_permlane32_swap_b32 v236,v237\n"
"v_max_f32 v236,v236,v237\n"
"v_sub_f32 v235,v227,v236\n"
E_"v235,v235\n"
"v_mov_b32 v227,v236\n"
U_"v231,v231,v235\n"
"v_sub_f32 v204,v204,v236\n"
E_"v204,v204\n"
"s_nop 0\n"
"v_add_f32 v231,v231,v204\n"
"v_sub_f32 v205,v205,v236\n"
E_"v205,v205\n"
"s_nop 0\n"
"v_add_f32 v231,v231,v205\n"
"v_sub_f32 v206,v206,v236\n"
E_"v206,v206\n"
"s_nop 0\n"
"v_add_f32 v231,v231,v206\n"
"v_sub_f32 v207,v207,v236\n"
E_"v207,v207\n"
"s_nop 0\n"
"v_add_f32 v231,v231,v207\n"
"v_sub_f32 v220,v220,v236\n"
E_"v220,v220\n"
"s_nop 0\n"
"v_add_f32 v231,v231,v220\n"
"v_sub_f32 v221,v221,v236\n"
E_"v221,v221\n"
"s_nop 0\n"
"v_add_f32 v231,v231,v221\n"
"v_sub_f32 v222,v222,v236\n"
E_"v222,v222\n"
"s_nop 0\n"
"v_add_f32 v231,v231,v222\n"
"v_sub_f32 v223,v223,v236\n"
E_"v223,v223\n"
"s_nop 0\n"
"v_add_f32 v231,v231,v223\n"
C_"v204,v204,v205\n"
C_"v205,v206,v207\n"
C_"v206,v220,v221\n"
C_"v207,v222,v223\n"
U_"v76,v76,v235\n"
U_"v77,v77,v235\n"
U_"v78,v78,v235\n"
U_"v79,v79,v235\n"
U_"v92,v92,v235\n"
U_"v93,v93,v235\n"
U_"v94,v94,v235\n"
U_"v95,v95,v235\n"
U_"v108,v108,v235\n"
U_"v109,v109,v235\n"
U_"v110,v110,v235\n"
U_"v111,v111,v235\n"
U_"v124,v124,v235\n"
U_"v125,v125,v235\n"
U_"v126,v126,v235\n"
U_"v127,v127,v235\n"
U_"v140,v140,v235\n"
U_"v141,v141,v235\n"
U_"v142,v142,v235\n"
U_"v143,v143,v235\n"
U_"v156,v156,v235\n"
U_"v157,v157,v235\n"
U_"v158,v158,v235\n"
U_"v159,v159,v235\n"
U_"v172,v172,v235\n"
U_"v173,v173,v235\n"
U_"v174,v174,v235\n"
U_"v175,v175,v235\n"
U_"v188,v188,v235\n"
U_"v189,v189,v235\n"
U_"v190,v190,v235\n"
U_"v191,v191,v235\n"
"buffer_load_ushort v208,v239,s[44:47],s84 offen\n"
"buffer_load_ushort v209,v239,s[44:47],s84 offen offset:256\n"
"buffer_load_ushort v210,v239,s[44:47],s84 offen offset:512\n"
"buffer_load_ushort v211,v239,s[44:47],s84 offen offset:768\n"
"buffer_load_ushort v212,v239,s[44:47],s62 offen\n"
"buffer_load_ushort v213,v239,s[44:47],s62 offen offset:256\n"
"buffer_load_ushort v214,v239,s[44:47],s62 offen offset:512\n"
"buffer_load_ushort v215,v239,s[44:47],s62 offen offset:768\n"
"s_waitcnt vmcnt(6)\n"
"v_lshl_or_b32 v216,v209,16,v208\n"
"s_waitcnt vmcnt(4)\n"
"v_lshl_or_b32 v217,v211,16,v210\n"
"s_waitcnt vmcnt(2)\n"
"v_lshl_or_b32 v218,v213,16,v212\n"
"s_waitcnt vmcnt(0)\n"
"v_lshl_or_b32 v219,v215,16,v214\n"
"s_nop 1\n"
M_"v[64:67],v[216:219],v[192:195],v[64:67]\n"
M_"v[68:71],v[216:219],v[196:199],v[68:71]\n"
M_"v[72:75],v[216:219],v[200:203],v[72:75]\n"
M_"v[76:79],v[216:219],v[204:207],v[76:79]\n"
"buffer_load_ushort v208,v239,s[44:47],s84 offen offset:32\n"
"buffer_load_ushort v209,v239,s[44:47],s84 offen offset:288\n"
"buffer_load_ushort v210,v239,s[44:47],s84 offen offset:544\n"
"buffer_load_ushort v211,v239,s[44:47],s84 offen offset:800\n"
"buffer_load_ushort v212,v239,s[44:47],s62 offen offset:32\n"
"buffer_load_ushort v213,v239,s[44:47],s62 offen offset:288\n"
"buffer_load_ushort v214,v239,s[44:47],s62 offen offset:544\n"
"buffer_load_ushort v215,v239,s[44:47],s62 offen offset:800\n"
"s_waitcnt vmcnt(6)\n"
"v_lshl_or_b32 v220,v209,16,v208\n"
"s_waitcnt vmcnt(4)\n"
"v_lshl_or_b32 v221,v211,16,v210\n"
"s_waitcnt vmcnt(2)\n"
"v_lshl_or_b32 v222,v213,16,v212\n"
"s_waitcnt vmcnt(0)\n"
"v_lshl_or_b32 v223,v215,16,v214\n"
"s_nop 1\n"
M_"v[80:83],v[220:223],v[192:195],v[80:83]\n"
M_"v[84:87],v[220:223],v[196:199],v[84:87]\n"
M_"v[88:91],v[220:223],v[200:203],v[88:91]\n"
M_"v[92:95],v[220:223],v[204:207],v[92:95]\n"
"buffer_load_ushort v208,v239,s[44:47],s84 offen offset:64\n"
"buffer_load_ushort v209,v239,s[44:47],s84 offen offset:320\n"
"buffer_load_ushort v210,v239,s[44:47],s84 offen offset:576\n"
"buffer_load_ushort v211,v239,s[44:47],s84 offen offset:832\n"
"buffer_load_ushort v212,v239,s[44:47],s62 offen offset:64\n"
"buffer_load_ushort v213,v239,s[44:47],s62 offen offset:320\n"
"buffer_load_ushort v214,v239,s[44:47],s62 offen offset:576\n"
"buffer_load_ushort v215,v239,s[44:47],s62 offen offset:832\n"
"s_waitcnt vmcnt(6)\n"
"v_lshl_or_b32 v216,v209,16,v208\n"
"s_waitcnt vmcnt(4)\n"
"v_lshl_or_b32 v217,v211,16,v210\n"
"s_waitcnt vmcnt(2)\n"
"v_lshl_or_b32 v218,v213,16,v212\n"
"s_waitcnt vmcnt(0)\n"
"v_lshl_or_b32 v219,v215,16,v214\n"
"s_nop 1\n"
M_"v[96:99],v[216:219],v[192:195],v[96:99]\n"
M_"v[100:103],v[216:219],v[196:199],v[100:103]\n"
M_"v[104:107],v[216:219],v[200:203],v[104:107]\n"
M_"v[108:111],v[216:219],v[204:207],v[108:111]\n"
"buffer_load_ushort v208,v239,s[44:47],s84 offen offset:96\n"
"buffer_load_ushort v209,v239,s[44:47],s84 offen offset:352\n"
"buffer_load_ushort v210,v239,s[44:47],s84 offen offset:608\n"
"buffer_load_ushort v211,v239,s[44:47],s84 offen offset:864\n"
"buffer_load_ushort v212,v239,s[44:47],s62 offen offset:96\n"
"buffer_load_ushort v213,v239,s[44:47],s62 offen offset:352\n"
"buffer_load_ushort v214,v239,s[44:47],s62 offen offset:608\n"
"buffer_load_ushort v215,v239,s[44:47],s62 offen offset:864\n"
"s_waitcnt vmcnt(6)\n"
"v_lshl_or_b32 v220,v209,16,v208\n"
"s_waitcnt vmcnt(4)\n"
"v_lshl_or_b32 v221,v211,16,v210\n"
"s_waitcnt vmcnt(2)\n"
"v_lshl_or_b32 v222,v213,16,v212\n"
"s_waitcnt vmcnt(0)\n"
"v_lshl_or_b32 v223,v215,16,v214\n"
"s_nop 1\n"
M_"v[112:115],v[220:223],v[192:195],v[112:115]\n"
M_"v[116:119],v[220:223],v[196:199],v[116:119]\n"
M_"v[120:123],v[220:223],v[200:203],v[120:123]\n"
M_"v[124:127],v[220:223],v[204:207],v[124:127]\n"
"buffer_load_ushort v208,v239,s[44:47],s84 offen offset:128\n"
"buffer_load_ushort v209,v239,s[44:47],s84 offen offset:384\n"
"buffer_load_ushort v210,v239,s[44:47],s84 offen offset:640\n"
"buffer_load_ushort v211,v239,s[44:47],s84 offen offset:896\n"
"buffer_load_ushort v212,v239,s[44:47],s62 offen offset:128\n"
"buffer_load_ushort v213,v239,s[44:47],s62 offen offset:384\n"
"buffer_load_ushort v214,v239,s[44:47],s62 offen offset:640\n"
"buffer_load_ushort v215,v239,s[44:47],s62 offen offset:896\n"
"s_waitcnt vmcnt(6)\n"
"v_lshl_or_b32 v216,v209,16,v208\n"
"s_waitcnt vmcnt(4)\n"
"v_lshl_or_b32 v217,v211,16,v210\n"
"s_waitcnt vmcnt(2)\n"
"v_lshl_or_b32 v218,v213,16,v212\n"
"s_waitcnt vmcnt(0)\n"
"v_lshl_or_b32 v219,v215,16,v214\n"
"s_nop 1\n"
M_"v[128:131],v[216:219],v[192:195],v[128:131]\n"
M_"v[132:135],v[216:219],v[196:199],v[132:135]\n"
M_"v[136:139],v[216:219],v[200:203],v[136:139]\n"
M_"v[140:143],v[216:219],v[204:207],v[140:143]\n"
"buffer_load_ushort v208,v239,s[44:47],s84 offen offset:160\n"
"buffer_load_ushort v209,v239,s[44:47],s84 offen offset:416\n"
"buffer_load_ushort v210,v239,s[44:47],s84 offen offset:672\n"
"buffer_load_ushort v211,v239,s[44:47],s84 offen offset:928\n"
"buffer_load_ushort v212,v239,s[44:47],s62 offen offset:160\n"
"buffer_load_ushort v213,v239,s[44:47],s62 offen offset:416\n"
"buffer_load_ushort v214,v239,s[44:47],s62 offen offset:672\n"
"buffer_load_ushort v215,v239,s[44:47],s62 offen offset:928\n"
"s_waitcnt vmcnt(6)\n"
"v_lshl_or_b32 v220,v209,16,v208\n"
"s_waitcnt vmcnt(4)\n"
"v_lshl_or_b32 v221,v211,16,v210\n"
"s_waitcnt vmcnt(2)\n"
"v_lshl_or_b32 v222,v213,16,v212\n"
"s_waitcnt vmcnt(0)\n"
"v_lshl_or_b32 v223,v215,16,v214\n"
"s_nop 1\n"
M_"v[144:147],v[220:223],v[192:195],v[144:147]\n"
M_"v[148:151],v[220:223],v[196:199],v[148:151]\n"
M_"v[152:155],v[220:223],v[200:203],v[152:155]\n"
M_"v[156:159],v[220:223],v[204:207],v[156:159]\n"
"buffer_load_ushort v208,v239,s[44:47],s84 offen offset:192\n"
"buffer_load_ushort v209,v239,s[44:47],s84 offen offset:448\n"
"buffer_load_ushort v210,v239,s[44:47],s84 offen offset:704\n"
"buffer_load_ushort v211,v239,s[44:47],s84 offen offset:960\n"
"buffer_load_ushort v212,v239,s[44:47],s62 offen offset:192\n"
"buffer_load_ushort v213,v239,s[44:47],s62 offen offset:448\n"
"buffer_load_ushort v214,v239,s[44:47],s62 offen offset:704\n"
"buffer_load_ushort v215,v239,s[44:47],s62 offen offset:960\n"
"s_waitcnt vmcnt(6)\n"
"v_lshl_or_b32 v216,v209,16,v208\n"
"s_waitcnt vmcnt(4)\n"
"v_lshl_or_b32 v217,v211,16,v210\n"
"s_waitcnt vmcnt(2)\n"
"v_lshl_or_b32 v218,v213,16,v212\n"
"s_waitcnt vmcnt(0)\n"
"v_lshl_or_b32 v219,v215,16,v214\n"
"s_nop 1\n"
M_"v[160:163],v[216:219],v[192:195],v[160:163]\n"
M_"v[164:167],v[216:219],v[196:199],v[164:167]\n"
M_"v[168:171],v[216:219],v[200:203],v[168:171]\n"
M_"v[172:175],v[216:219],v[204:207],v[172:175]\n"
"buffer_load_ushort v208,v239,s[44:47],s84 offen offset:224\n"
"buffer_load_ushort v209,v239,s[44:47],s84 offen offset:480\n"
"buffer_load_ushort v210,v239,s[44:47],s84 offen offset:736\n"
"buffer_load_ushort v211,v239,s[44:47],s84 offen offset:992\n"
"buffer_load_ushort v212,v239,s[44:47],s62 offen offset:224\n"
"buffer_load_ushort v213,v239,s[44:47],s62 offen offset:480\n"
"buffer_load_ushort v214,v239,s[44:47],s62 offen offset:736\n"
"buffer_load_ushort v215,v239,s[44:47],s62 offen offset:992\n"
"s_waitcnt vmcnt(6)\n"
"v_lshl_or_b32 v220,v209,16,v208\n"
"s_waitcnt vmcnt(4)\n"
"v_lshl_or_b32 v221,v211,16,v210\n"
"s_waitcnt vmcnt(2)\n"
"v_lshl_or_b32 v222,v213,16,v212\n"
"s_waitcnt vmcnt(0)\n"
"v_lshl_or_b32 v223,v215,16,v214\n"
"s_nop 1\n"
M_"v[176:179],v[220:223],v[192:195],v[176:179]\n"
M_"v[180:183],v[220:223],v[196:199],v[180:183]\n"
M_"v[184:187],v[220:223],v[200:203],v[184:187]\n"
M_"v[188:191],v[220:223],v[204:207],v[188:191]\n"
A_"s84,s84,8192\n"
"s_cmp_lt_u32 s84,0x80000\n"
"s_nop 7\n"
"s_cbranch_scc1 FLOOP_%=\n"
"v_mov_b32 v236,v228\n"
"s_nop 1\n"
"v_permlane16_swap_b32 v228,v236\n"
"v_add_f32 v228,v228,v236\n"
"v_mov_b32 v236,v228\n"
"s_nop 1\n"
"v_permlane32_swap_b32 v228,v236\n"
"v_add_f32 v228,v228,v236\n"
"v_rcp_f32 v228,v228\n"
"v_mov_b32 v236,v229\n"
"s_nop 1\n"
"v_permlane16_swap_b32 v229,v236\n"
"v_add_f32 v229,v229,v236\n"
"v_mov_b32 v236,v229\n"
"s_nop 1\n"
"v_permlane32_swap_b32 v229,v236\n"
"v_add_f32 v229,v229,v236\n"
"v_rcp_f32 v229,v229\n"
"v_mov_b32 v236,v230\n"
"s_nop 1\n"
"v_permlane16_swap_b32 v230,v236\n"
"v_add_f32 v230,v230,v236\n"
"v_mov_b32 v236,v230\n"
"s_nop 1\n"
"v_permlane32_swap_b32 v230,v236\n"
"v_add_f32 v230,v230,v236\n"
"v_rcp_f32 v230,v230\n"
"v_mov_b32 v236,v231\n"
"s_nop 1\n"
"v_permlane16_swap_b32 v231,v236\n"
"v_add_f32 v231,v231,v236\n"
"v_mov_b32 v236,v231\n"
"s_nop 1\n"
"v_permlane32_swap_b32 v231,v236\n"
"v_add_f32 v231,v231,v236\n"
"v_rcp_f32 v231,v231\n"
U_"v64,v64,v228\n"
U_"v65,v65,v228\n"
U_"v66,v66,v228\n"
U_"v67,v67,v228\n"
C_"v64,v64,v65\n"
C_"v65,v66,v67\n"
U_"v80,v80,v228\n"
U_"v81,v81,v228\n"
U_"v82,v82,v228\n"
U_"v83,v83,v228\n"
C_"v66,v80,v81\n"
C_"v67,v82,v83\n"
O_"v[64:67],v255,s[52:55],0 offen sc1\n"
U_"v68,v68,v229\n"
U_"v69,v69,v229\n"
U_"v70,v70,v229\n"
U_"v71,v71,v229\n"
C_"v68,v68,v69\n"
C_"v69,v70,v71\n"
U_"v84,v84,v229\n"
U_"v85,v85,v229\n"
U_"v86,v86,v229\n"
U_"v87,v87,v229\n"
C_"v70,v84,v85\n"
C_"v71,v86,v87\n"
O_"v[68:71],v255,s[52:55],s91 offen sc1\n"
U_"v72,v72,v230\n"
U_"v73,v73,v230\n"
U_"v74,v74,v230\n"
U_"v75,v75,v230\n"
C_"v72,v72,v73\n"
C_"v73,v74,v75\n"
U_"v88,v88,v230\n"
U_"v89,v89,v230\n"
U_"v90,v90,v230\n"
U_"v91,v91,v230\n"
C_"v74,v88,v89\n"
C_"v75,v90,v91\n"
O_"v[72:75],v255,s[52:55],s92 offen sc1\n"
U_"v76,v76,v231\n"
U_"v77,v77,v231\n"
U_"v78,v78,v231\n"
U_"v79,v79,v231\n"
C_"v76,v76,v77\n"
C_"v77,v78,v79\n"
U_"v92,v92,v231\n"
U_"v93,v93,v231\n"
U_"v94,v94,v231\n"
U_"v95,v95,v231\n"
C_"v78,v92,v93\n"
C_"v79,v94,v95\n"
O_"v[76:79],v255,s[52:55],s93 offen sc1\n"
U_"v96,v96,v228\n"
U_"v97,v97,v228\n"
U_"v98,v98,v228\n"
U_"v99,v99,v228\n"
C_"v80,v96,v97\n"
C_"v81,v98,v99\n"
U_"v112,v112,v228\n"
U_"v113,v113,v228\n"
U_"v114,v114,v228\n"
U_"v115,v115,v228\n"
C_"v82,v112,v113\n"
C_"v83,v114,v115\n"
O_"v[80:83],v255,s[52:55],0 offen offset:1024 sc1\n"
U_"v100,v100,v229\n"
U_"v101,v101,v229\n"
U_"v102,v102,v229\n"
U_"v103,v103,v229\n"
C_"v84,v100,v101\n"
C_"v85,v102,v103\n"
U_"v116,v116,v229\n"
U_"v117,v117,v229\n"
U_"v118,v118,v229\n"
U_"v119,v119,v229\n"
C_"v86,v116,v117\n"
C_"v87,v118,v119\n"
O_"v[84:87],v255,s[52:55],s91 offen offset:1024 sc1\n"
U_"v104,v104,v230\n"
U_"v105,v105,v230\n"
U_"v106,v106,v230\n"
U_"v107,v107,v230\n"
C_"v88,v104,v105\n"
C_"v89,v106,v107\n"
U_"v120,v120,v230\n"
U_"v121,v121,v230\n"
U_"v122,v122,v230\n"
U_"v123,v123,v230\n"
C_"v90,v120,v121\n"
C_"v91,v122,v123\n"
O_"v[88:91],v255,s[52:55],s92 offen offset:1024 sc1\n"
U_"v108,v108,v231\n"
U_"v109,v109,v231\n"
U_"v110,v110,v231\n"
U_"v111,v111,v231\n"
C_"v92,v108,v109\n"
C_"v93,v110,v111\n"
U_"v124,v124,v231\n"
U_"v125,v125,v231\n"
U_"v126,v126,v231\n"
U_"v127,v127,v231\n"
C_"v94,v124,v125\n"
C_"v95,v126,v127\n"
O_"v[92:95],v255,s[52:55],s93 offen offset:1024 sc1\n"
U_"v128,v128,v228\n"
U_"v129,v129,v228\n"
U_"v130,v130,v228\n"
U_"v131,v131,v228\n"
C_"v96,v128,v129\n"
C_"v97,v130,v131\n"
U_"v144,v144,v228\n"
U_"v145,v145,v228\n"
U_"v146,v146,v228\n"
U_"v147,v147,v228\n"
C_"v98,v144,v145\n"
C_"v99,v146,v147\n"
O_"v[96:99],v255,s[52:55],0 offen offset:2048 sc1\n"
U_"v132,v132,v229\n"
U_"v133,v133,v229\n"
U_"v134,v134,v229\n"
U_"v135,v135,v229\n"
C_"v100,v132,v133\n"
C_"v101,v134,v135\n"
U_"v148,v148,v229\n"
U_"v149,v149,v229\n"
U_"v150,v150,v229\n"
U_"v151,v151,v229\n"
C_"v102,v148,v149\n"
C_"v103,v150,v151\n"
O_"v[100:103],v255,s[52:55],s91 offen offset:2048 sc1\n"
U_"v136,v136,v230\n"
U_"v137,v137,v230\n"
U_"v138,v138,v230\n"
U_"v139,v139,v230\n"
C_"v104,v136,v137\n"
C_"v105,v138,v139\n"
U_"v152,v152,v230\n"
U_"v153,v153,v230\n"
U_"v154,v154,v230\n"
U_"v155,v155,v230\n"
C_"v106,v152,v153\n"
C_"v107,v154,v155\n"
O_"v[104:107],v255,s[52:55],s92 offen offset:2048 sc1\n"
U_"v140,v140,v231\n"
U_"v141,v141,v231\n"
U_"v142,v142,v231\n"
U_"v143,v143,v231\n"
C_"v108,v140,v141\n"
C_"v109,v142,v143\n"
U_"v156,v156,v231\n"
U_"v157,v157,v231\n"
U_"v158,v158,v231\n"
U_"v159,v159,v231\n"
C_"v110,v156,v157\n"
C_"v111,v158,v159\n"
O_"v[108:111],v255,s[52:55],s93 offen offset:2048 sc1\n"
U_"v160,v160,v228\n"
U_"v161,v161,v228\n"
U_"v162,v162,v228\n"
U_"v163,v163,v228\n"
C_"v112,v160,v161\n"
C_"v113,v162,v163\n"
U_"v176,v176,v228\n"
U_"v177,v177,v228\n"
U_"v178,v178,v228\n"
U_"v179,v179,v228\n"
C_"v114,v176,v177\n"
C_"v115,v178,v179\n"
O_"v[112:115],v255,s[52:55],0 offen offset:3072 sc1\n"
U_"v164,v164,v229\n"
U_"v165,v165,v229\n"
U_"v166,v166,v229\n"
U_"v167,v167,v229\n"
C_"v116,v164,v165\n"
C_"v117,v166,v167\n"
U_"v180,v180,v229\n"
U_"v181,v181,v229\n"
U_"v182,v182,v229\n"
U_"v183,v183,v229\n"
C_"v118,v180,v181\n"
C_"v119,v182,v183\n"
O_"v[116:119],v255,s[52:55],s91 offen offset:3072 sc1\n"
U_"v168,v168,v230\n"
U_"v169,v169,v230\n"
U_"v170,v170,v230\n"
U_"v171,v171,v230\n"
C_"v120,v168,v169\n"
C_"v121,v170,v171\n"
U_"v184,v184,v230\n"
U_"v185,v185,v230\n"
U_"v186,v186,v230\n"
U_"v187,v187,v230\n"
C_"v122,v184,v185\n"
C_"v123,v186,v187\n"
O_"v[120:123],v255,s[52:55],s92 offen offset:3072 sc1\n"
U_"v172,v172,v231\n"
U_"v173,v173,v231\n"
U_"v174,v174,v231\n"
U_"v175,v175,v231\n"
C_"v124,v172,v173\n"
C_"v125,v174,v175\n"
U_"v188,v188,v231\n"
U_"v189,v189,v231\n"
U_"v190,v190,v231\n"
U_"v191,v191,v231\n"
C_"v126,v188,v189\n"
C_"v127,v190,v191\n"
O_"v[124:127],v255,s[52:55],s93 offen offset:3072 sc1\n"
"s_waitcnt vmcnt(0)\n"
"s_branch REJOIN_%=\n"
"THEEND_%=:\n"
    :
    : [ka] "s"(__builtin_amdgcn_kernarg_segment_ptr()), [bid] "s"(blockIdx.x), [wid] "s"(wid)
    : "v0","v1","v2","v3","v4","v5","v6","v7","v8","v9","v10","v11","v12","v13","v14","v15","v16","v17","v18","v19","v20","v21","v22","v23","v24","v25","v26","v27","v28","v29","v30","v31","v32","v33","v34","v35","v36","v37","v38","v39","v40","v41","v42","v43","v44","v45","v46","v47","v48","v49","v50","v51","v52","v53","v54","v55","v56","v57","v58","v59","v60","v61","v62","v63","v64","v65","v66","v67","v68","v69","v70","v71","v72","v73","v74","v75","v76","v77","v78","v79","v80","v81","v82","v83","v84","v85","v86","v87","v88","v89","v90","v91","v92","v93","v94","v95","v96","v97","v98","v99","v100","v101","v102","v103","v104","v105","v106","v107","v108","v109","v110","v111","v112","v113","v114","v115","v116","v117","v118","v119","v120","v121","v122","v123","v124","v125","v126","v127","v128","v129","v130","v131","v132","v133","v134","v135","v136","v137","v138","v139","v140","v141","v142","v143","v144","v145","v146","v147","v148","v149","v150","v151","v152","v153","v154","v155","v156","v157","v158","v159","v160","v161","v162","v163","v164","v165","v166","v167","v168","v169","v170","v171","v172","v173","v174","v175","v176","v177","v178","v179","v180","v181","v182","v183","v184","v185","v186","v187","v188","v189","v190","v191","v192","v193","v194","v195","v196","v197","v198","v199","v200","v201","v202","v203","v204","v205","v206","v207","v208","v209","v210","v211","v212","v213","v214","v215","v216","v217","v218","v219","v220","v221","v222","v223","v224","v225","v226","v227","v228","v229","v230","v231","v232","v233","v234","v235","v236","v237","v238","v239","v240","v241","v242","v243","v244","v245","v246","v247","v248","v249","v250","v251","v252","v253","v254","v255","a0","a1","a2","a3","a4","a5","a6","a7","a8","a9","a10","a11","a12","a13","a14","a15","a16","a17","a18","a19","a20","a21","a22","a23","a24","a25","a26","a27","a28","a29","a30","a31","a32","a33","a34","a35","a36","a37","a38","a39","a40","a41","a42","a43","a44","a45","a46","a47","a48","a49","a50","a51","a52","a53","a54","a55","a56","a57","a58","a59","a60","a61","a62","a63","a64","a65","a66","a67","a68","a69","a70","a71","a72","a73","a74","a75","a76","a77","a78","a79","a80","a81","a82","a83","a84","a85","a86","a87","a88","a89","a90","a91","a92","a93","a94","a95","a96","a97","a98","a99","a100","a101","a102","a103","a104","a105","a106","a107","a108","a109","a110","a111","a112","a113","a114","a115","a116","a117","a118","a119","a120","a121","a122","a123","a124","a125","a126","a127","a128","a129","a130","a131","a132","a133","a134","a135","a136","a137","a138","a139","a140","a141","a142","a143","a144","a145","a146","a147","a148","a149","a150","a151","a152","a153","a154","a155","a156","a157","a158","a159","a160","a161","a162","a163","a164","a165","a166","a167","a168","a169","a170","a171","a172","a173","a174","a175","a176","a177","a178","a179","a180","a181","a182","a183","a184","a185","a186","a187","a188","a189","a190","a191","a192","a193","a194","a195","a196","a197","a198","a199","a200","a201","a202","a203","a204","a205","a206","a207","a208","a209","a210","a211","a212","a213","a214","a215","a216","a217","a218","a219","a220","a221","a222","a223","a224","a225","a226","a227","a228","a229","a230","a231","a232","a233","a234","a235","a236","a237","a238","a239","a240","a241","a242","a243","a244","a245","a246","a247","a248","a249","a250","a251","a252","a253","a254","a255","s16","s17","s18","s19","s20","s21","s22","s23","s24","s25","s26","s27","s28","s29","s30","s31","s33","s34","s35","s36","s37","s38","s39","s40","s41","s42","s43","s44","s45","s46","s47","s48","s49","s50","s51","s52","s53","s54","s55","s56","s57","s58","s59","s60","s61","s62","s63","s64","s65","s66","s67","s68","s69","s70","s71","s72","s73","s74","s75","s76","s77","s78","s79","s80","s81","s82","s83","s84","s85","s86","s87","s88","s89","s90","s91","s92","s93","s94","s95","s96","s97","s98","s99","vcc","scc","memory");
}

extern "C" void kernel_launch(void* const* d_in, const int* in_sizes, int n_in, void* d_out, int out_size, void* d_ws, size_t ws_size, hipStream_t stream) {
  Args a{d_in[0], d_in[1], d_in[2], d_in[3], d_in[4], d_in[6], d_in[7], d_in[8], d_out, d_ws};
  (void)hipFuncSetAttribute((const void*)fused_kernel, hipFuncAttributeMaxDynamicSharedMemorySize, 163840);
  fused_kernel<<<256, 256, 163840, stream>>>(a);
}
```
